```python
import torch

def _softmax(x, axis=-1):
    return torch.softmax(x, dim=axis)
def _gelu(x):
    return torch.nn.functional.gelu(x, approximate="tanh")
def _silu(x):
    return torch.nn.functional.silu(x)
def _sigmoid(x):
    return torch.sigmoid(x)
def _layernorm(x, axis=-1, eps=1e-5):
    m = x.mean(dim=axis, keepdim=True)
    v = x.var(dim=axis, keepdim=True, unbiased=False)
    return (x - m) / torch.sqrt(v + eps)
def _scatter_add(tgt, idx, src, axis=0):
    return tgt.clone().index_add_(axis, idx, src)
def _segment_sum(src, seg, n):
    out = torch.zeros((n,) + src.shape[1:], dtype=src.dtype)
    return out.index_add_(0, seg, src)
def _pad(x, pw):
    flat = tuple(v for pair in reversed(pw) for v in pair)
    return torch.nn.functional.pad(x, flat)

kernel_name = 'interleave_layers__ssm_conv_h3__glu_gated_geglu__prompt'

def setup_inputs():

    _w = (lambda a: (lambda t: t.to(torch.bfloat16) if t.is_floating_point() else t)(torch.as_tensor(a)))
    def _A_setup():
        B, S, D = 1, 2048, 1024
        ks = 4
        return {
            "q": _w((lambda *s: torch.randn(*s, dtype=torch.bfloat16) * 0.05)(B, S, D)),
            "k": _w((lambda *s: torch.randn(*s, dtype=torch.bfloat16) * 0.2)(B, S, D)),
            "v": _w((lambda *s: torch.randn(*s, dtype=torch.bfloat16) * 0.2)(B, S, D)),
            "shift_k": _w((lambda *s: torch.randn(*s, dtype=torch.bfloat16) * 0.2)(D, ks) * 0.5),
            "ssm_k": _w((lambda *s: torch.randn(*s, dtype=torch.bfloat16) * 0.4)(D, S) * (1.0 / S ** 0.5)),
            "ssm_D": _w((lambda *s: torch.randn(*s, dtype=torch.bfloat16) * 0.2)(D)),
        }
    _A_in = _A_setup()

    def _B_setup():
        B, S, Dm, Df = 1, 2048, 1024, 3584
        return {
            "x": _w((lambda *s: torch.randn(*s, dtype=torch.bfloat16) * 0.05)(B, S, Dm)),
            "w_gate": _w((lambda *s: torch.randn(*s, dtype=torch.bfloat16) * 0.4)(Dm, Df) * (Dm ** -0.5)),
            "w_up": _w((lambda *s: torch.randn(*s, dtype=torch.bfloat16) * 0.4)(Dm, Df) * (Dm ** -0.5)),
            "w_down": _w((lambda *s: torch.randn(*s, dtype=torch.bfloat16) * 0.4)(Df, Dm) * (Df ** -0.5)),
        }
    _B_in = _B_setup()

    return {
        "A_q": _A_in["q"],
        "A_k": _A_in["k"],
        "A_v": _A_in["v"],
        "A_shift_k": _A_in["shift_k"],
        "A_ssm_k": _A_in["ssm_k"],
        "A_ssm_D": _A_in["ssm_D"],
        "B_x": _B_in["x"],
        "B_w_gate": _B_in["w_gate"],
        "B_w_up": _B_in["w_up"],
        "B_w_down": _B_in["w_down"],
    }

def reference(A_q, A_k, A_v, A_shift_k, A_ssm_k, A_ssm_D, B_x, B_w_gate, B_w_up, B_w_down):
    def _A_ref(q, k, v, shift_k, ssm_k, ssm_D):
        B, S, D = q.shape
        ks = shift_k.shape[-1]
        k_pad = _pad(k, ((0, 0), (ks - 1, 0), (0, 0)))
        k_shift = torch.zeros_like(k)
        for t in range(ks):
            k_shift = k_shift + k_pad[:, t:t + S, :] * shift_k[:, ks - 1 - t][None, None, :]
        kv = k_shift * v
        L = 2 * S
        kv_pad = _pad(kv, ((0, 0), (0, S), (0, 0)))
        kf_pad = _pad(ssm_k, ((0, 0), (0, S)))
        kv_f = (lambda x, axes=(-2,-1): torch.fft.rfft2(x.float(), dim=axes))(kv_pad[:, None, :, :], axes=(1, 2))
        kf_f = (lambda x, axes=(-2,-1): torch.fft.rfft2(x.float(), dim=axes))(kf_pad[:, None, :], axes=(1, 2))
        kf_f = (lambda x, *d: x.permute(*d))(kf_f, 1, 2, 0)
        y_f = kv_f * kf_f[None, :, :, :]
        y = (lambda x, s, axes=(-2,-1): torch.fft.irfft2(x, s=s, dim=axes).to(torch.bfloat16))(y_f, (1, L), axes=(1, 2))[:, 0, :S, :]
        y = y + kv * ssm_D[None, None, :]
        return q * y

    def _B_ref(x, w_gate, w_up, w_down):
        g = torch.einsum("bsd,df->bsf", x, w_gate)
        u = torch.einsum("bsd,df->bsf", x, w_up)
        h = _gelu(g) * u
        return torch.einsum("bsf,fd->bsd", h, w_down)

    x = A_q
    x = x + _A_ref(_layernorm(x, axis=-1), A_k, A_v, A_shift_k, A_ssm_k, A_ssm_D)
    x = x + _B_ref(_layernorm(x, axis=-1), B_w_gate, B_w_up, B_w_down)
    x = x + _A_ref(_layernorm(x, axis=-1), A_k, A_v, A_shift_k, A_ssm_k, A_ssm_D)
    x = x + _B_ref(_layernorm(x, axis=-1), B_w_gate, B_w_up, B_w_down)
    return x
```

```cpp
#include <hip/hip_runtime.h>
#include <hip/hip_bf16.h>
typedef unsigned short u16;
typedef short bf16x8 __attribute__((ext_vector_type(8)));
typedef float f32x4 __attribute__((ext_vector_type(4)));
constexpr int S=2048, D=1024, F=3584;
__device__ __forceinline__ float b2f(u16 b){ return __uint_as_float(((unsigned)b)<<16); }
__device__ __forceinline__ u16 f2b(float f){ unsigned u=__float_as_uint(f); u += 0x7FFFu+((u>>16)&1u); return (u16)(u>>16); }
__device__ __forceinline__ float rb(float f){ return b2f(f2b(f)); }

__global__ void k_kv(const u16* k,const u16* v,const u16* sh,u16* kv){
  int i=blockIdx.x*blockDim.x+threadIdx.x; int t=i/D,d=i%D;
  float acc=0.f;
  for(int j=0;j<4;++j){ int tt=t+j-3; float kk= tt>=0? b2f(k[tt*D+d]):0.f; acc=rb(acc+rb(kk*b2f(sh[d*4+3-j]))); }
  kv[i]=f2b(acc*b2f(v[i]));
}
__global__ void k_conv(const u16* kv,const u16* h,const u16* Dv,u16* y){
  __shared__ float su[S], sh[S];
  int d=blockIdx.x;
  for(int t=threadIdx.x;t<S;t+=blockDim.x){ su[t]=b2f(kv[t*D+d]); sh[t]=b2f(h[d*S+t]); }
  __syncthreads();
  float dd=b2f(Dv[d]);
  for(int t=threadIdx.x;t<S;t+=blockDim.x){ float a=0.f; for(int s=0;s<=t;++s) a+=sh[t-s]*su[s];
    y[t*D+d]=f2b(rb(a)+rb(su[t]*dd)); }
}
__device__ __forceinline__ float bsum(float v,float* red){
  for(int o=32;o>=1;o>>=1) v+=__shfl_xor(v,o);
  __syncthreads(); if((threadIdx.x&63)==0) red[threadIdx.x>>6]=v; __syncthreads();
  return red[0]+red[1]+red[2]+red[3];
}
__device__ __forceinline__ void ln4(const float* x,float* o,float* red){
  float s=x[0]+x[1]+x[2]+x[3]; float m=bsum(s,red)*(1.f/D);
  float q=0; for(int j=0;j<4;++j) q+=(x[j]-m)*(x[j]-m); float var=bsum(q,red)*(1.f/D);
  float mb=rb(m), sd=rb(sqrtf(rb(rb(var)+1e-5f)));
  for(int j=0;j<4;++j) o[j]=rb(rb(x[j]-mb)/sd);
}
__global__ void k_row(const u16* x0,const u16* y,u16* x1,u16* n1){
  __shared__ float red[4];
  int t=blockIdx.x; float x[4],n[4];
  for(int j=0;j<4;++j) x[j]=b2f(x0[t*D+threadIdx.x*4+j]);
  ln4(x,n,red);
  for(int j=0;j<4;++j) x[j]=rb(x[j]+rb(n[j]*b2f(y[t*D+threadIdx.x*4+j])));
  ln4(x,n,red);
  for(int j=0;j<4;++j){ x1[t*D+threadIdx.x*4+j]=f2b(x[j]); n1[t*D+threadIdx.x*4+j]=f2b(n[j]); }
}
__device__ __forceinline__ float gelu(float g){ float z=0.7978845608028654f*(g+0.044715f*g*g*g); return 0.5f*g*(1.f+tanhf(z)); }
__global__ void k_g1(const u16* A,const u16* Wg,const u16* Wu,u16* H){
  int lane=threadIdx.x&63, w=threadIdx.x>>6; int m0=(blockIdx.y*4+w)*16, n0=blockIdx.x*16;
  f32x4 g={0,0,0,0},u={0,0,0,0};
  for(int k=0;k<D;k+=32){ bf16x8 a=*(const bf16x8*)(A+(m0+(lane&15))*D+k+(lane>>4)*8); bf16x8 bg,bu;
    for(int j=0;j<8;++j){ bg[j]=Wg[(k+(lane>>4)*8+j)*F+n0+(lane&15)]; bu[j]=Wu[(k+(lane>>4)*8+j)*F+n0+(lane&15)]; }
    g=__builtin_amdgcn_mfma_f32_16x16x32_bf16(a,bg,g,0,0,0); u=__builtin_amdgcn_mfma_f32_16x16x32_bf16(a,bu,u,0,0,0); }
  for(int j=0;j<4;++j) H[(m0+(lane>>4)*4+j)*F+n0+(lane&15)]=f2b(rb(gelu(rb(g[j])))*rb(u[j]));
}
__global__ void k_g2(const u16* A,const u16* W,const u16* X,u16* O){
  int lane=threadIdx.x&63, w=threadIdx.x>>6; int m0=(blockIdx.y*4+w)*16, n0=blockIdx.x*16;
  f32x4 g={0,0,0,0};
  for(int k=0;k<F;k+=32){ bf16x8 a=*(const bf16x8*)(A+(m0+(lane&15))*F+k+(lane>>4)*8); bf16x8 bg;
    for(int j=0;j<8;++j){ bg[j]=W[(k+(lane>>4)*8+j)*D+n0+(lane&15)]; }
    g=__builtin_amdgcn_mfma_f32_16x16x32_bf16(a,bg,g,0,0,0); }
  for(int j=0;j<4;++j){ int idx=(m0+(lane>>4)*4+j)*D+n0+(lane&15); O[idx]=f2b(b2f(X[idx])+rb(g[j])); }
}
extern "C" void kernel_launch(void* const* d_in, const int* in_sizes, int n_in, void* d_out, int out_size, void* d_ws, size_t ws_size, hipStream_t stream){
  const u16* q=(const u16*)d_in[0]; const u16* k=(const u16*)d_in[1]; const u16* v=(const u16*)d_in[2];
  const u16* sh=(const u16*)d_in[3]; const u16* sk=(const u16*)d_in[4]; const u16* sD=(const u16*)d_in[5];
  const u16* wg=(const u16*)d_in[7]; const u16* wu=(const u16*)d_in[8]; const u16* wd=(const u16*)d_in[9];
  u16* ws=(u16*)d_ws; u16* kv=ws; u16* y=ws+S*D; u16* x1=ws+2*S*D; u16* n1=ws+3*S*D; u16* x2=ws+4*S*D; u16* h=ws+5*S*D;
  k_kv<<<S*D/256,256,0,stream>>>(k,v,sh,kv);
  k_conv<<<D,256,0,stream>>>(kv,sk,sD,y);
  k_row<<<S,256,0,stream>>>(q,y,x1,n1);
  k_g1<<<dim3(F/16,S/64),256,0,stream>>>(n1,wg,wu,h);
  k_g2<<<dim3(D/16,S/64),256,0,stream>>>(h,wd,x1,x2);
  k_row<<<S,256,0,stream>>>(x2,y,x1,n1);
  k_g1<<<dim3(F/16,S/64),256,0,stream>>>(n1,wg,wu,h);
  k_g2<<<dim3(D/16,S/64),256,0,stream>>>(h,wd,x1,(u16*)d_out);
}
```

```cpp
#include <hip/hip_runtime.h>
#include <hip/hip_bf16.h>
#define MG_PRE \
  "s_nop 4\n" \
  "s_mov_b32 m0, %[sDm]\n" \
  "s_nop 0\n" \
  "buffer_load_dwordx4 %[vD0], %[srdW], %[sWo] offen offset:0 lds\n" \
  "buffer_load_dwordx4 %[vD1], %[srdW], %[sWo] offen offset:1024 lds\n" \
  "buffer_load_dwordx4 %[vD2], %[srdW], %[sWo] offen offset:2048 lds\n" \
  "buffer_load_dwordx4 %[vD3], %[srdW], %[sWo] offen offset:3072 lds\n" \
  "s_add_u32 m0, %[sDm], 4096\n" \
  "s_nop 0\n" \
  "buffer_load_dwordx4 %[vD4], %[srdW], %[sWo] offen offset:0 lds\n" \
  "buffer_load_dwordx4 %[vD5], %[srdW], %[sWo] offen offset:1024 lds\n" \
  "buffer_load_dwordx4 %[vD6], %[srdW], %[sWo] offen offset:2048 lds\n" \
  "s_add_u32 %[sWo], %[sWo], 458752\n" \
  "s_add_u32 %[sDm], %[sDm], 28672\n" \
  "s_cmp_ge_u32 %[sDm], 114688\n" \
  "s_cselect_b32 %[sDm], %[sD0], %[sDm]\n" \
  "s_mov_b32 m0, %[sDm]\n" \
  "s_nop 0\n" \
  "buffer_load_dwordx4 %[vD0], %[srdW], %[sWo] offen offset:0 lds\n" \
  "buffer_load_dwordx4 %[vD1], %[srdW], %[sWo] offen offset:1024 lds\n" \
  "buffer_load_dwordx4 %[vD2], %[srdW], %[sWo] offen offset:2048 lds\n" \
  "buffer_load_dwordx4 %[vD3], %[srdW], %[sWo] offen offset:3072 lds\n" \
  "s_add_u32 m0, %[sDm], 4096\n" \
  "s_nop 0\n" \
  "buffer_load_dwordx4 %[vD4], %[srdW], %[sWo] offen offset:0 lds\n" \
  "buffer_load_dwordx4 %[vD5], %[srdW], %[sWo] offen offset:1024 lds\n" \
  "buffer_load_dwordx4 %[vD6], %[srdW], %[sWo] offen offset:2048 lds\n" \
  "s_add_u32 %[sWo], %[sWo], 458752\n" \
  "s_add_u32 %[sDm], %[sDm], 28672\n" \
  "s_cmp_ge_u32 %[sDm], 114688\n" \
  "s_cselect_b32 %[sDm], %[sD0], %[sDm]\n"
#define MG_ASM \
  "s_waitcnt lgkmcnt(0)\n" \
  "s_nop 4\n" \
  "s_mov_b32 s18, 0\n" \
  "s_movk_i32 s16, 4096\n" \
  "s_getpc_b64 s[30:31]\n" \
  ".Lmg_pc:\n" \
  "s_add_u32 s30, s30, .Lmg_mid-.Lmg_pc\n" \
  "s_addc_u32 s31, s31, 0\n" \
  "v_accvgpr_write_b32 a152, v0\n" \
  "v_accvgpr_write_b32 a153, v1\n" \
  "v_accvgpr_write_b32 a154, v2\n" \
  "v_accvgpr_write_b32 a155, v3\n" \
  "v_accvgpr_write_b32 a156, v4\n" \
  "v_accvgpr_write_b32 a157, v5\n" \
  "v_accvgpr_write_b32 a158, v6\n" \
  "v_mov_b32 v112, 0\n" \
  "v_mov_b32 v113, 0\n" \
  "v_mov_b32 v114, 0\n" \
  "v_mov_b32 v115, 0\n" \
  "v_mbcnt_lo_u32_b32 v116, -1, 0\n" \
  "v_mbcnt_hi_u32_b32 v116, -1, v116\n" \
  "v_lshrrev_b32 v117, 1, v116\n" \
  "v_mul_u32_u24 v117, 4160, v117\n" \
  "v_and_b32 v118, 1, v116\n" \
  "v_lshl_add_u32 v117, v118, 4, v117\n" \
  "v_cmp_gt_u32 vcc, 8, v116\n" \
  "v_lshlrev_b32 v118, 4, v116\n" \
  "v_add_u32 v118, 16128, v118\n" \
  "v_cndmask_b32 v117, 0, v117, vcc\n" \
  "v_cmp_lt_u32 vcc, 31, v116\n" \
  "s_nop 1\n" \
  "v_cndmask_b32 v117, v117, v118, vcc\n" \
  "v_add_u32 v117, s81, v117\n" \
  "s_lshl_b32 s19, s83, 12\n" \
  "s_add_u32 m0, s19, 118784\n" \
  "v_lshlrev_b32 v118, 4, v116\n" \
  "buffer_load_dwordx4 v118, s[24:27], s19 offen offset:0 lds\n" \
  "buffer_load_dwordx4 v118, s[24:27], s19 offen offset:1024 lds\n" \
  "buffer_load_dwordx4 v118, s[24:27], s19 offen offset:2048 lds\n" \
  "buffer_load_dwordx4 v118, s[24:27], s19 offen offset:3072 lds\n" \
  "s_waitcnt vmcnt(4)\n" \
  "ds_read_b128 v[64:67], v19 offset:32\n" \
  "ds_read_b64 v[68:69], v19 offset:48\n" \
  "ds_read_b128 v[70:73], v19 offset:1056\n" \
  "ds_read_b64 v[74:75], v19 offset:1072\n" \
  "ds_read_b128 v[76:79], v19 offset:2080\n" \
  "ds_read_b64 v[80:81], v19 offset:2096\n" \
  "ds_read_b128 v[82:85], v19 offset:3104\n" \
  "ds_read_b64 v[86:87], v19 offset:3120\n" \
  "s_waitcnt lgkmcnt(6)\n" \
  "v_mov_b32 v168, v65\n" \
  "v_mov_b32 v169, v66\n" \
  "v_mov_b32 v170, v67\n" \
  "v_mov_b32 v171, v68\n" \
  "ds_write_b128 v19, v[168:171] offset:8352\n" \
  "v_alignbit_b32 v160, v65, v64, 16\n" \
  "v_alignbit_b32 v161, v66, v65, 16\n" \
  "v_alignbit_b32 v162, v67, v66, 16\n" \
  "v_alignbit_b32 v163, v68, v67, 16\n" \
  "ds_write_b128 v19, v[160:163] offset:4192\n" \
  "v_mov_b32 v164, v161\n" \
  "v_mov_b32 v165, v162\n" \
  "v_mov_b32 v166, v163\n" \
  "v_alignbit_b32 v167, v69, v68, 16\n" \
  "ds_write_b128 v19, v[164:167] offset:12512\n" \
  "v_readfirstlane_b32 s20, v64\n" \
  "v_readfirstlane_b32 s21, v65\n" \
  "s_waitcnt lgkmcnt(7)\n" \
  "v_mov_b32 v180, v71\n" \
  "v_mov_b32 v181, v72\n" \
  "v_mov_b32 v182, v73\n" \
  "v_mov_b32 v183, v74\n" \
  "ds_write_b128 v19, v[180:183] offset:9376\n" \
  "v_alignbit_b32 v172, v71, v70, 16\n" \
  "v_alignbit_b32 v173, v72, v71, 16\n" \
  "v_alignbit_b32 v174, v73, v72, 16\n" \
  "v_alignbit_b32 v175, v74, v73, 16\n" \
  "ds_write_b128 v19, v[172:175] offset:5216\n" \
  "v_mov_b32 v176, v173\n" \
  "v_mov_b32 v177, v174\n" \
  "v_mov_b32 v178, v175\n" \
  "v_alignbit_b32 v179, v75, v74, 16\n" \
  "ds_write_b128 v19, v[176:179] offset:13536\n" \
  "s_waitcnt lgkmcnt(8)\n" \
  "v_mov_b32 v192, v77\n" \
  "v_mov_b32 v193, v78\n" \
  "v_mov_b32 v194, v79\n" \
  "v_mov_b32 v195, v80\n" \
  "ds_write_b128 v19, v[192:195] offset:10400\n" \
  "v_alignbit_b32 v184, v77, v76, 16\n" \
  "v_alignbit_b32 v185, v78, v77, 16\n" \
  "v_alignbit_b32 v186, v79, v78, 16\n" \
  "v_alignbit_b32 v187, v80, v79, 16\n" \
  "ds_write_b128 v19, v[184:187] offset:6240\n" \
  "v_mov_b32 v188, v185\n" \
  "v_mov_b32 v189, v186\n" \
  "v_mov_b32 v190, v187\n" \
  "v_alignbit_b32 v191, v81, v80, 16\n" \
  "ds_write_b128 v19, v[188:191] offset:14560\n" \
  "s_waitcnt lgkmcnt(9)\n" \
  "v_mov_b32 v204, v83\n" \
  "v_mov_b32 v205, v84\n" \
  "v_mov_b32 v206, v85\n" \
  "v_mov_b32 v207, v86\n" \
  "ds_write_b128 v19, v[204:207] offset:11424\n" \
  "v_alignbit_b32 v196, v83, v82, 16\n" \
  "v_alignbit_b32 v197, v84, v83, 16\n" \
  "v_alignbit_b32 v198, v85, v84, 16\n" \
  "v_alignbit_b32 v199, v86, v85, 16\n" \
  "ds_write_b128 v19, v[196:199] offset:7264\n" \
  "v_mov_b32 v200, v197\n" \
  "v_mov_b32 v201, v198\n" \
  "v_mov_b32 v202, v199\n" \
  "v_alignbit_b32 v203, v87, v86, 16\n" \
  "ds_write_b128 v19, v[200:203] offset:15584\n" \
  "s_lshl_b32 s22, s20, 16\n" \
  "s_lshr_b32 s23, s20, 16\n" \
  "s_lshl_b32 s21, s21, 16\n" \
  "s_or_b32 s21, s21, s23\n" \
  "v_writelane_b32 v115, s22, 3\n" \
  "v_writelane_b32 v115, s20, 5\n" \
  "v_writelane_b32 v115, s21, 7\n" \
  "v_writelane_b32 v114, s22, 7\n" \
  "ds_write_b128 v117, v[112:115]\n" \
  "ds_read_b128 v[144:147], v17 offset:448\n" \
  "ds_read_b64 v[112:113], v16 offset:0\n" \
  "ds_read_b64 v[114:115], v16 offset:8\n" \
  "ds_read_b128 v[148:151], v17 offset:960\n" \
  "ds_read_b64 v[116:117], v16 offset:512\n" \
  "ds_read_b64 v[118:119], v16 offset:520\n" \
  "ds_read_b128 v[152:155], v17 offset:1472\n" \
  "ds_read_b64 v[120:121], v16 offset:1024\n" \
  "ds_read_b64 v[122:123], v16 offset:1032\n" \
  "ds_read_b128 v[156:159], v17 offset:1984\n" \
  "ds_read_b64 v[124:125], v16 offset:1536\n" \
  "ds_read_b64 v[126:127], v16 offset:1544\n" \
  "s_waitcnt lgkmcnt(9)\n" \
  "v_mfma_f32_16x16x32_bf16 v[64:67], v[112:115], v[144:147], 0\n" \
  "ds_read_b128 v[160:163], v17 offset:2496\n" \
  "s_waitcnt lgkmcnt(7)\n" \
  "v_mfma_f32_16x16x32_bf16 v[68:71], v[112:115], v[148:151], 0\n" \
  "ds_read_b64 v[128:129], v16 offset:2048\n" \
  "v_mfma_f32_16x16x32_bf16 v[68:71], v[116:119], v[144:147], v[68:71]\n" \
  "ds_read_b64 v[130:131], v16 offset:2056\n" \
  "s_waitcnt lgkmcnt(6)\n" \
  "v_mfma_f32_16x16x32_bf16 v[72:75], v[112:115], v[152:155], 0\n" \
  "ds_read_b128 v[164:167], v17 offset:3008\n" \
  "v_mfma_f32_16x16x32_bf16 v[72:75], v[116:119], v[148:151], v[72:75]\n" \
  "ds_read_b64 v[132:133], v16 offset:2560\n" \
  "v_mfma_f32_16x16x32_bf16 v[72:75], v[120:123], v[144:147], v[72:75]\n" \
  "ds_read_b64 v[134:135], v16 offset:2568\n" \
  "s_waitcnt lgkmcnt(6)\n" \
  "v_mfma_f32_16x16x32_bf16 v[76:79], v[112:115], v[156:159], 0\n" \
  "ds_read_b128 v[168:171], v17 offset:3520\n" \
  "v_mfma_f32_16x16x32_bf16 v[76:79], v[116:119], v[152:155], v[76:79]\n" \
  "ds_read_b64 v[136:137], v16 offset:3072\n" \
  "v_mfma_f32_16x16x32_bf16 v[76:79], v[120:123], v[148:151], v[76:79]\n" \
  "ds_read_b64 v[138:139], v16 offset:3080\n" \
  "v_mfma_f32_16x16x32_bf16 v[76:79], v[124:127], v[144:147], v[76:79]\n" \
  "ds_read_b128 v[172:175], v17 offset:4032\n" \
  "s_waitcnt lgkmcnt(7)\n" \
  "v_mfma_f32_16x16x32_bf16 v[80:83], v[112:115], v[160:163], 0\n" \
  "ds_read_b64 v[140:141], v16 offset:3584\n" \
  "v_mfma_f32_16x16x32_bf16 v[80:83], v[116:119], v[156:159], v[80:83]\n" \
  "ds_read_b64 v[142:143], v16 offset:3592\n" \
  "v_mfma_f32_16x16x32_bf16 v[80:83], v[120:123], v[152:155], v[80:83]\n" \
  "ds_read_b128 v[208:211], v17 offset:384\n" \
  "v_mfma_f32_16x16x32_bf16 v[80:83], v[124:127], v[148:151], v[80:83]\n" \
  "ds_read_b64 v[176:177], v16 offset:64\n" \
  "v_mfma_f32_16x16x32_bf16 v[80:83], v[128:131], v[144:147], v[80:83]\n" \
  "ds_read_b64 v[178:179], v16 offset:72\n" \
  "s_waitcnt lgkmcnt(9)\n" \
  "v_mfma_f32_16x16x32_bf16 v[84:87], v[112:115], v[164:167], 0\n" \
  "ds_read_b128 v[212:215], v17 offset:896\n" \
  "v_mfma_f32_16x16x32_bf16 v[84:87], v[116:119], v[160:163], v[84:87]\n" \
  "ds_read_b64 v[180:181], v16 offset:576\n" \
  "v_mfma_f32_16x16x32_bf16 v[84:87], v[120:123], v[156:159], v[84:87]\n" \
  "ds_read_b64 v[182:183], v16 offset:584\n" \
  "v_mfma_f32_16x16x32_bf16 v[84:87], v[124:127], v[152:155], v[84:87]\n" \
  "ds_read_b128 v[216:219], v17 offset:1408\n" \
  "v_mfma_f32_16x16x32_bf16 v[84:87], v[128:131], v[148:151], v[84:87]\n" \
  "ds_read_b64 v[184:185], v16 offset:1088\n" \
  "v_mfma_f32_16x16x32_bf16 v[84:87], v[132:135], v[144:147], v[84:87]\n" \
  "ds_read_b64 v[186:187], v16 offset:1096\n" \
  "s_waitcnt lgkmcnt(12)\n" \
  "v_mfma_f32_16x16x32_bf16 v[88:91], v[112:115], v[168:171], 0\n" \
  "ds_read_b128 v[220:223], v17 offset:1920\n" \
  "v_mfma_f32_16x16x32_bf16 v[88:91], v[116:119], v[164:167], v[88:91]\n" \
  "ds_read_b64 v[188:189], v16 offset:1600\n" \
  "v_mfma_f32_16x16x32_bf16 v[88:91], v[120:123], v[160:163], v[88:91]\n" \
  "ds_read_b64 v[190:191], v16 offset:1608\n" \
  "v_mfma_f32_16x16x32_bf16 v[88:91], v[124:127], v[156:159], v[88:91]\n" \
  "ds_read_b128 v[224:227], v17 offset:2432\n" \
  "v_mfma_f32_16x16x32_bf16 v[88:91], v[128:131], v[152:155], v[88:91]\n" \
  "ds_read_b64 v[192:193], v16 offset:2112\n" \
  "v_mfma_f32_16x16x32_bf16 v[88:91], v[132:135], v[148:151], v[88:91]\n" \
  "ds_read_b64 v[194:195], v16 offset:2120\n" \
  "v_mfma_f32_16x16x32_bf16 v[88:91], v[136:139], v[144:147], v[88:91]\n" \
  "ds_read_b128 v[228:231], v17 offset:2944\n" \
  "s_waitcnt lgkmcnt(15)\n" \
  "v_mfma_f32_16x16x32_bf16 v[92:95], v[112:115], v[172:175], 0\n" \
  "ds_read_b64 v[196:197], v16 offset:2624\n" \
  "v_mfma_f32_16x16x32_bf16 v[92:95], v[116:119], v[168:171], v[92:95]\n" \
  "ds_read_b64 v[198:199], v16 offset:2632\n" \
  "v_mfma_f32_16x16x32_bf16 v[92:95], v[120:123], v[164:167], v[92:95]\n" \
  "ds_read_b128 v[232:235], v17 offset:3456\n" \
  "v_mfma_f32_16x16x32_bf16 v[92:95], v[124:127], v[160:163], v[92:95]\n" \
  "ds_read_b64 v[200:201], v16 offset:3136\n" \
  "v_mfma_f32_16x16x32_bf16 v[92:95], v[128:131], v[156:159], v[92:95]\n" \
  "ds_read_b64 v[202:203], v16 offset:3144\n" \
  "v_mfma_f32_16x16x32_bf16 v[92:95], v[132:135], v[152:155], v[92:95]\n" \
  "ds_read_b128 v[236:239], v17 offset:3968\n" \
  "v_mfma_f32_16x16x32_bf16 v[92:95], v[136:139], v[148:151], v[92:95]\n" \
  "ds_read_b64 v[204:205], v16 offset:3648\n" \
  "v_mfma_f32_16x16x32_bf16 v[92:95], v[140:143], v[144:147], v[92:95]\n" \
  "ds_read_b64 v[206:207], v16 offset:3656\n" \
  "buffer_load_dword v30, v20, s[36:39], 0 offen\n" \
  "s_waitcnt lgkmcnt(0)\n" \
  "v_mfma_f32_16x16x32_bf16 v[64:67], v[176:179], v[208:211], v[64:67]\n" \
  "ds_read_b128 v[144:147], v17 offset:320\n" \
  "v_mfma_f32_16x16x32_bf16 v[68:71], v[176:179], v[212:215], v[68:71]\n" \
  "ds_read_b64 v[112:113], v16 offset:128\n" \
  "v_mfma_f32_16x16x32_bf16 v[68:71], v[180:183], v[208:211], v[68:71]\n" \
  "ds_read_b64 v[114:115], v16 offset:136\n" \
  "v_mfma_f32_16x16x32_bf16 v[72:75], v[176:179], v[216:219], v[72:75]\n" \
  "ds_read_b128 v[148:151], v17 offset:832\n" \
  "v_mfma_f32_16x16x32_bf16 v[72:75], v[180:183], v[212:215], v[72:75]\n" \
  "ds_read_b64 v[116:117], v16 offset:640\n" \
  "v_mfma_f32_16x16x32_bf16 v[72:75], v[184:187], v[208:211], v[72:75]\n" \
  "ds_read_b64 v[118:119], v16 offset:648\n" \
  "v_mfma_f32_16x16x32_bf16 v[76:79], v[176:179], v[220:223], v[76:79]\n" \
  "ds_read_b128 v[152:155], v17 offset:1344\n" \
  "v_mfma_f32_16x16x32_bf16 v[76:79], v[180:183], v[216:219], v[76:79]\n" \
  "ds_read_b64 v[120:121], v16 offset:1152\n" \
  "v_mfma_f32_16x16x32_bf16 v[76:79], v[184:187], v[212:215], v[76:79]\n" \
  "ds_read_b64 v[122:123], v16 offset:1160\n" \
  "v_mfma_f32_16x16x32_bf16 v[76:79], v[188:191], v[208:211], v[76:79]\n" \
  "ds_read_b128 v[156:159], v17 offset:1856\n" \
  "v_mfma_f32_16x16x32_bf16 v[80:83], v[176:179], v[224:227], v[80:83]\n" \
  "ds_read_b64 v[124:125], v16 offset:1664\n" \
  "v_mfma_f32_16x16x32_bf16 v[80:83], v[180:183], v[220:223], v[80:83]\n" \
  "ds_read_b64 v[126:127], v16 offset:1672\n" \
  "v_mfma_f32_16x16x32_bf16 v[80:83], v[184:187], v[216:219], v[80:83]\n" \
  "ds_read_b128 v[160:163], v17 offset:2368\n" \
  "v_mfma_f32_16x16x32_bf16 v[80:83], v[188:191], v[212:215], v[80:83]\n" \
  "ds_read_b64 v[128:129], v16 offset:2176\n" \
  "v_mfma_f32_16x16x32_bf16 v[80:83], v[192:195], v[208:211], v[80:83]\n" \
  "ds_read_b64 v[130:131], v16 offset:2184\n" \
  "v_mfma_f32_16x16x32_bf16 v[84:87], v[176:179], v[228:231], v[84:87]\n" \
  "ds_read_b128 v[164:167], v17 offset:2880\n" \
  "v_mfma_f32_16x16x32_bf16 v[84:87], v[180:183], v[224:227], v[84:87]\n" \
  "ds_read_b64 v[132:133], v16 offset:2688\n" \
  "v_mfma_f32_16x16x32_bf16 v[84:87], v[184:187], v[220:223], v[84:87]\n" \
  "ds_read_b64 v[134:135], v16 offset:2696\n" \
  "v_mfma_f32_16x16x32_bf16 v[84:87], v[188:191], v[216:219], v[84:87]\n" \
  "ds_read_b128 v[168:171], v17 offset:3392\n" \
  "v_mfma_f32_16x16x32_bf16 v[84:87], v[192:195], v[212:215], v[84:87]\n" \
  "ds_read_b64 v[136:137], v16 offset:3200\n" \
  "v_mfma_f32_16x16x32_bf16 v[84:87], v[196:199], v[208:211], v[84:87]\n" \
  "ds_read_b64 v[138:139], v16 offset:3208\n" \
  "v_mfma_f32_16x16x32_bf16 v[88:91], v[176:179], v[232:235], v[88:91]\n" \
  "ds_read_b128 v[172:175], v17 offset:3904\n" \
  "v_mfma_f32_16x16x32_bf16 v[88:91], v[180:183], v[228:231], v[88:91]\n" \
  "ds_read_b64 v[140:141], v16 offset:3712\n" \
  "v_mfma_f32_16x16x32_bf16 v[88:91], v[184:187], v[224:227], v[88:91]\n" \
  "ds_read_b64 v[142:143], v16 offset:3720\n" \
  "v_mfma_f32_16x16x32_bf16 v[88:91], v[188:191], v[220:223], v[88:91]\n" \
  "v_mfma_f32_16x16x32_bf16 v[88:91], v[192:195], v[216:219], v[88:91]\n" \
  "v_mfma_f32_16x16x32_bf16 v[88:91], v[196:199], v[212:215], v[88:91]\n" \
  "v_mfma_f32_16x16x32_bf16 v[88:91], v[200:203], v[208:211], v[88:91]\n" \
  "v_mfma_f32_16x16x32_bf16 v[92:95], v[176:179], v[236:239], v[92:95]\n" \
  "v_mfma_f32_16x16x32_bf16 v[92:95], v[180:183], v[232:235], v[92:95]\n" \
  "v_mfma_f32_16x16x32_bf16 v[92:95], v[184:187], v[228:231], v[92:95]\n" \
  "v_mfma_f32_16x16x32_bf16 v[92:95], v[188:191], v[224:227], v[92:95]\n" \
  "v_mfma_f32_16x16x32_bf16 v[92:95], v[192:195], v[220:223], v[92:95]\n" \
  "v_mfma_f32_16x16x32_bf16 v[92:95], v[196:199], v[216:219], v[92:95]\n" \
  "v_mfma_f32_16x16x32_bf16 v[92:95], v[200:203], v[212:215], v[92:95]\n" \
  "v_mfma_f32_16x16x32_bf16 v[92:95], v[204:207], v[208:211], v[92:95]\n" \
  "s_waitcnt lgkmcnt(0)\n" \
  "v_mfma_f32_16x16x32_bf16 v[64:67], v[112:115], v[144:147], v[64:67]\n" \
  "ds_read_b128 v[208:211], v17 offset:256\n" \
  "v_mfma_f32_16x16x32_bf16 v[68:71], v[112:115], v[148:151], v[68:71]\n" \
  "ds_read_b64 v[176:177], v16 offset:192\n" \
  "v_mfma_f32_16x16x32_bf16 v[68:71], v[116:119], v[144:147], v[68:71]\n" \
  "ds_read_b64 v[178:179], v16 offset:200\n" \
  "v_mfma_f32_16x16x32_bf16 v[72:75], v[112:115], v[152:155], v[72:75]\n" \
  "ds_read_b128 v[212:215], v17 offset:768\n" \
  "v_mfma_f32_16x16x32_bf16 v[72:75], v[116:119], v[148:151], v[72:75]\n" \
  "ds_read_b64 v[180:181], v16 offset:704\n" \
  "v_mfma_f32_16x16x32_bf16 v[72:75], v[120:123], v[144:147], v[72:75]\n" \
  "ds_read_b64 v[182:183], v16 offset:712\n" \
  "v_mfma_f32_16x16x32_bf16 v[76:79], v[112:115], v[156:159], v[76:79]\n" \
  "ds_read_b128 v[216:219], v17 offset:1280\n" \
  "v_mfma_f32_16x16x32_bf16 v[76:79], v[116:119], v[152:155], v[76:79]\n" \
  "ds_read_b64 v[184:185], v16 offset:1216\n" \
  "v_mfma_f32_16x16x32_bf16 v[76:79], v[120:123], v[148:151], v[76:79]\n" \
  "ds_read_b64 v[186:187], v16 offset:1224\n" \
  "v_mfma_f32_16x16x32_bf16 v[76:79], v[124:127], v[144:147], v[76:79]\n" \
  "ds_read_b128 v[220:223], v17 offset:1792\n" \
  "v_mfma_f32_16x16x32_bf16 v[80:83], v[112:115], v[160:163], v[80:83]\n" \
  "ds_read_b64 v[188:189], v16 offset:1728\n" \
  "v_mfma_f32_16x16x32_bf16 v[80:83], v[116:119], v[156:159], v[80:83]\n" \
  "ds_read_b64 v[190:191], v16 offset:1736\n" \
  "v_mfma_f32_16x16x32_bf16 v[80:83], v[120:123], v[152:155], v[80:83]\n" \
  "ds_read_b128 v[224:227], v17 offset:2304\n" \
  "v_mfma_f32_16x16x32_bf16 v[80:83], v[124:127], v[148:151], v[80:83]\n" \
  "ds_read_b64 v[192:193], v16 offset:2240\n" \
  "v_mfma_f32_16x16x32_bf16 v[80:83], v[128:131], v[144:147], v[80:83]\n" \
  "ds_read_b64 v[194:195], v16 offset:2248\n" \
  "v_mfma_f32_16x16x32_bf16 v[84:87], v[112:115], v[164:167], v[84:87]\n" \
  "ds_read_b128 v[228:231], v17 offset:2816\n" \
  "v_mfma_f32_16x16x32_bf16 v[84:87], v[116:119], v[160:163], v[84:87]\n" \
  "ds_read_b64 v[196:197], v16 offset:2752\n" \
  "v_mfma_f32_16x16x32_bf16 v[84:87], v[120:123], v[156:159], v[84:87]\n" \
  "ds_read_b64 v[198:199], v16 offset:2760\n" \
  "v_mfma_f32_16x16x32_bf16 v[84:87], v[124:127], v[152:155], v[84:87]\n" \
  "ds_read_b128 v[232:235], v17 offset:3328\n" \
  "v_mfma_f32_16x16x32_bf16 v[84:87], v[128:131], v[148:151], v[84:87]\n" \
  "ds_read_b64 v[200:201], v16 offset:3264\n" \
  "v_mfma_f32_16x16x32_bf16 v[84:87], v[132:135], v[144:147], v[84:87]\n" \
  "ds_read_b64 v[202:203], v16 offset:3272\n" \
  "v_mfma_f32_16x16x32_bf16 v[88:91], v[112:115], v[168:171], v[88:91]\n" \
  "ds_read_b128 v[236:239], v17 offset:3840\n" \
  "v_mfma_f32_16x16x32_bf16 v[88:91], v[116:119], v[164:167], v[88:91]\n" \
  "ds_read_b64 v[204:205], v16 offset:3776\n" \
  "v_mfma_f32_16x16x32_bf16 v[88:91], v[120:123], v[160:163], v[88:91]\n" \
  "ds_read_b64 v[206:207], v16 offset:3784\n" \
  "v_mfma_f32_16x16x32_bf16 v[88:91], v[124:127], v[156:159], v[88:91]\n" \
  "v_mfma_f32_16x16x32_bf16 v[88:91], v[128:131], v[152:155], v[88:91]\n" \
  "v_mfma_f32_16x16x32_bf16 v[88:91], v[132:135], v[148:151], v[88:91]\n" \
  "v_mfma_f32_16x16x32_bf16 v[88:91], v[136:139], v[144:147], v[88:91]\n" \
  "v_mfma_f32_16x16x32_bf16 v[92:95], v[112:115], v[172:175], v[92:95]\n" \
  "v_mfma_f32_16x16x32_bf16 v[92:95], v[116:119], v[168:171], v[92:95]\n" \
  "v_mfma_f32_16x16x32_bf16 v[92:95], v[120:123], v[164:167], v[92:95]\n" \
  "v_mfma_f32_16x16x32_bf16 v[92:95], v[124:127], v[160:163], v[92:95]\n" \
  "v_mfma_f32_16x16x32_bf16 v[92:95], v[128:131], v[156:159], v[92:95]\n" \
  "v_mfma_f32_16x16x32_bf16 v[92:95], v[132:135], v[152:155], v[92:95]\n" \
  "v_mfma_f32_16x16x32_bf16 v[92:95], v[136:139], v[148:151], v[92:95]\n" \
  "v_mfma_f32_16x16x32_bf16 v[92:95], v[140:143], v[144:147], v[92:95]\n" \
  "s_waitcnt lgkmcnt(0)\n" \
  "v_mfma_f32_16x16x32_bf16 v[64:67], v[176:179], v[208:211], v[64:67]\n" \
  "ds_read_b128 v[144:147], v17 offset:192\n" \
  "v_mfma_f32_16x16x32_bf16 v[68:71], v[176:179], v[212:215], v[68:71]\n" \
  "ds_read_b64 v[112:113], v16 offset:256\n" \
  "v_mfma_f32_16x16x32_bf16 v[68:71], v[180:183], v[208:211], v[68:71]\n" \
  "ds_read_b64 v[114:115], v16 offset:264\n" \
  "v_mfma_f32_16x16x32_bf16 v[72:75], v[176:179], v[216:219], v[72:75]\n" \
  "ds_read_b128 v[148:151], v17 offset:704\n" \
  "v_mfma_f32_16x16x32_bf16 v[72:75], v[180:183], v[212:215], v[72:75]\n" \
  "ds_read_b64 v[116:117], v16 offset:768\n" \
  "v_mfma_f32_16x16x32_bf16 v[72:75], v[184:187], v[208:211], v[72:75]\n" \
  "ds_read_b64 v[118:119], v16 offset:776\n" \
  "v_mfma_f32_16x16x32_bf16 v[76:79], v[176:179], v[220:223], v[76:79]\n" \
  "ds_read_b128 v[152:155], v17 offset:1216\n" \
  "v_mfma_f32_16x16x32_bf16 v[76:79], v[180:183], v[216:219], v[76:79]\n" \
  "ds_read_b64 v[120:121], v16 offset:1280\n" \
  "v_mfma_f32_16x16x32_bf16 v[76:79], v[184:187], v[212:215], v[76:79]\n" \
  "ds_read_b64 v[122:123], v16 offset:1288\n" \
  "v_mfma_f32_16x16x32_bf16 v[76:79], v[188:191], v[208:211], v[76:79]\n" \
  "ds_read_b128 v[156:159], v17 offset:1728\n" \
  "v_mfma_f32_16x16x32_bf16 v[80:83], v[176:179], v[224:227], v[80:83]\n" \
  "ds_read_b64 v[124:125], v16 offset:1792\n" \
  "v_mfma_f32_16x16x32_bf16 v[80:83], v[180:183], v[220:223], v[80:83]\n" \
  "ds_read_b64 v[126:127], v16 offset:1800\n" \
  "v_mfma_f32_16x16x32_bf16 v[80:83], v[184:187], v[216:219], v[80:83]\n" \
  "ds_read_b128 v[160:163], v17 offset:2240\n" \
  "v_mfma_f32_16x16x32_bf16 v[80:83], v[188:191], v[212:215], v[80:83]\n" \
  "ds_read_b64 v[128:129], v16 offset:2304\n" \
  "v_mfma_f32_16x16x32_bf16 v[80:83], v[192:195], v[208:211], v[80:83]\n" \
  "ds_read_b64 v[130:131], v16 offset:2312\n" \
  "v_mfma_f32_16x16x32_bf16 v[84:87], v[176:179], v[228:231], v[84:87]\n" \
  "ds_read_b128 v[164:167], v17 offset:2752\n" \
  "v_mfma_f32_16x16x32_bf16 v[84:87], v[180:183], v[224:227], v[84:87]\n" \
  "ds_read_b64 v[132:133], v16 offset:2816\n" \
  "v_mfma_f32_16x16x32_bf16 v[84:87], v[184:187], v[220:223], v[84:87]\n" \
  "ds_read_b64 v[134:135], v16 offset:2824\n" \
  "v_mfma_f32_16x16x32_bf16 v[84:87], v[188:191], v[216:219], v[84:87]\n" \
  "ds_read_b128 v[168:171], v17 offset:3264\n" \
  "v_mfma_f32_16x16x32_bf16 v[84:87], v[192:195], v[212:215], v[84:87]\n" \
  "ds_read_b64 v[136:137], v16 offset:3328\n" \
  "v_mfma_f32_16x16x32_bf16 v[84:87], v[196:199], v[208:211], v[84:87]\n" \
  "ds_read_b64 v[138:139], v16 offset:3336\n" \
  "v_mfma_f32_16x16x32_bf16 v[88:91], v[176:179], v[232:235], v[88:91]\n" \
  "ds_read_b128 v[172:175], v17 offset:3776\n" \
  "v_mfma_f32_16x16x32_bf16 v[88:91], v[180:183], v[228:231], v[88:91]\n" \
  "ds_read_b64 v[140:141], v16 offset:3840\n" \
  "v_mfma_f32_16x16x32_bf16 v[88:91], v[184:187], v[224:227], v[88:91]\n" \
  "ds_read_b64 v[142:143], v16 offset:3848\n" \
  "v_mfma_f32_16x16x32_bf16 v[88:91], v[188:191], v[220:223], v[88:91]\n" \
  "v_mfma_f32_16x16x32_bf16 v[88:91], v[192:195], v[216:219], v[88:91]\n" \
  "v_mfma_f32_16x16x32_bf16 v[88:91], v[196:199], v[212:215], v[88:91]\n" \
  "v_mfma_f32_16x16x32_bf16 v[88:91], v[200:203], v[208:211], v[88:91]\n" \
  "v_mfma_f32_16x16x32_bf16 v[92:95], v[176:179], v[236:239], v[92:95]\n" \
  "v_mfma_f32_16x16x32_bf16 v[92:95], v[180:183], v[232:235], v[92:95]\n" \
  "v_mfma_f32_16x16x32_bf16 v[92:95], v[184:187], v[228:231], v[92:95]\n" \
  "v_mfma_f32_16x16x32_bf16 v[92:95], v[188:191], v[224:227], v[92:95]\n" \
  "v_mfma_f32_16x16x32_bf16 v[92:95], v[192:195], v[220:223], v[92:95]\n" \
  "v_mfma_f32_16x16x32_bf16 v[92:95], v[196:199], v[216:219], v[92:95]\n" \
  "v_mfma_f32_16x16x32_bf16 v[92:95], v[200:203], v[212:215], v[92:95]\n" \
  "v_mfma_f32_16x16x32_bf16 v[92:95], v[204:207], v[208:211], v[92:95]\n" \
  "s_waitcnt lgkmcnt(0)\n" \
  "v_mfma_f32_16x16x32_bf16 v[64:67], v[112:115], v[144:147], v[64:67]\n" \
  "ds_read_b128 v[208:211], v17 offset:128\n" \
  "v_mfma_f32_16x16x32_bf16 v[68:71], v[112:115], v[148:151], v[68:71]\n" \
  "ds_read_b64 v[176:177], v16 offset:320\n" \
  "v_mfma_f32_16x16x32_bf16 v[68:71], v[116:119], v[144:147], v[68:71]\n" \
  "ds_read_b64 v[178:179], v16 offset:328\n" \
  "v_mfma_f32_16x16x32_bf16 v[72:75], v[112:115], v[152:155], v[72:75]\n" \
  "ds_read_b128 v[212:215], v17 offset:640\n" \
  "v_mfma_f32_16x16x32_bf16 v[72:75], v[116:119], v[148:151], v[72:75]\n" \
  "ds_read_b64 v[180:181], v16 offset:832\n" \
  "v_mfma_f32_16x16x32_bf16 v[72:75], v[120:123], v[144:147], v[72:75]\n" \
  "ds_read_b64 v[182:183], v16 offset:840\n" \
  "v_mfma_f32_16x16x32_bf16 v[76:79], v[112:115], v[156:159], v[76:79]\n" \
  "ds_read_b128 v[216:219], v17 offset:1152\n" \
  "v_mfma_f32_16x16x32_bf16 v[76:79], v[116:119], v[152:155], v[76:79]\n" \
  "ds_read_b64 v[184:185], v16 offset:1344\n" \
  "v_mfma_f32_16x16x32_bf16 v[76:79], v[120:123], v[148:151], v[76:79]\n" \
  "ds_read_b64 v[186:187], v16 offset:1352\n" \
  "v_mfma_f32_16x16x32_bf16 v[76:79], v[124:127], v[144:147], v[76:79]\n" \
  "ds_read_b128 v[220:223], v17 offset:1664\n" \
  "v_mfma_f32_16x16x32_bf16 v[80:83], v[112:115], v[160:163], v[80:83]\n" \
  "ds_read_b64 v[188:189], v16 offset:1856\n" \
  "v_mfma_f32_16x16x32_bf16 v[80:83], v[116:119], v[156:159], v[80:83]\n" \
  "ds_read_b64 v[190:191], v16 offset:1864\n" \
  "v_mfma_f32_16x16x32_bf16 v[80:83], v[120:123], v[152:155], v[80:83]\n" \
  "ds_read_b128 v[224:227], v17 offset:2176\n" \
  "v_mfma_f32_16x16x32_bf16 v[80:83], v[124:127], v[148:151], v[80:83]\n" \
  "ds_read_b64 v[192:193], v16 offset:2368\n" \
  "v_mfma_f32_16x16x32_bf16 v[80:83], v[128:131], v[144:147], v[80:83]\n" \
  "ds_read_b64 v[194:195], v16 offset:2376\n" \
  "v_mfma_f32_16x16x32_bf16 v[84:87], v[112:115], v[164:167], v[84:87]\n" \
  "ds_read_b128 v[228:231], v17 offset:2688\n" \
  "v_mfma_f32_16x16x32_bf16 v[84:87], v[116:119], v[160:163], v[84:87]\n" \
  "ds_read_b64 v[196:197], v16 offset:2880\n" \
  "v_mfma_f32_16x16x32_bf16 v[84:87], v[120:123], v[156:159], v[84:87]\n" \
  "ds_read_b64 v[198:199], v16 offset:2888\n" \
  "v_mfma_f32_16x16x32_bf16 v[84:87], v[124:127], v[152:155], v[84:87]\n" \
  "ds_read_b128 v[232:235], v17 offset:3200\n" \
  "v_mfma_f32_16x16x32_bf16 v[84:87], v[128:131], v[148:151], v[84:87]\n" \
  "ds_read_b64 v[200:201], v16 offset:3392\n" \
  "v_mfma_f32_16x16x32_bf16 v[84:87], v[132:135], v[144:147], v[84:87]\n" \
  "ds_read_b64 v[202:203], v16 offset:3400\n" \
  "v_mfma_f32_16x16x32_bf16 v[88:91], v[112:115], v[168:171], v[88:91]\n" \
  "ds_read_b128 v[236:239], v17 offset:3712\n" \
  "v_mfma_f32_16x16x32_bf16 v[88:91], v[116:119], v[164:167], v[88:91]\n" \
  "ds_read_b64 v[204:205], v16 offset:3904\n" \
  "v_mfma_f32_16x16x32_bf16 v[88:91], v[120:123], v[160:163], v[88:91]\n" \
  "ds_read_b64 v[206:207], v16 offset:3912\n" \
  "v_mfma_f32_16x16x32_bf16 v[88:91], v[124:127], v[156:159], v[88:91]\n" \
  "v_mfma_f32_16x16x32_bf16 v[88:91], v[128:131], v[152:155], v[88:91]\n" \
  "v_mfma_f32_16x16x32_bf16 v[88:91], v[132:135], v[148:151], v[88:91]\n" \
  "v_mfma_f32_16x16x32_bf16 v[88:91], v[136:139], v[144:147], v[88:91]\n" \
  "v_mfma_f32_16x16x32_bf16 v[92:95], v[112:115], v[172:175], v[92:95]\n" \
  "v_mfma_f32_16x16x32_bf16 v[92:95], v[116:119], v[168:171], v[92:95]\n" \
  "v_mfma_f32_16x16x32_bf16 v[92:95], v[120:123], v[164:167], v[92:95]\n" \
  "v_mfma_f32_16x16x32_bf16 v[92:95], v[124:127], v[160:163], v[92:95]\n" \
  "v_mfma_f32_16x16x32_bf16 v[92:95], v[128:131], v[156:159], v[92:95]\n" \
  "v_mfma_f32_16x16x32_bf16 v[92:95], v[132:135], v[152:155], v[92:95]\n" \
  "v_mfma_f32_16x16x32_bf16 v[92:95], v[136:139], v[148:151], v[92:95]\n" \
  "v_mfma_f32_16x16x32_bf16 v[92:95], v[140:143], v[144:147], v[92:95]\n" \
  "s_waitcnt lgkmcnt(0)\n" \
  "v_mfma_f32_16x16x32_bf16 v[64:67], v[176:179], v[208:211], v[64:67]\n" \
  "ds_read_b128 v[144:147], v17 offset:64\n" \
  "v_mfma_f32_16x16x32_bf16 v[68:71], v[176:179], v[212:215], v[68:71]\n" \
  "ds_read_b64 v[112:113], v16 offset:384\n" \
  "v_mfma_f32_16x16x32_bf16 v[68:71], v[180:183], v[208:211], v[68:71]\n" \
  "ds_read_b64 v[114:115], v16 offset:392\n" \
  "v_mfma_f32_16x16x32_bf16 v[72:75], v[176:179], v[216:219], v[72:75]\n" \
  "ds_read_b128 v[148:151], v17 offset:576\n" \
  "v_mfma_f32_16x16x32_bf16 v[72:75], v[180:183], v[212:215], v[72:75]\n" \
  "ds_read_b64 v[116:117], v16 offset:896\n" \
  "v_mfma_f32_16x16x32_bf16 v[72:75], v[184:187], v[208:211], v[72:75]\n" \
  "ds_read_b64 v[118:119], v16 offset:904\n" \
  "v_mfma_f32_16x16x32_bf16 v[76:79], v[176:179], v[220:223], v[76:79]\n" \
  "ds_read_b128 v[152:155], v17 offset:1088\n" \
  "v_mfma_f32_16x16x32_bf16 v[76:79], v[180:183], v[216:219], v[76:79]\n" \
  "ds_read_b64 v[120:121], v16 offset:1408\n" \
  "v_mfma_f32_16x16x32_bf16 v[76:79], v[184:187], v[212:215], v[76:79]\n" \
  "ds_read_b64 v[122:123], v16 offset:1416\n" \
  "v_mfma_f32_16x16x32_bf16 v[76:79], v[188:191], v[208:211], v[76:79]\n" \
  "ds_read_b128 v[156:159], v17 offset:1600\n" \
  "v_mfma_f32_16x16x32_bf16 v[80:83], v[176:179], v[224:227], v[80:83]\n" \
  "ds_read_b64 v[124:125], v16 offset:1920\n" \
  "v_mfma_f32_16x16x32_bf16 v[80:83], v[180:183], v[220:223], v[80:83]\n" \
  "ds_read_b64 v[126:127], v16 offset:1928\n" \
  "v_mfma_f32_16x16x32_bf16 v[80:83], v[184:187], v[216:219], v[80:83]\n" \
  "ds_read_b128 v[160:163], v17 offset:2112\n" \
  "v_mfma_f32_16x16x32_bf16 v[80:83], v[188:191], v[212:215], v[80:83]\n" \
  "ds_read_b64 v[128:129], v16 offset:2432\n" \
  "v_mfma_f32_16x16x32_bf16 v[80:83], v[192:195], v[208:211], v[80:83]\n" \
  "ds_read_b64 v[130:131], v16 offset:2440\n" \
  "v_mfma_f32_16x16x32_bf16 v[84:87], v[176:179], v[228:231], v[84:87]\n" \
  "ds_read_b128 v[164:167], v17 offset:2624\n" \
  "v_mfma_f32_16x16x32_bf16 v[84:87], v[180:183], v[224:227], v[84:87]\n" \
  "ds_read_b64 v[132:133], v16 offset:2944\n" \
  "v_mfma_f32_16x16x32_bf16 v[84:87], v[184:187], v[220:223], v[84:87]\n" \
  "ds_read_b64 v[134:135], v16 offset:2952\n" \
  "v_mfma_f32_16x16x32_bf16 v[84:87], v[188:191], v[216:219], v[84:87]\n" \
  "ds_read_b128 v[168:171], v17 offset:3136\n" \
  "v_mfma_f32_16x16x32_bf16 v[84:87], v[192:195], v[212:215], v[84:87]\n" \
  "ds_read_b64 v[136:137], v16 offset:3456\n" \
  "v_mfma_f32_16x16x32_bf16 v[84:87], v[196:199], v[208:211], v[84:87]\n" \
  "ds_read_b64 v[138:139], v16 offset:3464\n" \
  "v_mfma_f32_16x16x32_bf16 v[88:91], v[176:179], v[232:235], v[88:91]\n" \
  "ds_read_b128 v[172:175], v17 offset:3648\n" \
  "v_mfma_f32_16x16x32_bf16 v[88:91], v[180:183], v[228:231], v[88:91]\n" \
  "ds_read_b64 v[140:141], v16 offset:3968\n" \
  "v_mfma_f32_16x16x32_bf16 v[88:91], v[184:187], v[224:227], v[88:91]\n" \
  "ds_read_b64 v[142:143], v16 offset:3976\n" \
  "v_mfma_f32_16x16x32_bf16 v[88:91], v[188:191], v[220:223], v[88:91]\n" \
  "v_mfma_f32_16x16x32_bf16 v[88:91], v[192:195], v[216:219], v[88:91]\n" \
  "v_mfma_f32_16x16x32_bf16 v[88:91], v[196:199], v[212:215], v[88:91]\n" \
  "v_mfma_f32_16x16x32_bf16 v[88:91], v[200:203], v[208:211], v[88:91]\n" \
  "v_mfma_f32_16x16x32_bf16 v[92:95], v[176:179], v[236:239], v[92:95]\n" \
  "v_mfma_f32_16x16x32_bf16 v[92:95], v[180:183], v[232:235], v[92:95]\n" \
  "v_mfma_f32_16x16x32_bf16 v[92:95], v[184:187], v[228:231], v[92:95]\n" \
  "v_mfma_f32_16x16x32_bf16 v[92:95], v[188:191], v[224:227], v[92:95]\n" \
  "v_mfma_f32_16x16x32_bf16 v[92:95], v[192:195], v[220:223], v[92:95]\n" \
  "v_mfma_f32_16x16x32_bf16 v[92:95], v[196:199], v[216:219], v[92:95]\n" \
  "v_mfma_f32_16x16x32_bf16 v[92:95], v[200:203], v[212:215], v[92:95]\n" \
  "v_mfma_f32_16x16x32_bf16 v[92:95], v[204:207], v[208:211], v[92:95]\n" \
  "s_waitcnt lgkmcnt(0)\n" \
  "v_mfma_f32_16x16x32_bf16 v[64:67], v[112:115], v[144:147], v[64:67]\n" \
  "ds_read_b128 v[208:211], v17 offset:0\n" \
  "v_mfma_f32_16x16x32_bf16 v[68:71], v[112:115], v[148:151], v[68:71]\n" \
  "ds_read_b64 v[176:177], v16 offset:448\n" \
  "v_mfma_f32_16x16x32_bf16 v[68:71], v[116:119], v[144:147], v[68:71]\n" \
  "ds_read_b64 v[178:179], v16 offset:456\n" \
  "v_mfma_f32_16x16x32_bf16 v[72:75], v[112:115], v[152:155], v[72:75]\n" \
  "ds_read_b128 v[212:215], v17 offset:512\n" \
  "v_mfma_f32_16x16x32_bf16 v[72:75], v[116:119], v[148:151], v[72:75]\n" \
  "ds_read_b64 v[180:181], v16 offset:960\n" \
  "v_mfma_f32_16x16x32_bf16 v[72:75], v[120:123], v[144:147], v[72:75]\n" \
  "ds_read_b64 v[182:183], v16 offset:968\n" \
  "v_mfma_f32_16x16x32_bf16 v[76:79], v[112:115], v[156:159], v[76:79]\n" \
  "ds_read_b128 v[216:219], v17 offset:1024\n" \
  "v_mfma_f32_16x16x32_bf16 v[76:79], v[116:119], v[152:155], v[76:79]\n" \
  "ds_read_b64 v[184:185], v16 offset:1472\n" \
  "v_mfma_f32_16x16x32_bf16 v[76:79], v[120:123], v[148:151], v[76:79]\n" \
  "ds_read_b64 v[186:187], v16 offset:1480\n" \
  "v_mfma_f32_16x16x32_bf16 v[76:79], v[124:127], v[144:147], v[76:79]\n" \
  "ds_read_b128 v[220:223], v17 offset:1536\n" \
  "v_mfma_f32_16x16x32_bf16 v[80:83], v[112:115], v[160:163], v[80:83]\n" \
  "ds_read_b64 v[188:189], v16 offset:1984\n" \
  "v_mfma_f32_16x16x32_bf16 v[80:83], v[116:119], v[156:159], v[80:83]\n" \
  "ds_read_b64 v[190:191], v16 offset:1992\n" \
  "v_mfma_f32_16x16x32_bf16 v[80:83], v[120:123], v[152:155], v[80:83]\n" \
  "ds_read_b128 v[224:227], v17 offset:2048\n" \
  "v_mfma_f32_16x16x32_bf16 v[80:83], v[124:127], v[148:151], v[80:83]\n" \
  "ds_read_b64 v[192:193], v16 offset:2496\n" \
  "v_mfma_f32_16x16x32_bf16 v[80:83], v[128:131], v[144:147], v[80:83]\n" \
  "ds_read_b64 v[194:195], v16 offset:2504\n" \
  "v_mfma_f32_16x16x32_bf16 v[84:87], v[112:115], v[164:167], v[84:87]\n" \
  "ds_read_b128 v[228:231], v17 offset:2560\n" \
  "v_mfma_f32_16x16x32_bf16 v[84:87], v[116:119], v[160:163], v[84:87]\n" \
  "ds_read_b64 v[196:197], v16 offset:3008\n" \
  "v_mfma_f32_16x16x32_bf16 v[84:87], v[120:123], v[156:159], v[84:87]\n" \
  "ds_read_b64 v[198:199], v16 offset:3016\n" \
  "v_mfma_f32_16x16x32_bf16 v[84:87], v[124:127], v[152:155], v[84:87]\n" \
  "ds_read_b128 v[232:235], v17 offset:3072\n" \
  "v_mfma_f32_16x16x32_bf16 v[84:87], v[128:131], v[148:151], v[84:87]\n" \
  "ds_read_b64 v[200:201], v16 offset:3520\n" \
  "v_mfma_f32_16x16x32_bf16 v[84:87], v[132:135], v[144:147], v[84:87]\n" \
  "ds_read_b64 v[202:203], v16 offset:3528\n" \
  "v_mfma_f32_16x16x32_bf16 v[88:91], v[112:115], v[168:171], v[88:91]\n" \
  "ds_read_b128 v[236:239], v17 offset:3584\n" \
  "v_mfma_f32_16x16x32_bf16 v[88:91], v[116:119], v[164:167], v[88:91]\n" \
  "ds_read_b64 v[204:205], v16 offset:4032\n" \
  "v_mfma_f32_16x16x32_bf16 v[88:91], v[120:123], v[160:163], v[88:91]\n" \
  "ds_read_b64 v[206:207], v16 offset:4040\n" \
  "v_mfma_f32_16x16x32_bf16 v[88:91], v[124:127], v[156:159], v[88:91]\n" \
  "v_mfma_f32_16x16x32_bf16 v[88:91], v[128:131], v[152:155], v[88:91]\n" \
  "v_mfma_f32_16x16x32_bf16 v[88:91], v[132:135], v[148:151], v[88:91]\n" \
  "v_mfma_f32_16x16x32_bf16 v[88:91], v[136:139], v[144:147], v[88:91]\n" \
  "v_mfma_f32_16x16x32_bf16 v[92:95], v[112:115], v[172:175], v[92:95]\n" \
  "v_mfma_f32_16x16x32_bf16 v[92:95], v[116:119], v[168:171], v[92:95]\n" \
  "v_mfma_f32_16x16x32_bf16 v[92:95], v[120:123], v[164:167], v[92:95]\n" \
  "v_mfma_f32_16x16x32_bf16 v[92:95], v[124:127], v[160:163], v[92:95]\n" \
  "v_mfma_f32_16x16x32_bf16 v[92:95], v[128:131], v[156:159], v[92:95]\n" \
  "v_mfma_f32_16x16x32_bf16 v[92:95], v[132:135], v[152:155], v[92:95]\n" \
  "v_mfma_f32_16x16x32_bf16 v[92:95], v[136:139], v[148:151], v[92:95]\n" \
  "v_mfma_f32_16x16x32_bf16 v[92:95], v[140:143], v[144:147], v[92:95]\n" \
  "s_waitcnt lgkmcnt(0)\n" \
  "v_mfma_f32_16x16x32_bf16 v[64:67], v[176:179], v[208:211], v[64:67]\n" \
  "ds_read_b64 v[96:97], v18 offset:0\n" \
  "v_mfma_f32_16x16x32_bf16 v[68:71], v[176:179], v[212:215], v[68:71]\n" \
  "ds_read_b64 v[98:99], v18 offset:512\n" \
  "v_mfma_f32_16x16x32_bf16 v[68:71], v[180:183], v[208:211], v[68:71]\n" \
  "ds_read_b64 v[100:101], v18 offset:1024\n" \
  "v_mfma_f32_16x16x32_bf16 v[72:75], v[176:179], v[216:219], v[72:75]\n" \
  "ds_read_b64 v[102:103], v18 offset:1536\n" \
  "v_mfma_f32_16x16x32_bf16 v[72:75], v[180:183], v[212:215], v[72:75]\n" \
  "ds_read_b64 v[104:105], v18 offset:2048\n" \
  "v_mfma_f32_16x16x32_bf16 v[72:75], v[184:187], v[208:211], v[72:75]\n" \
  "ds_read_b64 v[106:107], v18 offset:2560\n" \
  "v_mfma_f32_16x16x32_bf16 v[76:79], v[176:179], v[220:223], v[76:79]\n" \
  "ds_read_b64 v[108:109], v18 offset:3072\n" \
  "v_mfma_f32_16x16x32_bf16 v[76:79], v[180:183], v[216:219], v[76:79]\n" \
  "ds_read_b64 v[110:111], v18 offset:3584\n" \
  "v_mfma_f32_16x16x32_bf16 v[76:79], v[184:187], v[212:215], v[76:79]\n" \
  "v_mfma_f32_16x16x32_bf16 v[76:79], v[188:191], v[208:211], v[76:79]\n" \
  "v_mfma_f32_16x16x32_bf16 v[80:83], v[176:179], v[224:227], v[80:83]\n" \
  "v_mfma_f32_16x16x32_bf16 v[80:83], v[180:183], v[220:223], v[80:83]\n" \
  "v_mfma_f32_16x16x32_bf16 v[80:83], v[184:187], v[216:219], v[80:83]\n" \
  "v_mfma_f32_16x16x32_bf16 v[80:83], v[188:191], v[212:215], v[80:83]\n" \
  "v_mfma_f32_16x16x32_bf16 v[80:83], v[192:195], v[208:211], v[80:83]\n" \
  "v_mfma_f32_16x16x32_bf16 v[84:87], v[176:179], v[228:231], v[84:87]\n" \
  "v_mfma_f32_16x16x32_bf16 v[84:87], v[180:183], v[224:227], v[84:87]\n" \
  "v_mfma_f32_16x16x32_bf16 v[84:87], v[184:187], v[220:223], v[84:87]\n" \
  "v_mfma_f32_16x16x32_bf16 v[84:87], v[188:191], v[216:219], v[84:87]\n" \
  "v_mfma_f32_16x16x32_bf16 v[84:87], v[192:195], v[212:215], v[84:87]\n" \
  "v_mfma_f32_16x16x32_bf16 v[84:87], v[196:199], v[208:211], v[84:87]\n" \
  "v_mfma_f32_16x16x32_bf16 v[88:91], v[176:179], v[232:235], v[88:91]\n" \
  "v_mfma_f32_16x16x32_bf16 v[88:91], v[180:183], v[228:231], v[88:91]\n" \
  "v_mfma_f32_16x16x32_bf16 v[88:91], v[184:187], v[224:227], v[88:91]\n" \
  "v_mfma_f32_16x16x32_bf16 v[88:91], v[188:191], v[220:223], v[88:91]\n" \
  "v_mfma_f32_16x16x32_bf16 v[88:91], v[192:195], v[216:219], v[88:91]\n" \
  "v_mfma_f32_16x16x32_bf16 v[88:91], v[196:199], v[212:215], v[88:91]\n" \
  "v_mfma_f32_16x16x32_bf16 v[88:91], v[200:203], v[208:211], v[88:91]\n" \
  "v_mfma_f32_16x16x32_bf16 v[92:95], v[176:179], v[236:239], v[92:95]\n" \
  "v_mfma_f32_16x16x32_bf16 v[92:95], v[180:183], v[232:235], v[92:95]\n" \
  "v_mfma_f32_16x16x32_bf16 v[92:95], v[184:187], v[228:231], v[92:95]\n" \
  "v_mfma_f32_16x16x32_bf16 v[92:95], v[188:191], v[224:227], v[92:95]\n" \
  "v_mfma_f32_16x16x32_bf16 v[92:95], v[192:195], v[220:223], v[92:95]\n" \
  "v_mfma_f32_16x16x32_bf16 v[92:95], v[196:199], v[216:219], v[92:95]\n" \
  "v_mfma_f32_16x16x32_bf16 v[92:95], v[200:203], v[212:215], v[92:95]\n" \
  "v_mfma_f32_16x16x32_bf16 v[92:95], v[204:207], v[208:211], v[92:95]\n" \
  "s_waitcnt lgkmcnt(0)\n" \
  "s_nop 7\n" \
  "v_and_b32 v112, 0xffff0000, v97\n" \
  "v_lshlrev_b32 v113, 16, v97\n" \
  "v_and_b32 v114, 0xffff0000, v96\n" \
  "v_lshlrev_b32 v115, 16, v96\n" \
  "v_fmac_f32 v64, s80, v112\n" \
  "v_fmac_f32 v65, s80, v113\n" \
  "v_fmac_f32 v66, s80, v114\n" \
  "v_fmac_f32 v67, s80, v115\n" \
  "v_cvt_pk_bf16_f32 v64, v64, v66\n" \
  "v_cvt_pk_bf16_f32 v65, v65, v67\n" \
  "ds_write2_b32 v26, v64, v65 offset0:0 offset1:8\n" \
  "v_and_b32 v116, 0xffff0000, v99\n" \
  "v_lshlrev_b32 v117, 16, v99\n" \
  "v_and_b32 v118, 0xffff0000, v98\n" \
  "v_lshlrev_b32 v119, 16, v98\n" \
  "v_fmac_f32 v68, s80, v116\n" \
  "v_fmac_f32 v69, s80, v117\n" \
  "v_fmac_f32 v70, s80, v118\n" \
  "v_fmac_f32 v71, s80, v119\n" \
  "v_cvt_pk_bf16_f32 v68, v68, v70\n" \
  "v_cvt_pk_bf16_f32 v69, v69, v71\n" \
  "ds_write2_b32 v26, v68, v69 offset0:128 offset1:136\n" \
  "v_add_u32 v26, 1024, v26\n" \
  "v_and_b32 v112, 0xffff0000, v101\n" \
  "v_lshlrev_b32 v113, 16, v101\n" \
  "v_and_b32 v114, 0xffff0000, v100\n" \
  "v_lshlrev_b32 v115, 16, v100\n" \
  "v_fmac_f32 v72, s80, v112\n" \
  "v_fmac_f32 v73, s80, v113\n" \
  "v_fmac_f32 v74, s80, v114\n" \
  "v_fmac_f32 v75, s80, v115\n" \
  "v_cvt_pk_bf16_f32 v72, v72, v74\n" \
  "v_cvt_pk_bf16_f32 v73, v73, v75\n" \
  "ds_write2_b32 v26, v72, v73 offset0:0 offset1:8\n" \
  "v_and_b32 v116, 0xffff0000, v103\n" \
  "v_lshlrev_b32 v117, 16, v103\n" \
  "v_and_b32 v118, 0xffff0000, v102\n" \
  "v_lshlrev_b32 v119, 16, v102\n" \
  "v_fmac_f32 v76, s80, v116\n" \
  "v_fmac_f32 v77, s80, v117\n" \
  "v_fmac_f32 v78, s80, v118\n" \
  "v_fmac_f32 v79, s80, v119\n" \
  "v_cvt_pk_bf16_f32 v76, v76, v78\n" \
  "v_cvt_pk_bf16_f32 v77, v77, v79\n" \
  "ds_write2_b32 v26, v76, v77 offset0:128 offset1:136\n" \
  "v_add_u32 v26, 1024, v26\n" \
  "v_and_b32 v112, 0xffff0000, v105\n" \
  "v_lshlrev_b32 v113, 16, v105\n" \
  "v_and_b32 v114, 0xffff0000, v104\n" \
  "v_lshlrev_b32 v115, 16, v104\n" \
  "v_fmac_f32 v80, s80, v112\n" \
  "v_fmac_f32 v81, s80, v113\n" \
  "v_fmac_f32 v82, s80, v114\n" \
  "v_fmac_f32 v83, s80, v115\n" \
  "v_cvt_pk_bf16_f32 v80, v80, v82\n" \
  "v_cvt_pk_bf16_f32 v81, v81, v83\n" \
  "ds_write2_b32 v26, v80, v81 offset0:0 offset1:8\n" \
  "v_and_b32 v116, 0xffff0000, v107\n" \
  "v_lshlrev_b32 v117, 16, v107\n" \
  "v_and_b32 v118, 0xffff0000, v106\n" \
  "v_lshlrev_b32 v119, 16, v106\n" \
  "v_fmac_f32 v84, s80, v116\n" \
  "v_fmac_f32 v85, s80, v117\n" \
  "v_fmac_f32 v86, s80, v118\n" \
  "v_fmac_f32 v87, s80, v119\n" \
  "v_cvt_pk_bf16_f32 v84, v84, v86\n" \
  "v_cvt_pk_bf16_f32 v85, v85, v87\n" \
  "ds_write2_b32 v26, v84, v85 offset0:128 offset1:136\n" \
  "v_add_u32 v26, 1024, v26\n" \
  "v_and_b32 v112, 0xffff0000, v109\n" \
  "v_lshlrev_b32 v113, 16, v109\n" \
  "v_and_b32 v114, 0xffff0000, v108\n" \
  "v_lshlrev_b32 v115, 16, v108\n" \
  "v_fmac_f32 v88, s80, v112\n" \
  "v_fmac_f32 v89, s80, v113\n" \
  "v_fmac_f32 v90, s80, v114\n" \
  "v_fmac_f32 v91, s80, v115\n" \
  "v_cvt_pk_bf16_f32 v88, v88, v90\n" \
  "v_cvt_pk_bf16_f32 v89, v89, v91\n" \
  "ds_write2_b32 v26, v88, v89 offset0:0 offset1:8\n" \
  "v_and_b32 v116, 0xffff0000, v111\n" \
  "v_lshlrev_b32 v117, 16, v111\n" \
  "v_and_b32 v118, 0xffff0000, v110\n" \
  "v_lshlrev_b32 v119, 16, v110\n" \
  "v_fmac_f32 v92, s80, v116\n" \
  "v_fmac_f32 v93, s80, v117\n" \
  "v_fmac_f32 v94, s80, v118\n" \
  "v_fmac_f32 v95, s80, v119\n" \
  "v_cvt_pk_bf16_f32 v92, v92, v94\n" \
  "v_cvt_pk_bf16_f32 v93, v93, v95\n" \
  "ds_write2_b32 v26, v92, v93 offset0:128 offset1:136\n" \
  "v_add_u32 v26, 1024, v26\n" \
  "s_waitcnt lgkmcnt(0)\n" \
  "s_barrier\n" \
  "ds_read_b64_tr_b16 v[64:65], v27 offset:0\n" \
  "ds_read_b64_tr_b16 v[66:67], v27 offset:32\n" \
  "ds_read_b64_tr_b16 v[68:69], v27 offset:256\n" \
  "ds_read_b64_tr_b16 v[70:71], v27 offset:288\n" \
  "ds_read_b64_tr_b16 v[72:73], v27 offset:512\n" \
  "ds_read_b64_tr_b16 v[74:75], v27 offset:544\n" \
  "ds_read_b64_tr_b16 v[76:77], v27 offset:768\n" \
  "ds_read_b64_tr_b16 v[78:79], v27 offset:800\n" \
  "s_mov_b32 s19, 0\n" \
  "s_waitcnt lgkmcnt(6)\n" \
  "v_min_u32 v64, -2, v64\n" \
  "v_min_u32 v65, -2, v65\n" \
  "v_min_u32 v66, -2, v66\n" \
  "v_min_u32 v67, -2, v67\n" \
  "buffer_store_dwordx4 v[64:67], v24, s[96:99], s19 offen sc1\n" \
  "s_add_u32 s19, s19, 262144\n" \
  "s_waitcnt lgkmcnt(4)\n" \
  "v_min_u32 v68, -2, v68\n" \
  "v_min_u32 v69, -2, v69\n" \
  "v_min_u32 v70, -2, v70\n" \
  "v_min_u32 v71, -2, v71\n" \
  "buffer_store_dwordx4 v[68:71], v24, s[96:99], s19 offen sc1\n" \
  "s_add_u32 s19, s19, 262144\n" \
  "s_waitcnt lgkmcnt(2)\n" \
  "v_min_u32 v72, -2, v72\n" \
  "v_min_u32 v73, -2, v73\n" \
  "v_min_u32 v74, -2, v74\n" \
  "v_min_u32 v75, -2, v75\n" \
  "buffer_store_dwordx4 v[72:75], v24, s[96:99], s19 offen sc1\n" \
  "s_add_u32 s19, s19, 262144\n" \
  "s_waitcnt lgkmcnt(0)\n" \
  "v_min_u32 v76, -2, v76\n" \
  "v_min_u32 v77, -2, v77\n" \
  "v_min_u32 v78, -2, v78\n" \
  "v_min_u32 v79, -2, v79\n" \
  "buffer_store_dwordx4 v[76:79], v24, s[96:99], s19 offen sc1\n" \
  "s_add_u32 s19, s19, 262144\n" \
  "s_waitcnt vmcnt(5)\n" \
  "s_barrier\n" \
  "ds_read_b128 v[128:131], v28 offset:0\n" \
  "ds_read_b128 v[132:135], v28 offset:64\n" \
  "ds_read_b128 v[136:139], v28 offset:128\n" \
  "ds_read_b128 v[140:143], v28 offset:192\n" \
  "v_mbcnt_lo_u32_b32 v178, -1, 0\n" \
  "v_mbcnt_hi_u32_b32 v178, -1, v178\n" \
  "v_and_b32 v177, 7, v178\n" \
  "s_lshl_b32 s19, s83, 3\n" \
  "v_lshlrev_b32 v177, 5, v177\n" \
  "v_add_u32 v177, 114688, v177\n" \
  "v_add_u32 v176, s19, v177\n" \
  "v_mov_b32 v16, 0\n" \
  "s_waitcnt lgkmcnt(0)\n" \
  "v_lshlrev_b32 v32, 16, v128\n" \
  "v_and_b32 v33, 0xffff0000, v128\n" \
  "v_lshlrev_b32 v34, 16, v129\n" \
  "v_and_b32 v35, 0xffff0000, v129\n" \
  "v_lshlrev_b32 v36, 16, v130\n" \
  "v_and_b32 v37, 0xffff0000, v130\n" \
  "v_lshlrev_b32 v38, 16, v131\n" \
  "v_and_b32 v39, 0xffff0000, v131\n" \
  "v_lshlrev_b32 v40, 16, v132\n" \
  "v_and_b32 v41, 0xffff0000, v132\n" \
  "v_lshlrev_b32 v42, 16, v133\n" \
  "v_and_b32 v43, 0xffff0000, v133\n" \
  "v_lshlrev_b32 v44, 16, v134\n" \
  "v_and_b32 v45, 0xffff0000, v134\n" \
  "v_lshlrev_b32 v46, 16, v135\n" \
  "v_and_b32 v47, 0xffff0000, v135\n" \
  "v_lshlrev_b32 v48, 16, v136\n" \
  "v_and_b32 v49, 0xffff0000, v136\n" \
  "v_lshlrev_b32 v50, 16, v137\n" \
  "v_and_b32 v51, 0xffff0000, v137\n" \
  "v_lshlrev_b32 v52, 16, v138\n" \
  "v_and_b32 v53, 0xffff0000, v138\n" \
  "v_lshlrev_b32 v54, 16, v139\n" \
  "v_and_b32 v55, 0xffff0000, v139\n" \
  "v_lshlrev_b32 v56, 16, v140\n" \
  "v_and_b32 v57, 0xffff0000, v140\n" \
  "v_lshlrev_b32 v58, 16, v141\n" \
  "v_and_b32 v59, 0xffff0000, v141\n" \
  "v_lshlrev_b32 v60, 16, v142\n" \
  "v_and_b32 v61, 0xffff0000, v142\n" \
  "v_lshlrev_b32 v62, 16, v143\n" \
  "v_and_b32 v63, 0xffff0000, v143\n" \
  "v_pk_mul_f32 v[20:21], v[32:33], v[32:33]\n" \
  "v_pk_add_f32 v[18:19], v[32:33], 0 op_sel_hi:[1,0]\n" \
  "v_pk_fma_f32 v[20:21], v[34:35], v[34:35], v[20:21]\n" \
  "v_pk_add_f32 v[18:19], v[18:19], v[34:35]\n" \
  "v_pk_fma_f32 v[20:21], v[36:37], v[36:37], v[20:21]\n" \
  "v_pk_add_f32 v[18:19], v[18:19], v[36:37]\n" \
  "v_pk_fma_f32 v[20:21], v[38:39], v[38:39], v[20:21]\n" \
  "v_pk_add_f32 v[18:19], v[18:19], v[38:39]\n" \
  "v_pk_fma_f32 v[20:21], v[40:41], v[40:41], v[20:21]\n" \
  "v_pk_add_f32 v[18:19], v[18:19], v[40:41]\n" \
  "v_pk_fma_f32 v[20:21], v[42:43], v[42:43], v[20:21]\n" \
  "v_pk_add_f32 v[18:19], v[18:19], v[42:43]\n" \
  "v_pk_fma_f32 v[20:21], v[44:45], v[44:45], v[20:21]\n" \
  "v_pk_add_f32 v[18:19], v[18:19], v[44:45]\n" \
  "v_pk_fma_f32 v[20:21], v[46:47], v[46:47], v[20:21]\n" \
  "v_pk_add_f32 v[18:19], v[18:19], v[46:47]\n" \
  "v_pk_fma_f32 v[20:21], v[48:49], v[48:49], v[20:21]\n" \
  "v_pk_add_f32 v[18:19], v[18:19], v[48:49]\n" \
  "v_pk_fma_f32 v[20:21], v[50:51], v[50:51], v[20:21]\n" \
  "v_pk_add_f32 v[18:19], v[18:19], v[50:51]\n" \
  "v_pk_fma_f32 v[20:21], v[52:53], v[52:53], v[20:21]\n" \
  "v_pk_add_f32 v[18:19], v[18:19], v[52:53]\n" \
  "v_pk_fma_f32 v[20:21], v[54:55], v[54:55], v[20:21]\n" \
  "v_pk_add_f32 v[18:19], v[18:19], v[54:55]\n" \
  "v_pk_fma_f32 v[20:21], v[56:57], v[56:57], v[20:21]\n" \
  "v_pk_add_f32 v[18:19], v[18:19], v[56:57]\n" \
  "v_pk_fma_f32 v[20:21], v[58:59], v[58:59], v[20:21]\n" \
  "v_pk_add_f32 v[18:19], v[18:19], v[58:59]\n" \
  "v_pk_fma_f32 v[20:21], v[60:61], v[60:61], v[20:21]\n" \
  "v_pk_add_f32 v[18:19], v[18:19], v[60:61]\n" \
  "v_pk_fma_f32 v[20:21], v[62:63], v[62:63], v[20:21]\n" \
  "v_pk_add_f32 v[18:19], v[18:19], v[62:63]\n" \
  "buffer_load_dwordx2 v[144:145], v25, s[96:99], 0 offen offset:0 sc1\n" \
  "buffer_load_dwordx2 v[146:147], v25, s[96:99], 0 offen offset:64 sc1\n" \
  "buffer_load_dwordx2 v[148:149], v25, s[96:99], 0 offen offset:512 sc1\n" \
  "buffer_load_dwordx2 v[150:151], v25, s[96:99], 0 offen offset:576 sc1\n" \
  "buffer_load_dwordx2 v[152:153], v25, s[96:99], 0 offen offset:1024 sc1\n" \
  "buffer_load_dwordx2 v[154:155], v25, s[96:99], 0 offen offset:1088 sc1\n" \
  "buffer_load_dwordx2 v[156:157], v25, s[96:99], 0 offen offset:1536 sc1\n" \
  "buffer_load_dwordx2 v[158:159], v25, s[96:99], 0 offen offset:1600 sc1\n" \
  "v_add_f32 v18, v18, v19\n" \
  "v_add_f32 v19, v20, v21\n" \
  "s_nop 0\n" \
  "v_add_f32_dpp v18, v18, v18 row_ror:8 row_mask:0xf bank_mask:0xf\n" \
  "v_add_f32_dpp v19, v19, v19 row_ror:8 row_mask:0xf bank_mask:0xf\n" \
  "v_mov_b32 v22, v18\n" \
  "v_mov_b32 v23, v19\n" \
  "s_nop 0\n" \
  "v_permlane16_swap_b32 v18, v22\n" \
  "v_permlane16_swap_b32 v19, v23\n" \
  "v_pk_add_f32 v[18:19], v[18:19], v[22:23]\n" \
  "v_mov_b32 v22, v18\n" \
  "v_mov_b32 v23, v19\n" \
  "s_nop 0\n" \
  "v_permlane32_swap_b32 v18, v22\n" \
  "v_permlane32_swap_b32 v19, v23\n" \
  "v_pk_add_f32 v[18:19], v[18:19], v[22:23]\n" \
  "ds_write_b64 v176, v[18:19] offset:0\n" \
  "s_waitcnt lgkmcnt(0)\n" \
  "s_barrier\n" \
  "ds_read_b128 v[180:183], v177 offset:0\n" \
  "ds_read_b128 v[184:187], v177 offset:16\n" \
  "s_waitcnt lgkmcnt(1)\n" \
  "v_pk_add_f32 v[180:181], v[180:181], v[182:183]\n" \
  "s_waitcnt lgkmcnt(0)\n" \
  "v_pk_add_f32 v[184:185], v[184:185], v[186:187]\n" \
  "v_pk_add_f32 v[180:181], v[180:181], v[184:185]\n" \
  "v_mul_f32 v180, 0x3a800000, v180\n" \
  "v_mul_f32 v181, 0x3a800000, v181\n" \
  "v_fma_f32 v181, -v180, v180, v181\n" \
  "v_add_f32 v180, v180, v16\n" \
  "v_max_f32 v181, 0, v181\n" \
  "v_cvt_pk_bf16_f32 v180, v180, v181\n" \
  "v_and_b32 v181, 0xffff0000, v180\n" \
  "v_lshlrev_b32 v180, 16, v180\n" \
  "v_add_f32 v181, 0x3727c5ac, v181\n" \
  "v_sub_f32 v22, v180, v16\n" \
  "v_cvt_pk_bf16_f32 v181, v181, v181\n" \
  "v_lshlrev_b32 v181, 16, v181\n" \
  "v_sqrt_f32 v181, v181\n" \
  "s_nop 0\n" \
  "v_cvt_pk_bf16_f32 v181, v181, v181\n" \
  "v_lshlrev_b32 v181, 16, v181\n" \
  "v_rcp_f32 v17, v181\n" \
  "s_nop 0\n" \
  "v_pk_add_f32 v[32:33], v[32:33], v[22:23] op_sel_hi:[1,0] neg_lo:[0,1] neg_hi:[0,1]\n" \
  "v_pk_add_f32 v[34:35], v[34:35], v[22:23] op_sel_hi:[1,0] neg_lo:[0,1] neg_hi:[0,1]\n" \
  "v_pk_add_f32 v[36:37], v[36:37], v[22:23] op_sel_hi:[1,0] neg_lo:[0,1] neg_hi:[0,1]\n" \
  "v_pk_add_f32 v[38:39], v[38:39], v[22:23] op_sel_hi:[1,0] neg_lo:[0,1] neg_hi:[0,1]\n" \
  "v_pk_add_f32 v[40:41], v[40:41], v[22:23] op_sel_hi:[1,0] neg_lo:[0,1] neg_hi:[0,1]\n" \
  "v_pk_add_f32 v[42:43], v[42:43], v[22:23] op_sel_hi:[1,0] neg_lo:[0,1] neg_hi:[0,1]\n" \
  "v_pk_add_f32 v[44:45], v[44:45], v[22:23] op_sel_hi:[1,0] neg_lo:[0,1] neg_hi:[0,1]\n" \
  "v_pk_add_f32 v[46:47], v[46:47], v[22:23] op_sel_hi:[1,0] neg_lo:[0,1] neg_hi:[0,1]\n" \
  "v_pk_add_f32 v[48:49], v[48:49], v[22:23] op_sel_hi:[1,0] neg_lo:[0,1] neg_hi:[0,1]\n" \
  "v_pk_add_f32 v[50:51], v[50:51], v[22:23] op_sel_hi:[1,0] neg_lo:[0,1] neg_hi:[0,1]\n" \
  "v_pk_add_f32 v[52:53], v[52:53], v[22:23] op_sel_hi:[1,0] neg_lo:[0,1] neg_hi:[0,1]\n" \
  "v_pk_add_f32 v[54:55], v[54:55], v[22:23] op_sel_hi:[1,0] neg_lo:[0,1] neg_hi:[0,1]\n" \
  "v_pk_add_f32 v[56:57], v[56:57], v[22:23] op_sel_hi:[1,0] neg_lo:[0,1] neg_hi:[0,1]\n" \
  "v_pk_add_f32 v[58:59], v[58:59], v[22:23] op_sel_hi:[1,0] neg_lo:[0,1] neg_hi:[0,1]\n" \
  "v_pk_add_f32 v[60:61], v[60:61], v[22:23] op_sel_hi:[1,0] neg_lo:[0,1] neg_hi:[0,1]\n" \
  "v_pk_add_f32 v[62:63], v[62:63], v[22:23] op_sel_hi:[1,0] neg_lo:[0,1] neg_hi:[0,1]\n" \
  "v_mov_b32 v16, v22\n" \
  "s_waitcnt vmcnt(0)\n" \
  "v_max3_u32 v188, v144, v145, v146\n" \
  "v_max3_u32 v188, v188, v147, v148\n" \
  "v_max3_u32 v188, v188, v149, v150\n" \
  "v_max3_u32 v188, v188, v151, v152\n" \
  "v_max3_u32 v188, v188, v153, v154\n" \
  "v_max3_u32 v188, v188, v155, v156\n" \
  "v_max3_u32 v188, v188, v157, v158\n" \
  "v_max_u32 v188, v188, v159\n" \
  "v_cmp_eq_u32 vcc, -1, v188\n" \
  "s_cbranch_vccz 31f\n" \
  "s_mov_b32 s15, 0x100000\n" \
  "30:\n" \
  "buffer_load_dwordx2 v[144:145], v25, s[96:99], 0 offen offset:0 sc1\n" \
  "buffer_load_dwordx2 v[146:147], v25, s[96:99], 0 offen offset:64 sc1\n" \
  "buffer_load_dwordx2 v[148:149], v25, s[96:99], 0 offen offset:512 sc1\n" \
  "buffer_load_dwordx2 v[150:151], v25, s[96:99], 0 offen offset:576 sc1\n" \
  "buffer_load_dwordx2 v[152:153], v25, s[96:99], 0 offen offset:1024 sc1\n" \
  "buffer_load_dwordx2 v[154:155], v25, s[96:99], 0 offen offset:1088 sc1\n" \
  "buffer_load_dwordx2 v[156:157], v25, s[96:99], 0 offen offset:1536 sc1\n" \
  "buffer_load_dwordx2 v[158:159], v25, s[96:99], 0 offen offset:1600 sc1\n" \
  "s_waitcnt vmcnt(0)\n" \
  "v_max3_u32 v188, v144, v145, v146\n" \
  "v_max3_u32 v188, v188, v147, v148\n" \
  "v_max3_u32 v188, v188, v149, v150\n" \
  "v_max3_u32 v188, v188, v151, v152\n" \
  "v_max3_u32 v188, v188, v153, v154\n" \
  "v_max3_u32 v188, v188, v155, v156\n" \
  "v_max3_u32 v188, v188, v157, v158\n" \
  "v_max_u32 v188, v188, v159\n" \
  "v_cmp_eq_u32 vcc, -1, v188\n" \
  "s_cbranch_vccz 31f\n" \
  "s_sub_u32 s15, s15, 1\n" \
  "s_cmp_lg_u32 s15, 0\n" \
  "s_cbranch_scc1 30b\n" \
  "31:\n" \
  "v_lshlrev_b32 v64, 16, v144\n" \
  "v_and_b32 v65, 0xffff0000, v144\n" \
  "v_lshlrev_b32 v66, 16, v145\n" \
  "v_and_b32 v67, 0xffff0000, v145\n" \
  "v_lshlrev_b32 v68, 16, v146\n" \
  "v_and_b32 v69, 0xffff0000, v146\n" \
  "v_lshlrev_b32 v70, 16, v147\n" \
  "v_and_b32 v71, 0xffff0000, v147\n" \
  "v_lshlrev_b32 v72, 16, v148\n" \
  "v_and_b32 v73, 0xffff0000, v148\n" \
  "v_lshlrev_b32 v74, 16, v149\n" \
  "v_and_b32 v75, 0xffff0000, v149\n" \
  "v_lshlrev_b32 v76, 16, v150\n" \
  "v_and_b32 v77, 0xffff0000, v150\n" \
  "v_lshlrev_b32 v78, 16, v151\n" \
  "v_and_b32 v79, 0xffff0000, v151\n" \
  "v_lshlrev_b32 v80, 16, v152\n" \
  "v_and_b32 v81, 0xffff0000, v152\n" \
  "v_lshlrev_b32 v82, 16, v153\n" \
  "v_and_b32 v83, 0xffff0000, v153\n" \
  "v_lshlrev_b32 v84, 16, v154\n" \
  "v_and_b32 v85, 0xffff0000, v154\n" \
  "v_lshlrev_b32 v86, 16, v155\n" \
  "v_and_b32 v87, 0xffff0000, v155\n" \
  "v_lshlrev_b32 v88, 16, v156\n" \
  "v_and_b32 v89, 0xffff0000, v156\n" \
  "v_lshlrev_b32 v90, 16, v157\n" \
  "v_and_b32 v91, 0xffff0000, v157\n" \
  "v_lshlrev_b32 v92, 16, v158\n" \
  "v_and_b32 v93, 0xffff0000, v158\n" \
  "v_lshlrev_b32 v94, 16, v159\n" \
  "v_and_b32 v95, 0xffff0000, v159\n" \
  "9:\n" \
  "v_pk_fma_f32 v[178:179], v[64:65], v[16:17], 1.0 op_sel:[0,1,0] op_sel_hi:[1,1,0]\n" \
  "v_pk_fma_f32 v[180:181], v[66:67], v[16:17], 1.0 op_sel:[0,1,0] op_sel_hi:[1,1,0]\n" \
  "v_pk_fma_f32 v[182:183], v[68:69], v[16:17], 1.0 op_sel:[0,1,0] op_sel_hi:[1,1,0]\n" \
  "v_pk_fma_f32 v[184:185], v[70:71], v[16:17], 1.0 op_sel:[0,1,0] op_sel_hi:[1,1,0]\n" \
  "v_pk_mul_f32 v[32:33], v[32:33], v[178:179]\n" \
  "v_pk_mul_f32 v[34:35], v[34:35], v[180:181]\n" \
  "v_pk_mul_f32 v[36:37], v[36:37], v[182:183]\n" \
  "v_pk_mul_f32 v[38:39], v[38:39], v[184:185]\n" \
  "v_pk_mul_f32 v[20:21], v[32:33], v[32:33]\n" \
  "v_pk_add_f32 v[18:19], v[32:33], 0 op_sel_hi:[1,0]\n" \
  "v_pk_fma_f32 v[20:21], v[34:35], v[34:35], v[20:21]\n" \
  "v_pk_add_f32 v[18:19], v[18:19], v[34:35]\n" \
  "v_pk_fma_f32 v[20:21], v[36:37], v[36:37], v[20:21]\n" \
  "v_pk_add_f32 v[18:19], v[18:19], v[36:37]\n" \
  "v_pk_fma_f32 v[20:21], v[38:39], v[38:39], v[20:21]\n" \
  "v_pk_add_f32 v[18:19], v[18:19], v[38:39]\n" \
  "v_pk_fma_f32 v[178:179], v[72:73], v[16:17], 1.0 op_sel:[0,1,0] op_sel_hi:[1,1,0]\n" \
  "v_pk_fma_f32 v[180:181], v[74:75], v[16:17], 1.0 op_sel:[0,1,0] op_sel_hi:[1,1,0]\n" \
  "v_pk_fma_f32 v[182:183], v[76:77], v[16:17], 1.0 op_sel:[0,1,0] op_sel_hi:[1,1,0]\n" \
  "v_pk_fma_f32 v[184:185], v[78:79], v[16:17], 1.0 op_sel:[0,1,0] op_sel_hi:[1,1,0]\n" \
  "v_pk_mul_f32 v[40:41], v[40:41], v[178:179]\n" \
  "v_pk_mul_f32 v[42:43], v[42:43], v[180:181]\n" \
  "v_pk_mul_f32 v[44:45], v[44:45], v[182:183]\n" \
  "v_pk_mul_f32 v[46:47], v[46:47], v[184:185]\n" \
  "v_pk_fma_f32 v[20:21], v[40:41], v[40:41], v[20:21]\n" \
  "v_pk_add_f32 v[18:19], v[18:19], v[40:41]\n" \
  "v_pk_fma_f32 v[20:21], v[42:43], v[42:43], v[20:21]\n" \
  "v_pk_add_f32 v[18:19], v[18:19], v[42:43]\n" \
  "v_pk_fma_f32 v[20:21], v[44:45], v[44:45], v[20:21]\n" \
  "v_pk_add_f32 v[18:19], v[18:19], v[44:45]\n" \
  "v_pk_fma_f32 v[20:21], v[46:47], v[46:47], v[20:21]\n" \
  "v_pk_add_f32 v[18:19], v[18:19], v[46:47]\n" \
  "v_pk_fma_f32 v[178:179], v[80:81], v[16:17], 1.0 op_sel:[0,1,0] op_sel_hi:[1,1,0]\n" \
  "v_pk_fma_f32 v[180:181], v[82:83], v[16:17], 1.0 op_sel:[0,1,0] op_sel_hi:[1,1,0]\n" \
  "v_pk_fma_f32 v[182:183], v[84:85], v[16:17], 1.0 op_sel:[0,1,0] op_sel_hi:[1,1,0]\n" \
  "v_pk_fma_f32 v[184:185], v[86:87], v[16:17], 1.0 op_sel:[0,1,0] op_sel_hi:[1,1,0]\n" \
  "v_pk_mul_f32 v[48:49], v[48:49], v[178:179]\n" \
  "v_pk_mul_f32 v[50:51], v[50:51], v[180:181]\n" \
  "v_pk_mul_f32 v[52:53], v[52:53], v[182:183]\n" \
  "v_pk_mul_f32 v[54:55], v[54:55], v[184:185]\n" \
  "v_pk_fma_f32 v[20:21], v[48:49], v[48:49], v[20:21]\n" \
  "v_pk_add_f32 v[18:19], v[18:19], v[48:49]\n" \
  "v_pk_fma_f32 v[20:21], v[50:51], v[50:51], v[20:21]\n" \
  "v_pk_add_f32 v[18:19], v[18:19], v[50:51]\n" \
  "v_pk_fma_f32 v[20:21], v[52:53], v[52:53], v[20:21]\n" \
  "v_pk_add_f32 v[18:19], v[18:19], v[52:53]\n" \
  "v_pk_fma_f32 v[20:21], v[54:55], v[54:55], v[20:21]\n" \
  "v_pk_add_f32 v[18:19], v[18:19], v[54:55]\n" \
  "v_pk_fma_f32 v[178:179], v[88:89], v[16:17], 1.0 op_sel:[0,1,0] op_sel_hi:[1,1,0]\n" \
  "v_pk_fma_f32 v[180:181], v[90:91], v[16:17], 1.0 op_sel:[0,1,0] op_sel_hi:[1,1,0]\n" \
  "v_pk_fma_f32 v[182:183], v[92:93], v[16:17], 1.0 op_sel:[0,1,0] op_sel_hi:[1,1,0]\n" \
  "v_pk_fma_f32 v[184:185], v[94:95], v[16:17], 1.0 op_sel:[0,1,0] op_sel_hi:[1,1,0]\n" \
  "v_pk_mul_f32 v[56:57], v[56:57], v[178:179]\n" \
  "v_pk_mul_f32 v[58:59], v[58:59], v[180:181]\n" \
  "v_pk_mul_f32 v[60:61], v[60:61], v[182:183]\n" \
  "v_pk_mul_f32 v[62:63], v[62:63], v[184:185]\n" \
  "v_pk_fma_f32 v[20:21], v[56:57], v[56:57], v[20:21]\n" \
  "v_pk_add_f32 v[18:19], v[18:19], v[56:57]\n" \
  "v_pk_fma_f32 v[20:21], v[58:59], v[58:59], v[20:21]\n" \
  "v_pk_add_f32 v[18:19], v[18:19], v[58:59]\n" \
  "v_pk_fma_f32 v[20:21], v[60:61], v[60:61], v[20:21]\n" \
  "v_pk_add_f32 v[18:19], v[18:19], v[60:61]\n" \
  "v_pk_fma_f32 v[20:21], v[62:63], v[62:63], v[20:21]\n" \
  "v_pk_add_f32 v[18:19], v[18:19], v[62:63]\n" \
  "v_add_f32 v18, v18, v19\n" \
  "v_add_f32 v19, v20, v21\n" \
  "s_nop 0\n" \
  "v_add_f32_dpp v18, v18, v18 row_ror:8 row_mask:0xf bank_mask:0xf\n" \
  "v_add_f32_dpp v19, v19, v19 row_ror:8 row_mask:0xf bank_mask:0xf\n" \
  "v_mov_b32 v22, v18\n" \
  "v_mov_b32 v23, v19\n" \
  "s_nop 0\n" \
  "v_permlane16_swap_b32 v18, v22\n" \
  "v_permlane16_swap_b32 v19, v23\n" \
  "v_pk_add_f32 v[18:19], v[18:19], v[22:23]\n" \
  "v_mov_b32 v22, v18\n" \
  "v_mov_b32 v23, v19\n" \
  "s_nop 0\n" \
  "v_permlane32_swap_b32 v18, v22\n" \
  "v_permlane32_swap_b32 v19, v23\n" \
  "v_pk_add_f32 v[18:19], v[18:19], v[22:23]\n" \
  "ds_write_b64 v176, v[18:19] offset:256\n" \
  "s_waitcnt lgkmcnt(0)\n" \
  "s_barrier\n" \
  "ds_read_b128 v[180:183], v177 offset:256\n" \
  "ds_read_b128 v[184:187], v177 offset:272\n" \
  "s_waitcnt lgkmcnt(1)\n" \
  "v_pk_add_f32 v[180:181], v[180:181], v[182:183]\n" \
  "s_waitcnt lgkmcnt(0)\n" \
  "v_pk_add_f32 v[184:185], v[184:185], v[186:187]\n" \
  "v_pk_add_f32 v[180:181], v[180:181], v[184:185]\n" \
  "v_mul_f32 v180, 0x3a800000, v180\n" \
  "v_mul_f32 v181, 0x3a800000, v181\n" \
  "v_fma_f32 v181, -v180, v180, v181\n" \
  "v_add_f32 v180, v180, v16\n" \
  "v_max_f32 v181, 0, v181\n" \
  "v_cvt_pk_bf16_f32 v180, v180, v181\n" \
  "v_and_b32 v181, 0xffff0000, v180\n" \
  "v_lshlrev_b32 v180, 16, v180\n" \
  "v_add_f32 v181, 0x3727c5ac, v181\n" \
  "v_sub_f32 v23, v180, v16\n" \
  "v_cvt_pk_bf16_f32 v181, v181, v181\n" \
  "v_lshlrev_b32 v181, 16, v181\n" \
  "v_sqrt_f32 v181, v181\n" \
  "s_nop 0\n" \
  "v_cvt_pk_bf16_f32 v181, v181, v181\n" \
  "v_lshlrev_b32 v181, 16, v181\n" \
  "v_rcp_f32 v22, v181\n" \
  "s_nop 0\n" \
  "v_mul_f32 v18, -v23, v22\n" \
  "v_pk_fma_f32 v[178:179], v[32:33], v[22:23], v[18:19] op_sel_hi:[1,0,0]\n" \
  "v_pk_fma_f32 v[180:181], v[34:35], v[22:23], v[18:19] op_sel_hi:[1,0,0]\n" \
  "v_pk_fma_f32 v[182:183], v[36:37], v[22:23], v[18:19] op_sel_hi:[1,0,0]\n" \
  "v_pk_fma_f32 v[184:185], v[38:39], v[22:23], v[18:19] op_sel_hi:[1,0,0]\n" \
  "v_cvt_pk_bf16_f32 v160, v178, v179\n" \
  "v_cvt_pk_bf16_f32 v161, v180, v181\n" \
  "v_cvt_pk_bf16_f32 v162, v182, v183\n" \
  "v_cvt_pk_bf16_f32 v163, v184, v185\n" \
  "buffer_store_dwordx4 v[160:163], v12, s[72:75], 0 offen offset:0 sc1\n" \
  "v_pk_fma_f32 v[178:179], v[40:41], v[22:23], v[18:19] op_sel_hi:[1,0,0]\n" \
  "v_pk_fma_f32 v[180:181], v[42:43], v[22:23], v[18:19] op_sel_hi:[1,0,0]\n" \
  "v_pk_fma_f32 v[182:183], v[44:45], v[22:23], v[18:19] op_sel_hi:[1,0,0]\n" \
  "v_pk_fma_f32 v[184:185], v[46:47], v[22:23], v[18:19] op_sel_hi:[1,0,0]\n" \
  "v_cvt_pk_bf16_f32 v164, v178, v179\n" \
  "v_cvt_pk_bf16_f32 v165, v180, v181\n" \
  "v_cvt_pk_bf16_f32 v166, v182, v183\n" \
  "v_cvt_pk_bf16_f32 v167, v184, v185\n" \
  "buffer_store_dwordx4 v[164:167], v12, s[72:75], 0 offen offset:2048 sc1\n" \
  "v_pk_fma_f32 v[178:179], v[48:49], v[22:23], v[18:19] op_sel_hi:[1,0,0]\n" \
  "v_pk_fma_f32 v[180:181], v[50:51], v[22:23], v[18:19] op_sel_hi:[1,0,0]\n" \
  "v_pk_fma_f32 v[182:183], v[52:53], v[22:23], v[18:19] op_sel_hi:[1,0,0]\n" \
  "v_pk_fma_f32 v[184:185], v[54:55], v[22:23], v[18:19] op_sel_hi:[1,0,0]\n" \
  "v_cvt_pk_bf16_f32 v168, v178, v179\n" \
  "v_cvt_pk_bf16_f32 v169, v180, v181\n" \
  "v_cvt_pk_bf16_f32 v170, v182, v183\n" \
  "v_cvt_pk_bf16_f32 v171, v184, v185\n" \
  "buffer_store_dwordx4 v[168:171], v12, s[72:75], s16 offen offset:0 sc1\n" \
  "v_pk_fma_f32 v[178:179], v[56:57], v[22:23], v[18:19] op_sel_hi:[1,0,0]\n" \
  "v_pk_fma_f32 v[180:181], v[58:59], v[22:23], v[18:19] op_sel_hi:[1,0,0]\n" \
  "v_pk_fma_f32 v[182:183], v[60:61], v[22:23], v[18:19] op_sel_hi:[1,0,0]\n" \
  "v_pk_fma_f32 v[184:185], v[62:63], v[22:23], v[18:19] op_sel_hi:[1,0,0]\n" \
  "v_cvt_pk_bf16_f32 v172, v178, v179\n" \
  "v_cvt_pk_bf16_f32 v173, v180, v181\n" \
  "v_cvt_pk_bf16_f32 v174, v182, v183\n" \
  "v_cvt_pk_bf16_f32 v175, v184, v185\n" \
  "buffer_store_dwordx4 v[172:175], v12, s[72:75], s16 offen offset:2048 sc1\n" \
  "v_pk_add_f32 v[32:33], v[32:33], v[16:17] op_sel_hi:[1,0]\n" \
  "v_pk_add_f32 v[34:35], v[34:35], v[16:17] op_sel_hi:[1,0]\n" \
  "v_pk_add_f32 v[36:37], v[36:37], v[16:17] op_sel_hi:[1,0]\n" \
  "v_pk_add_f32 v[38:39], v[38:39], v[16:17] op_sel_hi:[1,0]\n" \
  "v_pk_add_f32 v[40:41], v[40:41], v[16:17] op_sel_hi:[1,0]\n" \
  "v_pk_add_f32 v[42:43], v[42:43], v[16:17] op_sel_hi:[1,0]\n" \
  "v_pk_add_f32 v[44:45], v[44:45], v[16:17] op_sel_hi:[1,0]\n" \
  "v_pk_add_f32 v[46:47], v[46:47], v[16:17] op_sel_hi:[1,0]\n" \
  "v_pk_add_f32 v[48:49], v[48:49], v[16:17] op_sel_hi:[1,0]\n" \
  "v_pk_add_f32 v[50:51], v[50:51], v[16:17] op_sel_hi:[1,0]\n" \
  "v_pk_add_f32 v[52:53], v[52:53], v[16:17] op_sel_hi:[1,0]\n" \
  "v_pk_add_f32 v[54:55], v[54:55], v[16:17] op_sel_hi:[1,0]\n" \
  "v_pk_add_f32 v[56:57], v[56:57], v[16:17] op_sel_hi:[1,0]\n" \
  "v_pk_add_f32 v[58:59], v[58:59], v[16:17] op_sel_hi:[1,0]\n" \
  "v_pk_add_f32 v[60:61], v[60:61], v[16:17] op_sel_hi:[1,0]\n" \
  "v_pk_add_f32 v[62:63], v[62:63], v[16:17] op_sel_hi:[1,0]\n" \
  "v_cvt_pk_bf16_f32 v128, v32, v33\n" \
  "v_cvt_pk_bf16_f32 v129, v34, v35\n" \
  "v_cvt_pk_bf16_f32 v130, v36, v37\n" \
  "v_cvt_pk_bf16_f32 v131, v38, v39\n" \
  "v_cvt_pk_bf16_f32 v132, v40, v41\n" \
  "v_cvt_pk_bf16_f32 v133, v42, v43\n" \
  "v_cvt_pk_bf16_f32 v134, v44, v45\n" \
  "v_cvt_pk_bf16_f32 v135, v46, v47\n" \
  "v_cvt_pk_bf16_f32 v136, v48, v49\n" \
  "v_cvt_pk_bf16_f32 v137, v50, v51\n" \
  "v_cvt_pk_bf16_f32 v138, v52, v53\n" \
  "v_cvt_pk_bf16_f32 v139, v54, v55\n" \
  "v_cvt_pk_bf16_f32 v140, v56, v57\n" \
  "v_cvt_pk_bf16_f32 v141, v58, v59\n" \
  "v_cvt_pk_bf16_f32 v142, v60, v61\n" \
  "v_cvt_pk_bf16_f32 v143, v62, v63\n" \
  "v_add_f32 v16, v16, v23\n" \
  "v_accvgpr_read_b32 v20, a167\n" \
  "v_mbcnt_lo_u32_b32 v21, -1, 0\n" \
  "v_min_u32 v21, 2, v21\n" \
  "v_lshlrev_b32 v21, 7, v21\n" \
  "v_add_u32 v21, s29, v21\n" \
  "v_min_u32 v21, .Lmg_end-.Lmg_mid, v21\n" \
  "v_accvgpr_write_b32 a224, v128\n" \
  "v_accvgpr_write_b32 a240, v144\n" \
  "v_accvgpr_write_b32 a225, v129\n" \
  "v_accvgpr_write_b32 a241, v145\n" \
  "v_accvgpr_write_b32 a226, v130\n" \
  "v_accvgpr_write_b32 a242, v146\n" \
  "v_accvgpr_write_b32 a227, v131\n" \
  "v_accvgpr_write_b32 a243, v147\n" \
  "v_accvgpr_write_b32 a228, v132\n" \
  "v_accvgpr_write_b32 a244, v148\n" \
  "v_accvgpr_write_b32 a229, v133\n" \
  "v_accvgpr_write_b32 a245, v149\n" \
  "v_accvgpr_write_b32 a230, v134\n" \
  "v_accvgpr_write_b32 a246, v150\n" \
  "v_accvgpr_write_b32 a231, v135\n" \
  "v_accvgpr_write_b32 a247, v151\n" \
  "v_accvgpr_write_b32 a232, v136\n" \
  "v_accvgpr_write_b32 a248, v152\n" \
  "v_accvgpr_write_b32 a233, v137\n" \
  "v_accvgpr_write_b32 a249, v153\n" \
  "v_accvgpr_write_b32 a234, v138\n" \
  "v_accvgpr_write_b32 a250, v154\n" \
  "v_accvgpr_write_b32 a235, v139\n" \
  "v_accvgpr_write_b32 a251, v155\n" \
  "v_accvgpr_write_b32 a236, v140\n" \
  "v_accvgpr_write_b32 a252, v156\n" \
  "v_accvgpr_write_b32 a237, v141\n" \
  "v_accvgpr_write_b32 a253, v157\n" \
  "v_accvgpr_write_b32 a238, v142\n" \
  "v_accvgpr_write_b32 a254, v158\n" \
  "v_accvgpr_write_b32 a239, v143\n" \
  "v_accvgpr_write_b32 a255, v159\n" \
  "v_accvgpr_write_b32 a159, v16\n" \
  "v_mbcnt_lo_u32_b32 v16, -1, 0\n" \
  "v_mbcnt_hi_u32_b32 v16, -1, v16\n" \
  "s_lshl_b32 s19, s83, 6\n" \
  "v_add_u32 v17, s19, v16\n" \
  "v_and_b32 v16, 31, v16\n" \
  "v_cmp_gt_u32 vcc, 32, v17\n" \
  "v_lshlrev_b32 v17, 7, v16\n" \
  "v_lshlrev_b32 v16, 2, v16\n" \
  "v_add_u32 v17, s92, v17\n" \
  "v_add_u32 v16, s93, v16\n" \
  "v_bfrev_b32 v18, 1\n" \
  "v_cndmask_b32 v17, v18, v17, vcc\n" \
  "v_mov_b32 v24, 0xbdd2d3e8\n" \
  "v_mov_b32 v25, 0xbdd2d3e8\n" \
  "v_mov_b32 v26, 0xc0135761\n" \
  "v_mov_b32 v27, 0xc0135761\n" \
  "s_waitcnt vmcnt(0)\n" \
  "s_barrier\n" \
  "buffer_store_dword v24, v17, s[68:71], 0 offen sc1\n" \
  "s_mov_b32 s8, 0\n" \
  "s_mov_b32 s9, 0\n" \
  "s_mov_b32 s11, 0\n" \
  "s_mov_b32 s12, 0\n" \
  "s_mov_b32 s28, 0\n" \
  "s_mov_b32 s13, s88\n" \
  "s_mov_b32 s14, s87\n" \
  "s_mov_b32 s17, s91\n" \
  "v_mov_b32 v28, v9\n" \
  "s_mov_b32 s80, s82\n" \
  "s_mov_b32 s81, 0\n" \
  "s_mov_b32 m0, s80\n" \
  "s_nop 0\n" \
  "buffer_load_dwordx4 v0, s[36:39], s81 offen offset:0 lds\n" \
  "v_mov_b32 v32, 0\n" \
  "v_mov_b32 v33, 0\n" \
  "v_mov_b32 v34, 0\n" \
  "v_mov_b32 v35, 0\n" \
  "v_mov_b32 v36, 0\n" \
  "v_mov_b32 v37, 0\n" \
  "v_mov_b32 v38, 0\n" \
  "v_mov_b32 v39, 0\n" \
  "v_mov_b32 v40, 0\n" \
  "v_mov_b32 v41, 0\n" \
  "v_mov_b32 v42, 0\n" \
  "v_mov_b32 v43, 0\n" \
  "v_mov_b32 v44, 0\n" \
  "v_mov_b32 v45, 0\n" \
  "v_mov_b32 v46, 0\n" \
  "v_mov_b32 v47, 0\n" \
  "buffer_load_dwordx4 v1, s[36:39], s81 offen offset:1024 lds\n" \
  "v_mov_b32 v48, 0\n" \
  "v_mov_b32 v49, 0\n" \
  "v_mov_b32 v50, 0\n" \
  "v_mov_b32 v51, 0\n" \
  "v_mov_b32 v52, 0\n" \
  "v_mov_b32 v53, 0\n" \
  "v_mov_b32 v54, 0\n" \
  "v_mov_b32 v55, 0\n" \
  "v_mov_b32 v56, 0\n" \
  "v_mov_b32 v57, 0\n" \
  "v_mov_b32 v58, 0\n" \
  "v_mov_b32 v59, 0\n" \
  "v_mov_b32 v60, 0\n" \
  "v_mov_b32 v61, 0\n" \
  "v_mov_b32 v62, 0\n" \
  "v_mov_b32 v63, 0\n" \
  "buffer_load_dwordx4 v2, s[36:39], s81 offen offset:2048 lds\n" \
  "v_mov_b32 v64, 0\n" \
  "v_mov_b32 v65, 0\n" \
  "v_mov_b32 v66, 0\n" \
  "v_mov_b32 v67, 0\n" \
  "v_mov_b32 v68, 0\n" \
  "v_mov_b32 v69, 0\n" \
  "v_mov_b32 v70, 0\n" \
  "v_mov_b32 v71, 0\n" \
  "v_mov_b32 v72, 0\n" \
  "v_mov_b32 v73, 0\n" \
  "v_mov_b32 v74, 0\n" \
  "v_mov_b32 v75, 0\n" \
  "v_mov_b32 v76, 0\n" \
  "v_mov_b32 v77, 0\n" \
  "v_mov_b32 v78, 0\n" \
  "v_mov_b32 v79, 0\n" \
  "buffer_load_dwordx4 v3, s[36:39], s81 offen offset:3072 lds\n" \
  "v_mov_b32 v80, 0\n" \
  "v_mov_b32 v81, 0\n" \
  "v_mov_b32 v82, 0\n" \
  "v_mov_b32 v83, 0\n" \
  "v_mov_b32 v84, 0\n" \
  "v_mov_b32 v85, 0\n" \
  "v_mov_b32 v86, 0\n" \
  "v_mov_b32 v87, 0\n" \
  "v_mov_b32 v88, 0\n" \
  "v_mov_b32 v89, 0\n" \
  "v_mov_b32 v90, 0\n" \
  "v_mov_b32 v91, 0\n" \
  "v_mov_b32 v92, 0\n" \
  "v_mov_b32 v93, 0\n" \
  "v_mov_b32 v94, 0\n" \
  "v_mov_b32 v95, 0\n" \
  "s_add_u32 m0, s80, 4096\n" \
  "s_nop 0\n" \
  "buffer_load_dwordx4 v4, s[36:39], s81 offen offset:0 lds\n" \
  "v_mov_b32 v96, 0\n" \
  "v_mov_b32 v97, 0\n" \
  "v_mov_b32 v98, 0\n" \
  "v_mov_b32 v99, 0\n" \
  "v_mov_b32 v100, 0\n" \
  "v_mov_b32 v101, 0\n" \
  "v_mov_b32 v102, 0\n" \
  "v_mov_b32 v103, 0\n" \
  "v_mov_b32 v104, 0\n" \
  "v_mov_b32 v105, 0\n" \
  "v_mov_b32 v106, 0\n" \
  "v_mov_b32 v107, 0\n" \
  "v_mov_b32 v108, 0\n" \
  "v_mov_b32 v109, 0\n" \
  "v_mov_b32 v110, 0\n" \
  "v_mov_b32 v111, 0\n" \
  "buffer_load_dwordx4 v5, s[36:39], s81 offen offset:1024 lds\n" \
  "v_mov_b32 v112, 0\n" \
  "v_mov_b32 v113, 0\n" \
  "v_mov_b32 v114, 0\n" \
  "v_mov_b32 v115, 0\n" \
  "v_mov_b32 v116, 0\n" \
  "v_mov_b32 v117, 0\n" \
  "v_mov_b32 v118, 0\n" \
  "v_mov_b32 v119, 0\n" \
  "v_mov_b32 v120, 0\n" \
  "v_mov_b32 v121, 0\n" \
  "v_mov_b32 v122, 0\n" \
  "v_mov_b32 v123, 0\n" \
  "v_mov_b32 v124, 0\n" \
  "v_mov_b32 v125, 0\n" \
  "v_mov_b32 v126, 0\n" \
  "v_mov_b32 v127, 0\n" \
  "buffer_load_dwordx4 v6, s[36:39], s81 offen offset:2048 lds\n" \
  "v_mov_b32 v128, 0\n" \
  "v_mov_b32 v129, 0\n" \
  "v_mov_b32 v130, 0\n" \
  "v_mov_b32 v131, 0\n" \
  "v_mov_b32 v132, 0\n" \
  "v_mov_b32 v133, 0\n" \
  "v_mov_b32 v134, 0\n" \
  "v_mov_b32 v135, 0\n" \
  "v_mov_b32 v136, 0\n" \
  "v_mov_b32 v137, 0\n" \
  "v_mov_b32 v138, 0\n" \
  "v_mov_b32 v139, 0\n" \
  "v_mov_b32 v140, 0\n" \
  "v_mov_b32 v141, 0\n" \
  "v_mov_b32 v142, 0\n" \
  "v_mov_b32 v143, 0\n" \
  "s_add_u32 s81, s81, 458752\n" \
  "s_add_u32 s80, s80, 28672\n" \
  "s_cmp_ge_u32 s80, 114688\n" \
  "s_cselect_b32 s80, s82, s80\n" \
  "s_mov_b32 m0, s80\n" \
  "s_nop 0\n" \
  "buffer_load_dwordx4 v0, s[36:39], s81 offen offset:0 lds\n" \
  "v_mov_b32 v144, 0\n" \
  "v_mov_b32 v145, 0\n" \
  "v_mov_b32 v146, 0\n" \
  "v_mov_b32 v147, 0\n" \
  "v_mov_b32 v148, 0\n" \
  "v_mov_b32 v149, 0\n" \
  "v_mov_b32 v150, 0\n" \
  "v_mov_b32 v151, 0\n" \
  "v_mov_b32 v152, 0\n" \
  "v_mov_b32 v153, 0\n" \
  "v_mov_b32 v154, 0\n" \
  "v_mov_b32 v155, 0\n" \
  "v_mov_b32 v156, 0\n" \
  "v_mov_b32 v157, 0\n" \
  "v_mov_b32 v158, 0\n" \
  "v_mov_b32 v159, 0\n" \
  "buffer_load_dwordx4 v1, s[36:39], s81 offen offset:1024 lds\n" \
  "v_mov_b32 v160, 0\n" \
  "v_mov_b32 v161, 0\n" \
  "v_mov_b32 v162, 0\n" \
  "v_mov_b32 v163, 0\n" \
  "v_mov_b32 v164, 0\n" \
  "v_mov_b32 v165, 0\n" \
  "v_mov_b32 v166, 0\n" \
  "v_mov_b32 v167, 0\n" \
  "v_mov_b32 v168, 0\n" \
  "v_mov_b32 v169, 0\n" \
  "v_mov_b32 v170, 0\n" \
  "v_mov_b32 v171, 0\n" \
  "v_mov_b32 v172, 0\n" \
  "v_mov_b32 v173, 0\n" \
  "v_mov_b32 v174, 0\n" \
  "v_mov_b32 v175, 0\n" \
  "buffer_load_dwordx4 v2, s[36:39], s81 offen offset:2048 lds\n" \
  "v_mov_b32 v176, 0\n" \
  "v_mov_b32 v177, 0\n" \
  "v_mov_b32 v178, 0\n" \
  "v_mov_b32 v179, 0\n" \
  "v_mov_b32 v180, 0\n" \
  "v_mov_b32 v181, 0\n" \
  "v_mov_b32 v182, 0\n" \
  "v_mov_b32 v183, 0\n" \
  "v_mov_b32 v184, 0\n" \
  "v_mov_b32 v185, 0\n" \
  "v_mov_b32 v186, 0\n" \
  "v_mov_b32 v187, 0\n" \
  "v_mov_b32 v188, 0\n" \
  "v_mov_b32 v189, 0\n" \
  "v_mov_b32 v190, 0\n" \
  "v_mov_b32 v191, 0\n" \
  "buffer_load_dwordx4 v3, s[36:39], s81 offen offset:3072 lds\n" \
  "v_mov_b32 v192, 0\n" \
  "v_mov_b32 v193, 0\n" \
  "v_mov_b32 v194, 0\n" \
  "v_mov_b32 v195, 0\n" \
  "v_mov_b32 v196, 0\n" \
  "v_mov_b32 v197, 0\n" \
  "v_mov_b32 v198, 0\n" \
  "v_mov_b32 v199, 0\n" \
  "v_mov_b32 v200, 0\n" \
  "v_mov_b32 v201, 0\n" \
  "v_mov_b32 v202, 0\n" \
  "v_mov_b32 v203, 0\n" \
  "v_mov_b32 v204, 0\n" \
  "v_mov_b32 v205, 0\n" \
  "v_mov_b32 v206, 0\n" \
  "v_mov_b32 v207, 0\n" \
  "s_add_u32 m0, s80, 4096\n" \
  "s_nop 0\n" \
  "buffer_load_dwordx4 v4, s[36:39], s81 offen offset:0 lds\n" \
  "v_mov_b32 v208, 0\n" \
  "v_mov_b32 v209, 0\n" \
  "v_mov_b32 v210, 0\n" \
  "v_mov_b32 v211, 0\n" \
  "v_mov_b32 v212, 0\n" \
  "v_mov_b32 v213, 0\n" \
  "v_mov_b32 v214, 0\n" \
  "v_mov_b32 v215, 0\n" \
  "v_mov_b32 v216, 0\n" \
  "v_mov_b32 v217, 0\n" \
  "v_mov_b32 v218, 0\n" \
  "v_mov_b32 v219, 0\n" \
  "v_mov_b32 v220, 0\n" \
  "v_mov_b32 v221, 0\n" \
  "v_mov_b32 v222, 0\n" \
  "v_mov_b32 v223, 0\n" \
  "buffer_load_dwordx4 v5, s[36:39], s81 offen offset:1024 lds\n" \
  "v_mov_b32 v224, 0\n" \
  "v_mov_b32 v225, 0\n" \
  "v_mov_b32 v226, 0\n" \
  "v_mov_b32 v227, 0\n" \
  "v_mov_b32 v228, 0\n" \
  "v_mov_b32 v229, 0\n" \
  "v_mov_b32 v230, 0\n" \
  "v_mov_b32 v231, 0\n" \
  "v_mov_b32 v232, 0\n" \
  "v_mov_b32 v233, 0\n" \
  "v_mov_b32 v234, 0\n" \
  "v_mov_b32 v235, 0\n" \
  "v_mov_b32 v236, 0\n" \
  "v_mov_b32 v237, 0\n" \
  "v_mov_b32 v238, 0\n" \
  "v_mov_b32 v239, 0\n" \
  "buffer_load_dwordx4 v6, s[36:39], s81 offen offset:2048 lds\n" \
  "v_mov_b32 v240, 0\n" \
  "v_mov_b32 v241, 0\n" \
  "v_mov_b32 v242, 0\n" \
  "v_mov_b32 v243, 0\n" \
  "v_mov_b32 v244, 0\n" \
  "v_mov_b32 v245, 0\n" \
  "v_mov_b32 v246, 0\n" \
  "v_mov_b32 v247, 0\n" \
  "v_mov_b32 v248, 0\n" \
  "v_mov_b32 v249, 0\n" \
  "v_mov_b32 v250, 0\n" \
  "v_mov_b32 v251, 0\n" \
  "v_mov_b32 v252, 0\n" \
  "v_mov_b32 v253, 0\n" \
  "v_mov_b32 v254, 0\n" \
  "v_mov_b32 v255, 0\n" \
  "s_add_u32 s81, s81, 458752\n" \
  "s_add_u32 s80, s80, 28672\n" \
  "s_cmp_ge_u32 s80, 114688\n" \
  "s_cselect_b32 s80, s82, s80\n" \
  "s_cmp_lg_u32 s83, 0\n" \
  "s_cbranch_scc1 8f\n" \
  "s_mov_b32 s15, 0x400000\n" \
  "7:\n" \
  "buffer_load_dword v30, v16, s[68:71], 0 offen sc1\n" \
  "s_waitcnt vmcnt(0)\n" \
  "v_cmp_eq_u32 vcc, 0, v30\n" \
  "s_cbranch_vccz 8f\n" \
  "s_sub_u32 s15, s15, 1\n" \
  "s_cmp_lg_u32 s15, 0\n" \
  "s_cbranch_scc1 7b\n" \
  "8:\n" \
  "s_waitcnt vmcnt(0)\n" \
  "s_barrier\n" \
  "buffer_load_dwordx4 a[56:59], v7, s[40:43], s8 offen offset:0 sc1\n" \
  "buffer_load_dwordx4 a[60:63], v7, s[40:43], s8 offen offset:512 sc1\n" \
  "buffer_load_dwordx4 a[64:67], v8, s[40:43], s8 offen offset:0 sc1\n" \
  "buffer_load_dwordx4 a[68:71], v8, s[40:43], s8 offen offset:512 sc1\n" \
  "buffer_load_dwordx4 a[72:75], v7, s[40:43], s8 offen offset:2048 sc1\n" \
  "buffer_load_dwordx4 a[76:79], v7, s[40:43], s8 offen offset:2560 sc1\n" \
  "buffer_load_dwordx4 a[80:83], v8, s[40:43], s8 offen offset:2048 sc1\n" \
  "buffer_load_dwordx4 a[84:87], v8, s[40:43], s8 offen offset:2560 sc1\n" \
  "s_add_u32 s8, s8, 4096\n" \
  "buffer_load_dwordx4 a[88:91], v7, s[40:43], s8 offen offset:0 sc1\n" \
  "buffer_load_dwordx4 a[92:95], v7, s[40:43], s8 offen offset:512 sc1\n" \
  "buffer_load_dwordx4 a[96:99], v8, s[40:43], s8 offen offset:0 sc1\n" \
  "buffer_load_dwordx4 a[100:103], v8, s[40:43], s8 offen offset:512 sc1\n" \
  "buffer_load_dwordx4 a[104:107], v7, s[40:43], s8 offen offset:2048 sc1\n" \
  "buffer_load_dwordx4 a[108:111], v7, s[40:43], s8 offen offset:2560 sc1\n" \
  "buffer_load_dwordx4 a[112:115], v8, s[40:43], s8 offen offset:2048 sc1\n" \
  "buffer_load_dwordx4 a[116:119], v8, s[40:43], s8 offen offset:2560 sc1\n" \
  "s_add_u32 s8, s8, 4096\n" \
  "ds_read_b64_tr_b16 a[0:1], v28 offset:0\n" \
  "ds_read_b64_tr_b16 a[2:3], v28 offset:3584\n" \
  "ds_read_b64_tr_b16 a[4:5], v28 offset:7168\n" \
  "ds_read_b64_tr_b16 a[6:7], v28 offset:10752\n" \
  "ds_read_b64_tr_b16 a[8:9], v28 offset:14336\n" \
  "ds_read_b64_tr_b16 a[10:11], v28 offset:17920\n" \
  "ds_read_b64_tr_b16 a[12:13], v28 offset:21504\n" \
  "ds_read_b64_tr_b16 a[14:15], v28 offset:25088\n" \
  "ds_read_b64_tr_b16 a[16:17], v28 offset:32\n" \
  "ds_read_b64_tr_b16 a[18:19], v28 offset:3616\n" \
  "ds_read_b64_tr_b16 a[20:21], v28 offset:7200\n" \
  "ds_read_b64_tr_b16 a[22:23], v28 offset:10784\n" \
  "v_mov_b32 v16, -1\n" \
  "v_mov_b32 v17, -1\n" \
  "v_mov_b32 v18, -1\n" \
  "v_mov_b32 v19, -1\n" \
  "s_waitcnt vmcnt(8) lgkmcnt(8)\n" \
  "v_mfma_f32_16x16x32_bf16 v[32:35], a[0:3], a[56:59], v[32:35]\n" \
  "ds_read_b64_tr_b16 a[24:25], v28 offset:14368\n" \
  "v_mfma_f32_16x16x32_bf16 v[32:35], a[4:7], a[72:75], v[32:35]\n" \
  "s_mov_b32 m0, s80\n" \
  "v_mfma_f32_16x16x32_bf16 v[36:39], a[4:7], a[76:79], v[36:39]\n" \
  "ds_read_b64_tr_b16 a[26:27], v28 offset:17952\n" \
  "v_mfma_f32_16x16x32_bf16 v[36:39], a[0:3], a[60:63], v[36:39]\n" \
  "buffer_load_dwordx4 v0, s[36:39], s81 offen offset:0 lds\n" \
  "v_mfma_f32_16x16x32_bf16 v[40:43], a[0:3], a[64:67], v[40:43]\n" \
  "ds_read_b64_tr_b16 a[28:29], v28 offset:21536\n" \
  "v_mfma_f32_16x16x32_bf16 v[40:43], a[4:7], a[80:83], v[40:43]\n" \
  "v_mfma_f32_16x16x32_bf16 v[44:47], a[4:7], a[84:87], v[44:47]\n" \
  "ds_read_b64_tr_b16 a[30:31], v28 offset:25120\n" \
  "v_mfma_f32_16x16x32_bf16 v[44:47], a[0:3], a[68:71], v[44:47]\n" \
  "s_waitcnt lgkmcnt(8)\n" \
  "v_mfma_f32_16x16x32_bf16 v[48:51], a[8:11], a[56:59], v[48:51]\n" \
  "ds_read_b64_tr_b16 a[32:33], v28 offset:64\n" \
  "v_mfma_f32_16x16x32_bf16 v[48:51], a[12:15], a[72:75], v[48:51]\n" \
  "v_mfma_f32_16x16x32_bf16 v[52:55], a[12:15], a[76:79], v[52:55]\n" \
  "ds_read_b64_tr_b16 a[34:35], v28 offset:3648\n" \
  "v_mfma_f32_16x16x32_bf16 v[52:55], a[8:11], a[60:63], v[52:55]\n" \
  "buffer_load_dwordx4 v1, s[36:39], s81 offen offset:1024 lds\n" \
  "v_mfma_f32_16x16x32_bf16 v[56:59], a[8:11], a[64:67], v[56:59]\n" \
  "ds_read_b64_tr_b16 a[36:37], v28 offset:7232\n" \
  "v_mfma_f32_16x16x32_bf16 v[56:59], a[12:15], a[80:83], v[56:59]\n" \
  "v_mfma_f32_16x16x32_bf16 v[60:63], a[12:15], a[84:87], v[60:63]\n" \
  "ds_read_b64_tr_b16 a[38:39], v28 offset:10816\n" \
  "v_mfma_f32_16x16x32_bf16 v[60:63], a[8:11], a[68:71], v[60:63]\n" \
  "s_waitcnt lgkmcnt(8)\n" \
  "v_mfma_f32_16x16x32_bf16 v[64:67], a[16:19], a[56:59], v[64:67]\n" \
  "ds_read_b64_tr_b16 a[40:41], v28 offset:14400\n" \
  "v_mfma_f32_16x16x32_bf16 v[64:67], a[20:23], a[72:75], v[64:67]\n" \
  "v_mfma_f32_16x16x32_bf16 v[68:71], a[20:23], a[76:79], v[68:71]\n" \
  "ds_read_b64_tr_b16 a[42:43], v28 offset:17984\n" \
  "v_mfma_f32_16x16x32_bf16 v[68:71], a[16:19], a[60:63], v[68:71]\n" \
  "buffer_load_dwordx4 v2, s[36:39], s81 offen offset:2048 lds\n" \
  "v_mfma_f32_16x16x32_bf16 v[72:75], a[16:19], a[64:67], v[72:75]\n" \
  "ds_read_b64_tr_b16 a[44:45], v28 offset:21568\n" \
  "v_mfma_f32_16x16x32_bf16 v[72:75], a[20:23], a[80:83], v[72:75]\n" \
  "v_mfma_f32_16x16x32_bf16 v[76:79], a[20:23], a[84:87], v[76:79]\n" \
  "ds_read_b64_tr_b16 a[46:47], v28 offset:25152\n" \
  "v_mfma_f32_16x16x32_bf16 v[76:79], a[16:19], a[68:71], v[76:79]\n" \
  "s_waitcnt lgkmcnt(8)\n" \
  "v_mfma_f32_16x16x32_bf16 v[80:83], a[24:27], a[56:59], v[80:83]\n" \
  "ds_read_b64_tr_b16 a[48:49], v28 offset:96\n" \
  "v_mfma_f32_16x16x32_bf16 v[80:83], a[28:31], a[72:75], v[80:83]\n" \
  "v_mfma_f32_16x16x32_bf16 v[84:87], a[28:31], a[76:79], v[84:87]\n" \
  "ds_read_b64_tr_b16 a[50:51], v28 offset:3680\n" \
  "v_mfma_f32_16x16x32_bf16 v[84:87], a[24:27], a[60:63], v[84:87]\n" \
  "buffer_load_dwordx4 v3, s[36:39], s81 offen offset:3072 lds\n" \
  "v_mfma_f32_16x16x32_bf16 v[88:91], a[24:27], a[64:67], v[88:91]\n" \
  "ds_read_b64_tr_b16 a[52:53], v28 offset:7264\n" \
  "v_mfma_f32_16x16x32_bf16 v[88:91], a[28:31], a[80:83], v[88:91]\n" \
  "v_mfma_f32_16x16x32_bf16 v[92:95], a[28:31], a[84:87], v[92:95]\n" \
  "ds_read_b64_tr_b16 a[54:55], v28 offset:10848\n" \
  "v_mfma_f32_16x16x32_bf16 v[92:95], a[24:27], a[68:71], v[92:95]\n" \
  "s_waitcnt lgkmcnt(8)\n" \
  "v_mfma_f32_16x16x32_bf16 v[96:99], a[32:35], a[56:59], v[96:99]\n" \
  "ds_read_b64_tr_b16 a[0:1], v28 offset:14432\n" \
  "v_mfma_f32_16x16x32_bf16 v[96:99], a[36:39], a[72:75], v[96:99]\n" \
  "s_add_u32 m0, s80, 4096\n" \
  "v_mfma_f32_16x16x32_bf16 v[100:103], a[36:39], a[76:79], v[100:103]\n" \
  "ds_read_b64_tr_b16 a[2:3], v28 offset:18016\n" \
  "v_mfma_f32_16x16x32_bf16 v[100:103], a[32:35], a[60:63], v[100:103]\n" \
  "buffer_load_dwordx4 v4, s[36:39], s81 offen offset:0 lds\n" \
  "v_mfma_f32_16x16x32_bf16 v[104:107], a[32:35], a[64:67], v[104:107]\n" \
  "ds_read_b64_tr_b16 a[4:5], v28 offset:21600\n" \
  "v_mfma_f32_16x16x32_bf16 v[104:107], a[36:39], a[80:83], v[104:107]\n" \
  "v_mfma_f32_16x16x32_bf16 v[108:111], a[36:39], a[84:87], v[108:111]\n" \
  "ds_read_b64_tr_b16 a[6:7], v28 offset:25184\n" \
  "v_mfma_f32_16x16x32_bf16 v[108:111], a[32:35], a[68:71], v[108:111]\n" \
  "s_waitcnt lgkmcnt(8)\n" \
  "v_mfma_f32_16x16x32_bf16 v[112:115], a[40:43], a[56:59], v[112:115]\n" \
  "ds_read_b64_tr_b16 a[8:9], v28 offset:128\n" \
  "v_mfma_f32_16x16x32_bf16 v[112:115], a[44:47], a[72:75], v[112:115]\n" \
  "v_mfma_f32_16x16x32_bf16 v[116:119], a[44:47], a[76:79], v[116:119]\n" \
  "ds_read_b64_tr_b16 a[10:11], v28 offset:3712\n" \
  "v_mfma_f32_16x16x32_bf16 v[116:119], a[40:43], a[60:63], v[116:119]\n" \
  "buffer_load_dwordx4 v5, s[36:39], s81 offen offset:1024 lds\n" \
  "v_mfma_f32_16x16x32_bf16 v[120:123], a[40:43], a[64:67], v[120:123]\n" \
  "ds_read_b64_tr_b16 a[12:13], v28 offset:7296\n" \
  "v_mfma_f32_16x16x32_bf16 v[120:123], a[44:47], a[80:83], v[120:123]\n" \
  "v_mfma_f32_16x16x32_bf16 v[124:127], a[44:47], a[84:87], v[124:127]\n" \
  "ds_read_b64_tr_b16 a[14:15], v28 offset:10880\n" \
  "v_mfma_f32_16x16x32_bf16 v[124:127], a[40:43], a[68:71], v[124:127]\n" \
  "s_waitcnt lgkmcnt(8)\n" \
  "v_mfma_f32_16x16x32_bf16 v[128:131], a[48:51], a[56:59], v[128:131]\n" \
  "ds_read_b64_tr_b16 a[16:17], v28 offset:14464\n" \
  "v_mfma_f32_16x16x32_bf16 v[128:131], a[52:55], a[72:75], v[128:131]\n" \
  "v_mfma_f32_16x16x32_bf16 v[132:135], a[52:55], a[76:79], v[132:135]\n" \
  "ds_read_b64_tr_b16 a[18:19], v28 offset:18048\n" \
  "v_mfma_f32_16x16x32_bf16 v[132:135], a[48:51], a[60:63], v[132:135]\n" \
  "buffer_load_dwordx4 v6, s[36:39], s81 offen offset:2048 lds\n" \
  "v_mfma_f32_16x16x32_bf16 v[136:139], a[48:51], a[64:67], v[136:139]\n" \
  "ds_read_b64_tr_b16 a[20:21], v28 offset:21632\n" \
  "v_mfma_f32_16x16x32_bf16 v[136:139], a[52:55], a[80:83], v[136:139]\n" \
  "buffer_load_dwordx4 a[120:123], v7, s[40:43], s8 offen offset:0 sc1\n" \
  "s_add_u32 s81, s81, 458752\n" \
  "v_mfma_f32_16x16x32_bf16 v[140:143], a[52:55], a[84:87], v[140:143]\n" \
  "ds_read_b64_tr_b16 a[22:23], v28 offset:25216\n" \
  "v_mfma_f32_16x16x32_bf16 v[140:143], a[48:51], a[68:71], v[140:143]\n" \
  "s_waitcnt lgkmcnt(8)\n" \
  "v_mfma_f32_16x16x32_bf16 v[144:147], a[0:3], a[56:59], v[144:147]\n" \
  "ds_read_b64_tr_b16 a[24:25], v28 offset:160\n" \
  "v_mfma_f32_16x16x32_bf16 v[144:147], a[4:7], a[72:75], v[144:147]\n" \
  "v_mfma_f32_16x16x32_bf16 v[148:151], a[4:7], a[76:79], v[148:151]\n" \
  "ds_read_b64_tr_b16 a[26:27], v28 offset:3744\n" \
  "v_mfma_f32_16x16x32_bf16 v[148:151], a[0:3], a[60:63], v[148:151]\n" \
  "buffer_load_dwordx4 a[124:127], v7, s[40:43], s8 offen offset:512 sc1\n" \
  "v_mfma_f32_16x16x32_bf16 v[152:155], a[0:3], a[64:67], v[152:155]\n" \
  "ds_read_b64_tr_b16 a[28:29], v28 offset:7328\n" \
  "v_mfma_f32_16x16x32_bf16 v[152:155], a[4:7], a[80:83], v[152:155]\n" \
  "v_mfma_f32_16x16x32_bf16 v[156:159], a[4:7], a[84:87], v[156:159]\n" \
  "ds_read_b64_tr_b16 a[30:31], v28 offset:10912\n" \
  "v_mfma_f32_16x16x32_bf16 v[156:159], a[0:3], a[68:71], v[156:159]\n" \
  "s_waitcnt lgkmcnt(8)\n" \
  "v_mfma_f32_16x16x32_bf16 v[160:163], a[8:11], a[56:59], v[160:163]\n" \
  "ds_read_b64_tr_b16 a[32:33], v28 offset:14496\n" \
  "v_mfma_f32_16x16x32_bf16 v[160:163], a[12:15], a[72:75], v[160:163]\n" \
  "s_add_u32 s80, s80, 28672\n" \
  "v_mfma_f32_16x16x32_bf16 v[164:167], a[12:15], a[76:79], v[164:167]\n" \
  "ds_read_b64_tr_b16 a[34:35], v28 offset:18080\n" \
  "v_mfma_f32_16x16x32_bf16 v[164:167], a[8:11], a[60:63], v[164:167]\n" \
  "buffer_load_dwordx4 a[128:131], v8, s[40:43], s8 offen offset:0 sc1\n" \
  "v_mfma_f32_16x16x32_bf16 v[168:171], a[8:11], a[64:67], v[168:171]\n" \
  "ds_read_b64_tr_b16 a[36:37], v28 offset:21664\n" \
  "s_cmp_ge_u32 s80, 114688\n" \
  "v_mfma_f32_16x16x32_bf16 v[168:171], a[12:15], a[80:83], v[168:171]\n" \
  "s_cselect_b32 s80, s82, s80\n" \
  "v_mfma_f32_16x16x32_bf16 v[172:175], a[12:15], a[84:87], v[172:175]\n" \
  "ds_read_b64_tr_b16 a[38:39], v28 offset:25248\n" \
  "v_mfma_f32_16x16x32_bf16 v[172:175], a[8:11], a[68:71], v[172:175]\n" \
  "s_waitcnt lgkmcnt(8)\n" \
  "v_mfma_f32_16x16x32_bf16 v[176:179], a[16:19], a[56:59], v[176:179]\n" \
  "ds_read_b64_tr_b16 a[40:41], v28 offset:192\n" \
  "v_mfma_f32_16x16x32_bf16 v[176:179], a[20:23], a[72:75], v[176:179]\n" \
  "s_add_u32 s9, s9, 28672\n" \
  "v_mfma_f32_16x16x32_bf16 v[180:183], a[20:23], a[76:79], v[180:183]\n" \
  "ds_read_b64_tr_b16 a[42:43], v28 offset:3776\n" \
  "v_mfma_f32_16x16x32_bf16 v[180:183], a[16:19], a[60:63], v[180:183]\n" \
  "buffer_load_dwordx4 a[132:135], v8, s[40:43], s8 offen offset:512 sc1\n" \
  "v_mfma_f32_16x16x32_bf16 v[184:187], a[16:19], a[64:67], v[184:187]\n" \
  "ds_read_b64_tr_b16 a[44:45], v28 offset:7360\n" \
  "s_cmp_eq_u32 s9, 114688\n" \
  "v_mfma_f32_16x16x32_bf16 v[184:187], a[20:23], a[80:83], v[184:187]\n" \
  "s_cselect_b32 s9, 0, s9\n" \
  "v_mfma_f32_16x16x32_bf16 v[188:191], a[20:23], a[84:87], v[188:191]\n" \
  "ds_read_b64_tr_b16 a[46:47], v28 offset:10944\n" \
  "v_mfma_f32_16x16x32_bf16 v[188:191], a[16:19], a[68:71], v[188:191]\n" \
  "s_waitcnt lgkmcnt(8)\n" \
  "v_mfma_f32_16x16x32_bf16 v[192:195], a[24:27], a[56:59], v[192:195]\n" \
  "ds_read_b64_tr_b16 a[48:49], v28 offset:14528\n" \
  "v_mfma_f32_16x16x32_bf16 v[192:195], a[28:31], a[72:75], v[192:195]\n" \
  "v_mfma_f32_16x16x32_bf16 v[196:199], a[28:31], a[76:79], v[196:199]\n" \
  "ds_read_b64_tr_b16 a[50:51], v28 offset:18112\n" \
  "v_mfma_f32_16x16x32_bf16 v[196:199], a[24:27], a[60:63], v[196:199]\n" \
  "buffer_load_dwordx4 a[136:139], v7, s[40:43], s8 offen offset:2048 sc1\n" \
  "v_mfma_f32_16x16x32_bf16 v[200:203], a[24:27], a[64:67], v[200:203]\n" \
  "ds_read_b64_tr_b16 a[52:53], v28 offset:21696\n" \
  "v_mfma_f32_16x16x32_bf16 v[200:203], a[28:31], a[80:83], v[200:203]\n" \
  "v_mfma_f32_16x16x32_bf16 v[204:207], a[28:31], a[84:87], v[204:207]\n" \
  "ds_read_b64_tr_b16 a[54:55], v28 offset:25280\n" \
  "v_add_u32 v28, s9, v9\n" \
  "v_mfma_f32_16x16x32_bf16 v[204:207], a[24:27], a[68:71], v[204:207]\n" \
  "s_waitcnt lgkmcnt(8)\n" \
  "s_barrier\n" \
  "v_mfma_f32_16x16x32_bf16 v[208:211], a[32:35], a[56:59], v[208:211]\n" \
  "ds_read_b64_tr_b16 a[0:1], v28 offset:0\n" \
  "v_mfma_f32_16x16x32_bf16 v[208:211], a[36:39], a[72:75], v[208:211]\n" \
  "v_mfma_f32_16x16x32_bf16 v[212:215], a[36:39], a[76:79], v[212:215]\n" \
  "ds_read_b64_tr_b16 a[2:3], v28 offset:3584\n" \
  "v_mfma_f32_16x16x32_bf16 v[212:215], a[32:35], a[60:63], v[212:215]\n" \
  "buffer_load_dwordx4 a[140:143], v7, s[40:43], s8 offen offset:2560 sc1\n" \
  "v_mfma_f32_16x16x32_bf16 v[216:219], a[32:35], a[64:67], v[216:219]\n" \
  "ds_read_b64_tr_b16 a[4:5], v28 offset:7168\n" \
  "v_mfma_f32_16x16x32_bf16 v[216:219], a[36:39], a[80:83], v[216:219]\n" \
  "v_mfma_f32_16x16x32_bf16 v[220:223], a[36:39], a[84:87], v[220:223]\n" \
  "ds_read_b64_tr_b16 a[6:7], v28 offset:10752\n" \
  "v_mfma_f32_16x16x32_bf16 v[220:223], a[32:35], a[68:71], v[220:223]\n" \
  "s_waitcnt lgkmcnt(8)\n" \
  "v_mfma_f32_16x16x32_bf16 v[224:227], a[40:43], a[56:59], v[224:227]\n" \
  "ds_read_b64_tr_b16 a[8:9], v28 offset:14336\n" \
  "v_mfma_f32_16x16x32_bf16 v[224:227], a[44:47], a[72:75], v[224:227]\n" \
  "v_mfma_f32_16x16x32_bf16 v[228:231], a[44:47], a[76:79], v[228:231]\n" \
  "ds_read_b64_tr_b16 a[10:11], v28 offset:17920\n" \
  "v_mfma_f32_16x16x32_bf16 v[228:231], a[40:43], a[60:63], v[228:231]\n" \
  "buffer_load_dwordx4 a[144:147], v8, s[40:43], s8 offen offset:2048 sc1\n" \
  "v_mfma_f32_16x16x32_bf16 v[232:235], a[40:43], a[64:67], v[232:235]\n" \
  "ds_read_b64_tr_b16 a[12:13], v28 offset:21504\n" \
  "v_mfma_f32_16x16x32_bf16 v[232:235], a[44:47], a[80:83], v[232:235]\n" \
  "v_mfma_f32_16x16x32_bf16 v[236:239], a[44:47], a[84:87], v[236:239]\n" \
  "ds_read_b64_tr_b16 a[14:15], v28 offset:25088\n" \
  "v_mfma_f32_16x16x32_bf16 v[236:239], a[40:43], a[68:71], v[236:239]\n" \
  "s_waitcnt lgkmcnt(8)\n" \
  "v_mfma_f32_16x16x32_bf16 v[240:243], a[48:51], a[56:59], v[240:243]\n" \
  "ds_read_b64_tr_b16 a[16:17], v28 offset:32\n" \
  "v_mfma_f32_16x16x32_bf16 v[240:243], a[52:55], a[72:75], v[240:243]\n" \
  "v_mfma_f32_16x16x32_bf16 v[244:247], a[52:55], a[76:79], v[244:247]\n" \
  "ds_read_b64_tr_b16 a[18:19], v28 offset:3616\n" \
  "v_mfma_f32_16x16x32_bf16 v[244:247], a[48:51], a[60:63], v[244:247]\n" \
  "buffer_load_dwordx4 a[148:151], v8, s[40:43], s8 offen offset:2560 sc1\n" \
  "v_mfma_f32_16x16x32_bf16 v[248:251], a[48:51], a[64:67], v[248:251]\n" \
  "ds_read_b64_tr_b16 a[20:21], v28 offset:7200\n" \
  "v_mfma_f32_16x16x32_bf16 v[248:251], a[52:55], a[80:83], v[248:251]\n" \
  "v_mfma_f32_16x16x32_bf16 v[252:255], a[52:55], a[84:87], v[252:255]\n" \
  "ds_read_b64_tr_b16 a[22:23], v28 offset:10784\n" \
  "s_add_u32 s8, s8, 4096\n" \
  "v_mfma_f32_16x16x32_bf16 v[252:255], a[48:51], a[68:71], v[252:255]\n" \
  "s_waitcnt vmcnt(15) lgkmcnt(8)\n" \
  "v_mfma_f32_16x16x32_bf16 v[32:35], a[0:3], a[88:91], v[32:35]\n" \
  "ds_read_b64_tr_b16 a[24:25], v28 offset:14368\n" \
  "v_mfma_f32_16x16x32_bf16 v[32:35], a[4:7], a[104:107], v[32:35]\n" \
  "s_mov_b32 m0, s80\n" \
  "v_mfma_f32_16x16x32_bf16 v[36:39], a[4:7], a[108:111], v[36:39]\n" \
  "ds_read_b64_tr_b16 a[26:27], v28 offset:17952\n" \
  "v_mfma_f32_16x16x32_bf16 v[36:39], a[0:3], a[92:95], v[36:39]\n" \
  "buffer_load_dwordx4 v0, s[36:39], s81 offen offset:0 lds\n" \
  "v_mfma_f32_16x16x32_bf16 v[40:43], a[0:3], a[96:99], v[40:43]\n" \
  "ds_read_b64_tr_b16 a[28:29], v28 offset:21536\n" \
  "v_mfma_f32_16x16x32_bf16 v[40:43], a[4:7], a[112:115], v[40:43]\n" \
  "v_mfma_f32_16x16x32_bf16 v[44:47], a[4:7], a[116:119], v[44:47]\n" \
  "ds_read_b64_tr_b16 a[30:31], v28 offset:25120\n" \
  "v_mfma_f32_16x16x32_bf16 v[44:47], a[0:3], a[100:103], v[44:47]\n" \
  "s_waitcnt lgkmcnt(8)\n" \
  "v_mfma_f32_16x16x32_bf16 v[48:51], a[8:11], a[88:91], v[48:51]\n" \
  "ds_read_b64_tr_b16 a[32:33], v28 offset:64\n" \
  "v_mfma_f32_16x16x32_bf16 v[48:51], a[12:15], a[104:107], v[48:51]\n" \
  "v_mfma_f32_16x16x32_bf16 v[52:55], a[12:15], a[108:111], v[52:55]\n" \
  "ds_read_b64_tr_b16 a[34:35], v28 offset:3648\n" \
  "v_mfma_f32_16x16x32_bf16 v[52:55], a[8:11], a[92:95], v[52:55]\n" \
  "buffer_load_dwordx4 v1, s[36:39], s81 offen offset:1024 lds\n" \
  "v_mfma_f32_16x16x32_bf16 v[56:59], a[8:11], a[96:99], v[56:59]\n" \
  "ds_read_b64_tr_b16 a[36:37], v28 offset:7232\n" \
  "v_mfma_f32_16x16x32_bf16 v[56:59], a[12:15], a[112:115], v[56:59]\n" \
  "v_mfma_f32_16x16x32_bf16 v[60:63], a[12:15], a[116:119], v[60:63]\n" \
  "ds_read_b64_tr_b16 a[38:39], v28 offset:10816\n" \
  "v_mfma_f32_16x16x32_bf16 v[60:63], a[8:11], a[100:103], v[60:63]\n" \
  "s_waitcnt lgkmcnt(8)\n" \
  "v_mfma_f32_16x16x32_bf16 v[64:67], a[16:19], a[88:91], v[64:67]\n" \
  "ds_read_b64_tr_b16 a[40:41], v28 offset:14400\n" \
  "v_mfma_f32_16x16x32_bf16 v[64:67], a[20:23], a[104:107], v[64:67]\n" \
  "v_mfma_f32_16x16x32_bf16 v[68:71], a[20:23], a[108:111], v[68:71]\n" \
  "ds_read_b64_tr_b16 a[42:43], v28 offset:17984\n" \
  "v_mfma_f32_16x16x32_bf16 v[68:71], a[16:19], a[92:95], v[68:71]\n" \
  "buffer_load_dwordx4 v2, s[36:39], s81 offen offset:2048 lds\n" \
  "v_mfma_f32_16x16x32_bf16 v[72:75], a[16:19], a[96:99], v[72:75]\n" \
  "ds_read_b64_tr_b16 a[44:45], v28 offset:21568\n" \
  "v_mfma_f32_16x16x32_bf16 v[72:75], a[20:23], a[112:115], v[72:75]\n" \
  "v_mfma_f32_16x16x32_bf16 v[76:79], a[20:23], a[116:119], v[76:79]\n" \
  "ds_read_b64_tr_b16 a[46:47], v28 offset:25152\n" \
  "v_mfma_f32_16x16x32_bf16 v[76:79], a[16:19], a[100:103], v[76:79]\n" \
  "s_waitcnt lgkmcnt(8)\n" \
  "v_mfma_f32_16x16x32_bf16 v[80:83], a[24:27], a[88:91], v[80:83]\n" \
  "ds_read_b64_tr_b16 a[48:49], v28 offset:96\n" \
  "v_mfma_f32_16x16x32_bf16 v[80:83], a[28:31], a[104:107], v[80:83]\n" \
  "v_mfma_f32_16x16x32_bf16 v[84:87], a[28:31], a[108:111], v[84:87]\n" \
  "ds_read_b64_tr_b16 a[50:51], v28 offset:3680\n" \
  "v_mfma_f32_16x16x32_bf16 v[84:87], a[24:27], a[92:95], v[84:87]\n" \
  "buffer_load_dwordx4 v3, s[36:39], s81 offen offset:3072 lds\n" \
  "v_mfma_f32_16x16x32_bf16 v[88:91], a[24:27], a[96:99], v[88:91]\n" \
  "ds_read_b64_tr_b16 a[52:53], v28 offset:7264\n" \
  "v_mfma_f32_16x16x32_bf16 v[88:91], a[28:31], a[112:115], v[88:91]\n" \
  "v_mfma_f32_16x16x32_bf16 v[92:95], a[28:31], a[116:119], v[92:95]\n" \
  "ds_read_b64_tr_b16 a[54:55], v28 offset:10848\n" \
  "v_mfma_f32_16x16x32_bf16 v[92:95], a[24:27], a[100:103], v[92:95]\n" \
  "s_waitcnt lgkmcnt(8)\n" \
  "v_mfma_f32_16x16x32_bf16 v[96:99], a[32:35], a[88:91], v[96:99]\n" \
  "ds_read_b64_tr_b16 a[0:1], v28 offset:14432\n" \
  "v_mfma_f32_16x16x32_bf16 v[96:99], a[36:39], a[104:107], v[96:99]\n" \
  "s_add_u32 m0, s80, 4096\n" \
  "v_mfma_f32_16x16x32_bf16 v[100:103], a[36:39], a[108:111], v[100:103]\n" \
  "ds_read_b64_tr_b16 a[2:3], v28 offset:18016\n" \
  "v_mfma_f32_16x16x32_bf16 v[100:103], a[32:35], a[92:95], v[100:103]\n" \
  "buffer_load_dwordx4 v4, s[36:39], s81 offen offset:0 lds\n" \
  "v_mfma_f32_16x16x32_bf16 v[104:107], a[32:35], a[96:99], v[104:107]\n" \
  "ds_read_b64_tr_b16 a[4:5], v28 offset:21600\n" \
  "v_mfma_f32_16x16x32_bf16 v[104:107], a[36:39], a[112:115], v[104:107]\n" \
  "v_mfma_f32_16x16x32_bf16 v[108:111], a[36:39], a[116:119], v[108:111]\n" \
  "ds_read_b64_tr_b16 a[6:7], v28 offset:25184\n" \
  "v_mfma_f32_16x16x32_bf16 v[108:111], a[32:35], a[100:103], v[108:111]\n" \
  "s_waitcnt lgkmcnt(8)\n" \
  "v_mfma_f32_16x16x32_bf16 v[112:115], a[40:43], a[88:91], v[112:115]\n" \
  "ds_read_b64_tr_b16 a[8:9], v28 offset:128\n" \
  "v_mfma_f32_16x16x32_bf16 v[112:115], a[44:47], a[104:107], v[112:115]\n" \
  "v_mfma_f32_16x16x32_bf16 v[116:119], a[44:47], a[108:111], v[116:119]\n" \
  "ds_read_b64_tr_b16 a[10:11], v28 offset:3712\n" \
  "v_mfma_f32_16x16x32_bf16 v[116:119], a[40:43], a[92:95], v[116:119]\n" \
  "buffer_load_dwordx4 v5, s[36:39], s81 offen offset:1024 lds\n" \
  "v_mfma_f32_16x16x32_bf16 v[120:123], a[40:43], a[96:99], v[120:123]\n" \
  "ds_read_b64_tr_b16 a[12:13], v28 offset:7296\n" \
  "v_mfma_f32_16x16x32_bf16 v[120:123], a[44:47], a[112:115], v[120:123]\n" \
  "v_mfma_f32_16x16x32_bf16 v[124:127], a[44:47], a[116:119], v[124:127]\n" \
  "ds_read_b64_tr_b16 a[14:15], v28 offset:10880\n" \
  "v_mfma_f32_16x16x32_bf16 v[124:127], a[40:43], a[100:103], v[124:127]\n" \
  "s_waitcnt lgkmcnt(8)\n" \
  "v_mfma_f32_16x16x32_bf16 v[128:131], a[48:51], a[88:91], v[128:131]\n" \
  "ds_read_b64_tr_b16 a[16:17], v28 offset:14464\n" \
  "v_mfma_f32_16x16x32_bf16 v[128:131], a[52:55], a[104:107], v[128:131]\n" \
  "v_mfma_f32_16x16x32_bf16 v[132:135], a[52:55], a[108:111], v[132:135]\n" \
  "ds_read_b64_tr_b16 a[18:19], v28 offset:18048\n" \
  "v_mfma_f32_16x16x32_bf16 v[132:135], a[48:51], a[92:95], v[132:135]\n" \
  "buffer_load_dwordx4 v6, s[36:39], s81 offen offset:2048 lds\n" \
  "v_mfma_f32_16x16x32_bf16 v[136:139], a[48:51], a[96:99], v[136:139]\n" \
  "ds_read_b64_tr_b16 a[20:21], v28 offset:21632\n" \
  "v_mfma_f32_16x16x32_bf16 v[136:139], a[52:55], a[112:115], v[136:139]\n" \
  "buffer_load_dwordx4 a[56:59], v7, s[40:43], s8 offen offset:0 sc1\n" \
  "s_add_u32 s81, s81, 458752\n" \
  "v_mfma_f32_16x16x32_bf16 v[140:143], a[52:55], a[116:119], v[140:143]\n" \
  "ds_read_b64_tr_b16 a[22:23], v28 offset:25216\n" \
  "v_mfma_f32_16x16x32_bf16 v[140:143], a[48:51], a[100:103], v[140:143]\n" \
  "s_waitcnt lgkmcnt(8)\n" \
  "v_mfma_f32_16x16x32_bf16 v[144:147], a[0:3], a[88:91], v[144:147]\n" \
  "ds_read_b64_tr_b16 a[24:25], v28 offset:160\n" \
  "v_mfma_f32_16x16x32_bf16 v[144:147], a[4:7], a[104:107], v[144:147]\n" \
  "v_mfma_f32_16x16x32_bf16 v[148:151], a[4:7], a[108:111], v[148:151]\n" \
  "ds_read_b64_tr_b16 a[26:27], v28 offset:3744\n" \
  "v_mfma_f32_16x16x32_bf16 v[148:151], a[0:3], a[92:95], v[148:151]\n" \
  "buffer_load_dwordx4 a[60:63], v7, s[40:43], s8 offen offset:512 sc1\n" \
  "v_mfma_f32_16x16x32_bf16 v[152:155], a[0:3], a[96:99], v[152:155]\n" \
  "ds_read_b64_tr_b16 a[28:29], v28 offset:7328\n" \
  "v_mfma_f32_16x16x32_bf16 v[152:155], a[4:7], a[112:115], v[152:155]\n" \
  "v_mfma_f32_16x16x32_bf16 v[156:159], a[4:7], a[116:119], v[156:159]\n" \
  "ds_read_b64_tr_b16 a[30:31], v28 offset:10912\n" \
  "v_mfma_f32_16x16x32_bf16 v[156:159], a[0:3], a[100:103], v[156:159]\n" \
  "s_waitcnt lgkmcnt(8)\n" \
  "v_mfma_f32_16x16x32_bf16 v[160:163], a[8:11], a[88:91], v[160:163]\n" \
  "ds_read_b64_tr_b16 a[32:33], v28 offset:14496\n" \
  "v_mfma_f32_16x16x32_bf16 v[160:163], a[12:15], a[104:107], v[160:163]\n" \
  "s_add_u32 s80, s80, 28672\n" \
  "v_mfma_f32_16x16x32_bf16 v[164:167], a[12:15], a[108:111], v[164:167]\n" \
  "ds_read_b64_tr_b16 a[34:35], v28 offset:18080\n" \
  "v_mfma_f32_16x16x32_bf16 v[164:167], a[8:11], a[92:95], v[164:167]\n" \
  "buffer_load_dwordx4 a[64:67], v8, s[40:43], s8 offen offset:0 sc1\n" \
  "v_mfma_f32_16x16x32_bf16 v[168:171], a[8:11], a[96:99], v[168:171]\n" \
  "ds_read_b64_tr_b16 a[36:37], v28 offset:21664\n" \
  "s_cmp_ge_u32 s80, 114688\n" \
  "v_mfma_f32_16x16x32_bf16 v[168:171], a[12:15], a[112:115], v[168:171]\n" \
  "s_cselect_b32 s80, s82, s80\n" \
  "v_mfma_f32_16x16x32_bf16 v[172:175], a[12:15], a[116:119], v[172:175]\n" \
  "ds_read_b64_tr_b16 a[38:39], v28 offset:25248\n" \
  "v_mfma_f32_16x16x32_bf16 v[172:175], a[8:11], a[100:103], v[172:175]\n" \
  "s_waitcnt lgkmcnt(8)\n" \
  "v_mfma_f32_16x16x32_bf16 v[176:179], a[16:19], a[88:91], v[176:179]\n" \
  "ds_read_b64_tr_b16 a[40:41], v28 offset:192\n" \
  "v_mfma_f32_16x16x32_bf16 v[176:179], a[20:23], a[104:107], v[176:179]\n" \
  "s_add_u32 s9, s9, 28672\n" \
  "v_mfma_f32_16x16x32_bf16 v[180:183], a[20:23], a[108:111], v[180:183]\n" \
  "ds_read_b64_tr_b16 a[42:43], v28 offset:3776\n" \
  "v_mfma_f32_16x16x32_bf16 v[180:183], a[16:19], a[92:95], v[180:183]\n" \
  "buffer_load_dwordx4 a[68:71], v8, s[40:43], s8 offen offset:512 sc1\n" \
  "v_mfma_f32_16x16x32_bf16 v[184:187], a[16:19], a[96:99], v[184:187]\n" \
  "ds_read_b64_tr_b16 a[44:45], v28 offset:7360\n" \
  "s_cmp_eq_u32 s9, 114688\n" \
  "v_mfma_f32_16x16x32_bf16 v[184:187], a[20:23], a[112:115], v[184:187]\n" \
  "s_cselect_b32 s9, 0, s9\n" \
  "v_mfma_f32_16x16x32_bf16 v[188:191], a[20:23], a[116:119], v[188:191]\n" \
  "ds_read_b64_tr_b16 a[46:47], v28 offset:10944\n" \
  "v_mfma_f32_16x16x32_bf16 v[188:191], a[16:19], a[100:103], v[188:191]\n" \
  "s_waitcnt lgkmcnt(8)\n" \
  "v_mfma_f32_16x16x32_bf16 v[192:195], a[24:27], a[88:91], v[192:195]\n" \
  "ds_read_b64_tr_b16 a[48:49], v28 offset:14528\n" \
  "v_mfma_f32_16x16x32_bf16 v[192:195], a[28:31], a[104:107], v[192:195]\n" \
  "v_mfma_f32_16x16x32_bf16 v[196:199], a[28:31], a[108:111], v[196:199]\n" \
  "ds_read_b64_tr_b16 a[50:51], v28 offset:18112\n" \
  "v_mfma_f32_16x16x32_bf16 v[196:199], a[24:27], a[92:95], v[196:199]\n" \
  "buffer_load_dwordx4 a[72:75], v7, s[40:43], s8 offen offset:2048 sc1\n" \
  "v_mfma_f32_16x16x32_bf16 v[200:203], a[24:27], a[96:99], v[200:203]\n" \
  "ds_read_b64_tr_b16 a[52:53], v28 offset:21696\n" \
  "v_mfma_f32_16x16x32_bf16 v[200:203], a[28:31], a[112:115], v[200:203]\n" \
  "v_mfma_f32_16x16x32_bf16 v[204:207], a[28:31], a[116:119], v[204:207]\n" \
  "ds_read_b64_tr_b16 a[54:55], v28 offset:25280\n" \
  "v_add_u32 v28, s9, v9\n" \
  "v_mfma_f32_16x16x32_bf16 v[204:207], a[24:27], a[100:103], v[204:207]\n" \
  "s_waitcnt vmcnt(20) lgkmcnt(8)\n" \
  "s_barrier\n" \
  "v_mfma_f32_16x16x32_bf16 v[208:211], a[32:35], a[88:91], v[208:211]\n" \
  "ds_read_b64_tr_b16 a[0:1], v28 offset:0\n" \
  "v_mfma_f32_16x16x32_bf16 v[208:211], a[36:39], a[104:107], v[208:211]\n" \
  "v_mfma_f32_16x16x32_bf16 v[212:215], a[36:39], a[108:111], v[212:215]\n" \
  "ds_read_b64_tr_b16 a[2:3], v28 offset:3584\n" \
  "v_mfma_f32_16x16x32_bf16 v[212:215], a[32:35], a[92:95], v[212:215]\n" \
  "buffer_load_dwordx4 a[76:79], v7, s[40:43], s8 offen offset:2560 sc1\n" \
  "v_mfma_f32_16x16x32_bf16 v[216:219], a[32:35], a[96:99], v[216:219]\n" \
  "ds_read_b64_tr_b16 a[4:5], v28 offset:7168\n" \
  "v_mfma_f32_16x16x32_bf16 v[216:219], a[36:39], a[112:115], v[216:219]\n" \
  "v_mfma_f32_16x16x32_bf16 v[220:223], a[36:39], a[116:119], v[220:223]\n" \
  "ds_read_b64_tr_b16 a[6:7], v28 offset:10752\n" \
  "v_mfma_f32_16x16x32_bf16 v[220:223], a[32:35], a[100:103], v[220:223]\n" \
  "s_waitcnt lgkmcnt(8)\n" \
  "v_mfma_f32_16x16x32_bf16 v[224:227], a[40:43], a[88:91], v[224:227]\n" \
  "ds_read_b64_tr_b16 a[8:9], v28 offset:14336\n" \
  "v_mfma_f32_16x16x32_bf16 v[224:227], a[44:47], a[104:107], v[224:227]\n" \
  "v_mfma_f32_16x16x32_bf16 v[228:231], a[44:47], a[108:111], v[228:231]\n" \
  "ds_read_b64_tr_b16 a[10:11], v28 offset:17920\n" \
  "v_mfma_f32_16x16x32_bf16 v[228:231], a[40:43], a[92:95], v[228:231]\n" \
  "buffer_load_dwordx4 a[80:83], v8, s[40:43], s8 offen offset:2048 sc1\n" \
  "v_mfma_f32_16x16x32_bf16 v[232:235], a[40:43], a[96:99], v[232:235]\n" \
  "ds_read_b64_tr_b16 a[12:13], v28 offset:21504\n" \
  "v_mfma_f32_16x16x32_bf16 v[232:235], a[44:47], a[112:115], v[232:235]\n" \
  "v_mfma_f32_16x16x32_bf16 v[236:239], a[44:47], a[116:119], v[236:239]\n" \
  "ds_read_b64_tr_b16 a[14:15], v28 offset:25088\n" \
  "v_mfma_f32_16x16x32_bf16 v[236:239], a[40:43], a[100:103], v[236:239]\n" \
  "s_waitcnt lgkmcnt(8)\n" \
  "v_mfma_f32_16x16x32_bf16 v[240:243], a[48:51], a[88:91], v[240:243]\n" \
  "ds_read_b64_tr_b16 a[16:17], v28 offset:32\n" \
  "v_mfma_f32_16x16x32_bf16 v[240:243], a[52:55], a[104:107], v[240:243]\n" \
  "v_mfma_f32_16x16x32_bf16 v[244:247], a[52:55], a[108:111], v[244:247]\n" \
  "ds_read_b64_tr_b16 a[18:19], v28 offset:3616\n" \
  "v_mfma_f32_16x16x32_bf16 v[244:247], a[48:51], a[92:95], v[244:247]\n" \
  "buffer_load_dwordx4 a[84:87], v8, s[40:43], s8 offen offset:2560 sc1\n" \
  "v_mfma_f32_16x16x32_bf16 v[248:251], a[48:51], a[96:99], v[248:251]\n" \
  "ds_read_b64_tr_b16 a[20:21], v28 offset:7200\n" \
  "v_mfma_f32_16x16x32_bf16 v[248:251], a[52:55], a[112:115], v[248:251]\n" \
  "v_mfma_f32_16x16x32_bf16 v[252:255], a[52:55], a[116:119], v[252:255]\n" \
  "ds_read_b64_tr_b16 a[22:23], v28 offset:10784\n" \
  "s_add_u32 s8, s8, 4096\n" \
  "v_mfma_f32_16x16x32_bf16 v[252:255], a[48:51], a[100:103], v[252:255]\n" \
  "s_mov_b32 s10, 4\n" \
  "1:\n" \
  "s_waitcnt vmcnt(15) lgkmcnt(8)\n" \
  "v_mfma_f32_16x16x32_bf16 v[32:35], a[0:3], a[120:123], v[32:35]\n" \
  "ds_read_b64_tr_b16 a[24:25], v28 offset:14368\n" \
  "v_mfma_f32_16x16x32_bf16 v[32:35], a[4:7], a[136:139], v[32:35]\n" \
  "s_mov_b32 m0, s80\n" \
  "v_mfma_f32_16x16x32_bf16 v[36:39], a[4:7], a[140:143], v[36:39]\n" \
  "ds_read_b64_tr_b16 a[26:27], v28 offset:17952\n" \
  "v_mfma_f32_16x16x32_bf16 v[36:39], a[0:3], a[124:127], v[36:39]\n" \
  "buffer_load_dwordx4 v0, s[36:39], s81 offen offset:0 lds\n" \
  "v_mfma_f32_16x16x32_bf16 v[40:43], a[0:3], a[128:131], v[40:43]\n" \
  "ds_read_b64_tr_b16 a[28:29], v28 offset:21536\n" \
  "v_mfma_f32_16x16x32_bf16 v[40:43], a[4:7], a[144:147], v[40:43]\n" \
  "v_mfma_f32_16x16x32_bf16 v[44:47], a[4:7], a[148:151], v[44:47]\n" \
  "ds_read_b64_tr_b16 a[30:31], v28 offset:25120\n" \
  "v_mfma_f32_16x16x32_bf16 v[44:47], a[0:3], a[132:135], v[44:47]\n" \
  "s_waitcnt lgkmcnt(8)\n" \
  "v_mfma_f32_16x16x32_bf16 v[48:51], a[8:11], a[120:123], v[48:51]\n" \
  "ds_read_b64_tr_b16 a[32:33], v28 offset:64\n" \
  "v_mfma_f32_16x16x32_bf16 v[48:51], a[12:15], a[136:139], v[48:51]\n" \
  "v_mfma_f32_16x16x32_bf16 v[52:55], a[12:15], a[140:143], v[52:55]\n" \
  "ds_read_b64_tr_b16 a[34:35], v28 offset:3648\n" \
  "v_mfma_f32_16x16x32_bf16 v[52:55], a[8:11], a[124:127], v[52:55]\n" \
  "buffer_load_dwordx4 v1, s[36:39], s81 offen offset:1024 lds\n" \
  "v_mfma_f32_16x16x32_bf16 v[56:59], a[8:11], a[128:131], v[56:59]\n" \
  "ds_read_b64_tr_b16 a[36:37], v28 offset:7232\n" \
  "v_mfma_f32_16x16x32_bf16 v[56:59], a[12:15], a[144:147], v[56:59]\n" \
  "v_mfma_f32_16x16x32_bf16 v[60:63], a[12:15], a[148:151], v[60:63]\n" \
  "ds_read_b64_tr_b16 a[38:39], v28 offset:10816\n" \
  "v_mfma_f32_16x16x32_bf16 v[60:63], a[8:11], a[132:135], v[60:63]\n" \
  "s_waitcnt lgkmcnt(8)\n" \
  "v_mfma_f32_16x16x32_bf16 v[64:67], a[16:19], a[120:123], v[64:67]\n" \
  "ds_read_b64_tr_b16 a[40:41], v28 offset:14400\n" \
  "v_mfma_f32_16x16x32_bf16 v[64:67], a[20:23], a[136:139], v[64:67]\n" \
  "v_mfma_f32_16x16x32_bf16 v[68:71], a[20:23], a[140:143], v[68:71]\n" \
  "ds_read_b64_tr_b16 a[42:43], v28 offset:17984\n" \
  "v_mfma_f32_16x16x32_bf16 v[68:71], a[16:19], a[124:127], v[68:71]\n" \
  "buffer_load_dwordx4 v2, s[36:39], s81 offen offset:2048 lds\n" \
  "v_mfma_f32_16x16x32_bf16 v[72:75], a[16:19], a[128:131], v[72:75]\n" \
  "ds_read_b64_tr_b16 a[44:45], v28 offset:21568\n" \
  "v_mfma_f32_16x16x32_bf16 v[72:75], a[20:23], a[144:147], v[72:75]\n" \
  "v_mfma_f32_16x16x32_bf16 v[76:79], a[20:23], a[148:151], v[76:79]\n" \
  "ds_read_b64_tr_b16 a[46:47], v28 offset:25152\n" \
  "v_mfma_f32_16x16x32_bf16 v[76:79], a[16:19], a[132:135], v[76:79]\n" \
  "s_waitcnt lgkmcnt(8)\n" \
  "v_mfma_f32_16x16x32_bf16 v[80:83], a[24:27], a[120:123], v[80:83]\n" \
  "ds_read_b64_tr_b16 a[48:49], v28 offset:96\n" \
  "v_mfma_f32_16x16x32_bf16 v[80:83], a[28:31], a[136:139], v[80:83]\n" \
  "v_mfma_f32_16x16x32_bf16 v[84:87], a[28:31], a[140:143], v[84:87]\n" \
  "ds_read_b64_tr_b16 a[50:51], v28 offset:3680\n" \
  "v_mfma_f32_16x16x32_bf16 v[84:87], a[24:27], a[124:127], v[84:87]\n" \
  "buffer_load_dwordx4 v3, s[36:39], s81 offen offset:3072 lds\n" \
  "v_mfma_f32_16x16x32_bf16 v[88:91], a[24:27], a[128:131], v[88:91]\n" \
  "ds_read_b64_tr_b16 a[52:53], v28 offset:7264\n" \
  "v_mfma_f32_16x16x32_bf16 v[88:91], a[28:31], a[144:147], v[88:91]\n" \
  "v_mfma_f32_16x16x32_bf16 v[92:95], a[28:31], a[148:151], v[92:95]\n" \
  "ds_read_b64_tr_b16 a[54:55], v28 offset:10848\n" \
  "v_mfma_f32_16x16x32_bf16 v[92:95], a[24:27], a[132:135], v[92:95]\n" \
  "s_waitcnt lgkmcnt(8)\n" \
  "v_mfma_f32_16x16x32_bf16 v[96:99], a[32:35], a[120:123], v[96:99]\n" \
  "ds_read_b64_tr_b16 a[0:1], v28 offset:14432\n" \
  "v_mfma_f32_16x16x32_bf16 v[96:99], a[36:39], a[136:139], v[96:99]\n" \
  "s_add_u32 m0, s80, 4096\n" \
  "v_mfma_f32_16x16x32_bf16 v[100:103], a[36:39], a[140:143], v[100:103]\n" \
  "ds_read_b64_tr_b16 a[2:3], v28 offset:18016\n" \
  "v_mfma_f32_16x16x32_bf16 v[100:103], a[32:35], a[124:127], v[100:103]\n" \
  "buffer_load_dwordx4 v4, s[36:39], s81 offen offset:0 lds\n" \
  "v_mfma_f32_16x16x32_bf16 v[104:107], a[32:35], a[128:131], v[104:107]\n" \
  "ds_read_b64_tr_b16 a[4:5], v28 offset:21600\n" \
  "v_mfma_f32_16x16x32_bf16 v[104:107], a[36:39], a[144:147], v[104:107]\n" \
  "v_mfma_f32_16x16x32_bf16 v[108:111], a[36:39], a[148:151], v[108:111]\n" \
  "ds_read_b64_tr_b16 a[6:7], v28 offset:25184\n" \
  "v_mfma_f32_16x16x32_bf16 v[108:111], a[32:35], a[132:135], v[108:111]\n" \
  "s_waitcnt lgkmcnt(8)\n" \
  "v_mfma_f32_16x16x32_bf16 v[112:115], a[40:43], a[120:123], v[112:115]\n" \
  "ds_read_b64_tr_b16 a[8:9], v28 offset:128\n" \
  "v_mfma_f32_16x16x32_bf16 v[112:115], a[44:47], a[136:139], v[112:115]\n" \
  "v_mfma_f32_16x16x32_bf16 v[116:119], a[44:47], a[140:143], v[116:119]\n" \
  "ds_read_b64_tr_b16 a[10:11], v28 offset:3712\n" \
  "v_mfma_f32_16x16x32_bf16 v[116:119], a[40:43], a[124:127], v[116:119]\n" \
  "buffer_load_dwordx4 v5, s[36:39], s81 offen offset:1024 lds\n" \
  "v_mfma_f32_16x16x32_bf16 v[120:123], a[40:43], a[128:131], v[120:123]\n" \
  "ds_read_b64_tr_b16 a[12:13], v28 offset:7296\n" \
  "v_mfma_f32_16x16x32_bf16 v[120:123], a[44:47], a[144:147], v[120:123]\n" \
  "v_mfma_f32_16x16x32_bf16 v[124:127], a[44:47], a[148:151], v[124:127]\n" \
  "ds_read_b64_tr_b16 a[14:15], v28 offset:10880\n" \
  "v_mfma_f32_16x16x32_bf16 v[124:127], a[40:43], a[132:135], v[124:127]\n" \
  "s_waitcnt lgkmcnt(8)\n" \
  "v_mfma_f32_16x16x32_bf16 v[128:131], a[48:51], a[120:123], v[128:131]\n" \
  "ds_read_b64_tr_b16 a[16:17], v28 offset:14464\n" \
  "v_mfma_f32_16x16x32_bf16 v[128:131], a[52:55], a[136:139], v[128:131]\n" \
  "v_mfma_f32_16x16x32_bf16 v[132:135], a[52:55], a[140:143], v[132:135]\n" \
  "ds_read_b64_tr_b16 a[18:19], v28 offset:18048\n" \
  "v_mfma_f32_16x16x32_bf16 v[132:135], a[48:51], a[124:127], v[132:135]\n" \
  "buffer_load_dwordx4 v6, s[36:39], s81 offen offset:2048 lds\n" \
  "v_mfma_f32_16x16x32_bf16 v[136:139], a[48:51], a[128:131], v[136:139]\n" \
  "ds_read_b64_tr_b16 a[20:21], v28 offset:21632\n" \
  "v_mfma_f32_16x16x32_bf16 v[136:139], a[52:55], a[144:147], v[136:139]\n" \
  "buffer_load_dwordx4 a[88:91], v7, s[40:43], s8 offen offset:0 sc1\n" \
  "s_add_u32 s81, s81, 458752\n" \
  "v_mfma_f32_16x16x32_bf16 v[140:143], a[52:55], a[148:151], v[140:143]\n" \
  "ds_read_b64_tr_b16 a[22:23], v28 offset:25216\n" \
  "v_mfma_f32_16x16x32_bf16 v[140:143], a[48:51], a[132:135], v[140:143]\n" \
  "s_waitcnt lgkmcnt(8)\n" \
  "v_mfma_f32_16x16x32_bf16 v[144:147], a[0:3], a[120:123], v[144:147]\n" \
  "ds_read_b64_tr_b16 a[24:25], v28 offset:160\n" \
  "v_mfma_f32_16x16x32_bf16 v[144:147], a[4:7], a[136:139], v[144:147]\n" \
  "v_mfma_f32_16x16x32_bf16 v[148:151], a[4:7], a[140:143], v[148:151]\n" \
  "ds_read_b64_tr_b16 a[26:27], v28 offset:3744\n" \
  "v_mfma_f32_16x16x32_bf16 v[148:151], a[0:3], a[124:127], v[148:151]\n" \
  "buffer_load_dwordx4 a[92:95], v7, s[40:43], s8 offen offset:512 sc1\n" \
  "v_mfma_f32_16x16x32_bf16 v[152:155], a[0:3], a[128:131], v[152:155]\n" \
  "ds_read_b64_tr_b16 a[28:29], v28 offset:7328\n" \
  "v_mfma_f32_16x16x32_bf16 v[152:155], a[4:7], a[144:147], v[152:155]\n" \
  "buffer_store_dwordx4 v[16:19], v12, s[48:51], s11 offen sc1\n" \
  "v_mfma_f32_16x16x32_bf16 v[156:159], a[4:7], a[148:151], v[156:159]\n" \
  "ds_read_b64_tr_b16 a[30:31], v28 offset:10912\n" \
  "v_mfma_f32_16x16x32_bf16 v[156:159], a[0:3], a[132:135], v[156:159]\n" \
  "s_waitcnt lgkmcnt(8)\n" \
  "v_mfma_f32_16x16x32_bf16 v[160:163], a[8:11], a[120:123], v[160:163]\n" \
  "ds_read_b64_tr_b16 a[32:33], v28 offset:14496\n" \
  "v_mfma_f32_16x16x32_bf16 v[160:163], a[12:15], a[136:139], v[160:163]\n" \
  "s_add_u32 s80, s80, 28672\n" \
  "v_mfma_f32_16x16x32_bf16 v[164:167], a[12:15], a[140:143], v[164:167]\n" \
  "ds_read_b64_tr_b16 a[34:35], v28 offset:18080\n" \
  "v_mfma_f32_16x16x32_bf16 v[164:167], a[8:11], a[124:127], v[164:167]\n" \
  "buffer_load_dwordx4 a[96:99], v8, s[40:43], s8 offen offset:0 sc1\n" \
  "v_mfma_f32_16x16x32_bf16 v[168:171], a[8:11], a[128:131], v[168:171]\n" \
  "ds_read_b64_tr_b16 a[36:37], v28 offset:21664\n" \
  "s_cmp_ge_u32 s80, 114688\n" \
  "v_mfma_f32_16x16x32_bf16 v[168:171], a[12:15], a[144:147], v[168:171]\n" \
  "s_cselect_b32 s80, s82, s80\n" \
  "v_mfma_f32_16x16x32_bf16 v[172:175], a[12:15], a[148:151], v[172:175]\n" \
  "ds_read_b64_tr_b16 a[38:39], v28 offset:25248\n" \
  "v_mfma_f32_16x16x32_bf16 v[172:175], a[8:11], a[132:135], v[172:175]\n" \
  "s_waitcnt lgkmcnt(8)\n" \
  "v_mfma_f32_16x16x32_bf16 v[176:179], a[16:19], a[120:123], v[176:179]\n" \
  "ds_read_b64_tr_b16 a[40:41], v28 offset:192\n" \
  "v_mfma_f32_16x16x32_bf16 v[176:179], a[20:23], a[136:139], v[176:179]\n" \
  "s_add_u32 s9, s9, 28672\n" \
  "v_mfma_f32_16x16x32_bf16 v[180:183], a[20:23], a[140:143], v[180:183]\n" \
  "ds_read_b64_tr_b16 a[42:43], v28 offset:3776\n" \
  "v_mfma_f32_16x16x32_bf16 v[180:183], a[16:19], a[124:127], v[180:183]\n" \
  "buffer_load_dwordx4 a[100:103], v8, s[40:43], s8 offen offset:512 sc1\n" \
  "v_mfma_f32_16x16x32_bf16 v[184:187], a[16:19], a[128:131], v[184:187]\n" \
  "ds_read_b64_tr_b16 a[44:45], v28 offset:7360\n" \
  "s_cmp_eq_u32 s9, 114688\n" \
  "v_mfma_f32_16x16x32_bf16 v[184:187], a[20:23], a[144:147], v[184:187]\n" \
  "s_cselect_b32 s9, 0, s9\n" \
  "v_mfma_f32_16x16x32_bf16 v[188:191], a[20:23], a[148:151], v[188:191]\n" \
  "ds_read_b64_tr_b16 a[46:47], v28 offset:10944\n" \
  "v_mfma_f32_16x16x32_bf16 v[188:191], a[16:19], a[132:135], v[188:191]\n" \
  "s_waitcnt lgkmcnt(8)\n" \
  "v_mfma_f32_16x16x32_bf16 v[192:195], a[24:27], a[120:123], v[192:195]\n" \
  "ds_read_b64_tr_b16 a[48:49], v28 offset:14528\n" \
  "v_mfma_f32_16x16x32_bf16 v[192:195], a[28:31], a[136:139], v[192:195]\n" \
  "v_mfma_f32_16x16x32_bf16 v[196:199], a[28:31], a[140:143], v[196:199]\n" \
  "ds_read_b64_tr_b16 a[50:51], v28 offset:18112\n" \
  "v_mfma_f32_16x16x32_bf16 v[196:199], a[24:27], a[124:127], v[196:199]\n" \
  "buffer_load_dwordx4 a[104:107], v7, s[40:43], s8 offen offset:2048 sc1\n" \
  "v_mfma_f32_16x16x32_bf16 v[200:203], a[24:27], a[128:131], v[200:203]\n" \
  "ds_read_b64_tr_b16 a[52:53], v28 offset:21696\n" \
  "v_mfma_f32_16x16x32_bf16 v[200:203], a[28:31], a[144:147], v[200:203]\n" \
  "v_mfma_f32_16x16x32_bf16 v[204:207], a[28:31], a[148:151], v[204:207]\n" \
  "ds_read_b64_tr_b16 a[54:55], v28 offset:25280\n" \
  "v_add_u32 v28, s9, v9\n" \
  "v_mfma_f32_16x16x32_bf16 v[204:207], a[24:27], a[132:135], v[204:207]\n" \
  "s_waitcnt vmcnt(21) lgkmcnt(8)\n" \
  "s_barrier\n" \
  "v_mfma_f32_16x16x32_bf16 v[208:211], a[32:35], a[120:123], v[208:211]\n" \
  "ds_read_b64_tr_b16 a[0:1], v28 offset:0\n" \
  "v_mfma_f32_16x16x32_bf16 v[208:211], a[36:39], a[136:139], v[208:211]\n" \
  "v_mfma_f32_16x16x32_bf16 v[212:215], a[36:39], a[140:143], v[212:215]\n" \
  "ds_read_b64_tr_b16 a[2:3], v28 offset:3584\n" \
  "v_mfma_f32_16x16x32_bf16 v[212:215], a[32:35], a[124:127], v[212:215]\n" \
  "buffer_load_dwordx4 a[108:111], v7, s[40:43], s8 offen offset:2560 sc1\n" \
  "v_mfma_f32_16x16x32_bf16 v[216:219], a[32:35], a[128:131], v[216:219]\n" \
  "ds_read_b64_tr_b16 a[4:5], v28 offset:7168\n" \
  "v_mfma_f32_16x16x32_bf16 v[216:219], a[36:39], a[144:147], v[216:219]\n" \
  "v_mfma_f32_16x16x32_bf16 v[220:223], a[36:39], a[148:151], v[220:223]\n" \
  "ds_read_b64_tr_b16 a[6:7], v28 offset:10752\n" \
  "v_mfma_f32_16x16x32_bf16 v[220:223], a[32:35], a[132:135], v[220:223]\n" \
  "s_waitcnt lgkmcnt(8)\n" \
  "v_mfma_f32_16x16x32_bf16 v[224:227], a[40:43], a[120:123], v[224:227]\n" \
  "ds_read_b64_tr_b16 a[8:9], v28 offset:14336\n" \
  "v_mfma_f32_16x16x32_bf16 v[224:227], a[44:47], a[136:139], v[224:227]\n" \
  "v_mfma_f32_16x16x32_bf16 v[228:231], a[44:47], a[140:143], v[228:231]\n" \
  "ds_read_b64_tr_b16 a[10:11], v28 offset:17920\n" \
  "v_mfma_f32_16x16x32_bf16 v[228:231], a[40:43], a[124:127], v[228:231]\n" \
  "buffer_load_dwordx4 a[112:115], v8, s[40:43], s8 offen offset:2048 sc1\n" \
  "v_mfma_f32_16x16x32_bf16 v[232:235], a[40:43], a[128:131], v[232:235]\n" \
  "ds_read_b64_tr_b16 a[12:13], v28 offset:21504\n" \
  "v_mfma_f32_16x16x32_bf16 v[232:235], a[44:47], a[144:147], v[232:235]\n" \
  "buffer_load_dword v30, v20, s[36:39], s28 offen\n" \
  "v_mfma_f32_16x16x32_bf16 v[236:239], a[44:47], a[148:151], v[236:239]\n" \
  "ds_read_b64_tr_b16 a[14:15], v28 offset:25088\n" \
  "s_add_u32 s28, s28, 1376256\n" \
  "v_mfma_f32_16x16x32_bf16 v[236:239], a[40:43], a[132:135], v[236:239]\n" \
  "s_waitcnt lgkmcnt(8)\n" \
  "v_mfma_f32_16x16x32_bf16 v[240:243], a[48:51], a[120:123], v[240:243]\n" \
  "ds_read_b64_tr_b16 a[16:17], v28 offset:32\n" \
  "v_mfma_f32_16x16x32_bf16 v[240:243], a[52:55], a[136:139], v[240:243]\n" \
  "v_mfma_f32_16x16x32_bf16 v[244:247], a[52:55], a[140:143], v[244:247]\n" \
  "ds_read_b64_tr_b16 a[18:19], v28 offset:3616\n" \
  "v_mfma_f32_16x16x32_bf16 v[244:247], a[48:51], a[124:127], v[244:247]\n" \
  "buffer_load_dwordx4 a[116:119], v8, s[40:43], s8 offen offset:2560 sc1\n" \
  "v_mfma_f32_16x16x32_bf16 v[248:251], a[48:51], a[128:131], v[248:251]\n" \
  "ds_read_b64_tr_b16 a[20:21], v28 offset:7200\n" \
  "v_mfma_f32_16x16x32_bf16 v[248:251], a[52:55], a[144:147], v[248:251]\n" \
  "v_mfma_f32_16x16x32_bf16 v[252:255], a[52:55], a[148:151], v[252:255]\n" \
  "ds_read_b64_tr_b16 a[22:23], v28 offset:10784\n" \
  "s_add_u32 s8, s8, 4096\n" \
  "v_mfma_f32_16x16x32_bf16 v[252:255], a[48:51], a[132:135], v[252:255]\n" \
  "s_waitcnt vmcnt(17) lgkmcnt(8)\n" \
  "v_mfma_f32_16x16x32_bf16 v[32:35], a[0:3], a[56:59], v[32:35]\n" \
  "ds_read_b64_tr_b16 a[24:25], v28 offset:14368\n" \
  "v_mfma_f32_16x16x32_bf16 v[32:35], a[4:7], a[72:75], v[32:35]\n" \
  "s_mov_b32 m0, s80\n" \
  "v_mfma_f32_16x16x32_bf16 v[36:39], a[4:7], a[76:79], v[36:39]\n" \
  "ds_read_b64_tr_b16 a[26:27], v28 offset:17952\n" \
  "v_mfma_f32_16x16x32_bf16 v[36:39], a[0:3], a[60:63], v[36:39]\n" \
  "buffer_load_dwordx4 v0, s[36:39], s81 offen offset:0 lds\n" \
  "v_mfma_f32_16x16x32_bf16 v[40:43], a[0:3], a[64:67], v[40:43]\n" \
  "ds_read_b64_tr_b16 a[28:29], v28 offset:21536\n" \
  "v_mfma_f32_16x16x32_bf16 v[40:43], a[4:7], a[80:83], v[40:43]\n" \
  "v_mfma_f32_16x16x32_bf16 v[44:47], a[4:7], a[84:87], v[44:47]\n" \
  "ds_read_b64_tr_b16 a[30:31], v28 offset:25120\n" \
  "v_mfma_f32_16x16x32_bf16 v[44:47], a[0:3], a[68:71], v[44:47]\n" \
  "s_waitcnt lgkmcnt(8)\n" \
  "v_mfma_f32_16x16x32_bf16 v[48:51], a[8:11], a[56:59], v[48:51]\n" \
  "ds_read_b64_tr_b16 a[32:33], v28 offset:64\n" \
  "v_mfma_f32_16x16x32_bf16 v[48:51], a[12:15], a[72:75], v[48:51]\n" \
  "v_mfma_f32_16x16x32_bf16 v[52:55], a[12:15], a[76:79], v[52:55]\n" \
  "ds_read_b64_tr_b16 a[34:35], v28 offset:3648\n" \
  "v_mfma_f32_16x16x32_bf16 v[52:55], a[8:11], a[60:63], v[52:55]\n" \
  "buffer_load_dwordx4 v1, s[36:39], s81 offen offset:1024 lds\n" \
  "v_mfma_f32_16x16x32_bf16 v[56:59], a[8:11], a[64:67], v[56:59]\n" \
  "ds_read_b64_tr_b16 a[36:37], v28 offset:7232\n" \
  "v_mfma_f32_16x16x32_bf16 v[56:59], a[12:15], a[80:83], v[56:59]\n" \
  "v_mfma_f32_16x16x32_bf16 v[60:63], a[12:15], a[84:87], v[60:63]\n" \
  "ds_read_b64_tr_b16 a[38:39], v28 offset:10816\n" \
  "v_mfma_f32_16x16x32_bf16 v[60:63], a[8:11], a[68:71], v[60:63]\n" \
  "s_waitcnt lgkmcnt(8)\n" \
  "v_mfma_f32_16x16x32_bf16 v[64:67], a[16:19], a[56:59], v[64:67]\n" \
  "ds_read_b64_tr_b16 a[40:41], v28 offset:14400\n" \
  "v_mfma_f32_16x16x32_bf16 v[64:67], a[20:23], a[72:75], v[64:67]\n" \
  "v_mfma_f32_16x16x32_bf16 v[68:71], a[20:23], a[76:79], v[68:71]\n" \
  "ds_read_b64_tr_b16 a[42:43], v28 offset:17984\n" \
  "v_mfma_f32_16x16x32_bf16 v[68:71], a[16:19], a[60:63], v[68:71]\n" \
  "buffer_load_dwordx4 v2, s[36:39], s81 offen offset:2048 lds\n" \
  "v_mfma_f32_16x16x32_bf16 v[72:75], a[16:19], a[64:67], v[72:75]\n" \
  "ds_read_b64_tr_b16 a[44:45], v28 offset:21568\n" \
  "v_mfma_f32_16x16x32_bf16 v[72:75], a[20:23], a[80:83], v[72:75]\n" \
  "v_mfma_f32_16x16x32_bf16 v[76:79], a[20:23], a[84:87], v[76:79]\n" \
  "ds_read_b64_tr_b16 a[46:47], v28 offset:25152\n" \
  "v_mfma_f32_16x16x32_bf16 v[76:79], a[16:19], a[68:71], v[76:79]\n" \
  "s_waitcnt lgkmcnt(8)\n" \
  "v_mfma_f32_16x16x32_bf16 v[80:83], a[24:27], a[56:59], v[80:83]\n" \
  "ds_read_b64_tr_b16 a[48:49], v28 offset:96\n" \
  "v_mfma_f32_16x16x32_bf16 v[80:83], a[28:31], a[72:75], v[80:83]\n" \
  "v_mfma_f32_16x16x32_bf16 v[84:87], a[28:31], a[76:79], v[84:87]\n" \
  "ds_read_b64_tr_b16 a[50:51], v28 offset:3680\n" \
  "v_mfma_f32_16x16x32_bf16 v[84:87], a[24:27], a[60:63], v[84:87]\n" \
  "buffer_load_dwordx4 v3, s[36:39], s81 offen offset:3072 lds\n" \
  "v_mfma_f32_16x16x32_bf16 v[88:91], a[24:27], a[64:67], v[88:91]\n" \
  "ds_read_b64_tr_b16 a[52:53], v28 offset:7264\n" \
  "v_mfma_f32_16x16x32_bf16 v[88:91], a[28:31], a[80:83], v[88:91]\n" \
  "v_mfma_f32_16x16x32_bf16 v[92:95], a[28:31], a[84:87], v[92:95]\n" \
  "ds_read_b64_tr_b16 a[54:55], v28 offset:10848\n" \
  "v_mfma_f32_16x16x32_bf16 v[92:95], a[24:27], a[68:71], v[92:95]\n" \
  "s_waitcnt lgkmcnt(8)\n" \
  "v_mfma_f32_16x16x32_bf16 v[96:99], a[32:35], a[56:59], v[96:99]\n" \
  "ds_read_b64_tr_b16 a[0:1], v28 offset:14432\n" \
  "v_mfma_f32_16x16x32_bf16 v[96:99], a[36:39], a[72:75], v[96:99]\n" \
  "s_add_u32 m0, s80, 4096\n" \
  "v_mfma_f32_16x16x32_bf16 v[100:103], a[36:39], a[76:79], v[100:103]\n" \
  "ds_read_b64_tr_b16 a[2:3], v28 offset:18016\n" \
  "v_mfma_f32_16x16x32_bf16 v[100:103], a[32:35], a[60:63], v[100:103]\n" \
  "buffer_load_dwordx4 v4, s[36:39], s81 offen offset:0 lds\n" \
  "v_mfma_f32_16x16x32_bf16 v[104:107], a[32:35], a[64:67], v[104:107]\n" \
  "ds_read_b64_tr_b16 a[4:5], v28 offset:21600\n" \
  "v_mfma_f32_16x16x32_bf16 v[104:107], a[36:39], a[80:83], v[104:107]\n" \
  "v_mfma_f32_16x16x32_bf16 v[108:111], a[36:39], a[84:87], v[108:111]\n" \
  "ds_read_b64_tr_b16 a[6:7], v28 offset:25184\n" \
  "v_mfma_f32_16x16x32_bf16 v[108:111], a[32:35], a[68:71], v[108:111]\n" \
  "s_waitcnt lgkmcnt(8)\n" \
  "v_mfma_f32_16x16x32_bf16 v[112:115], a[40:43], a[56:59], v[112:115]\n" \
  "ds_read_b64_tr_b16 a[8:9], v28 offset:128\n" \
  "v_mfma_f32_16x16x32_bf16 v[112:115], a[44:47], a[72:75], v[112:115]\n" \
  "v_mfma_f32_16x16x32_bf16 v[116:119], a[44:47], a[76:79], v[116:119]\n" \
  "ds_read_b64_tr_b16 a[10:11], v28 offset:3712\n" \
  "v_mfma_f32_16x16x32_bf16 v[116:119], a[40:43], a[60:63], v[116:119]\n" \
  "buffer_load_dwordx4 v5, s[36:39], s81 offen offset:1024 lds\n" \
  "v_mfma_f32_16x16x32_bf16 v[120:123], a[40:43], a[64:67], v[120:123]\n" \
  "ds_read_b64_tr_b16 a[12:13], v28 offset:7296\n" \
  "v_mfma_f32_16x16x32_bf16 v[120:123], a[44:47], a[80:83], v[120:123]\n" \
  "v_mfma_f32_16x16x32_bf16 v[124:127], a[44:47], a[84:87], v[124:127]\n" \
  "ds_read_b64_tr_b16 a[14:15], v28 offset:10880\n" \
  "v_mfma_f32_16x16x32_bf16 v[124:127], a[40:43], a[68:71], v[124:127]\n" \
  "s_waitcnt lgkmcnt(8)\n" \
  "v_mfma_f32_16x16x32_bf16 v[128:131], a[48:51], a[56:59], v[128:131]\n" \
  "ds_read_b64_tr_b16 a[16:17], v28 offset:14464\n" \
  "v_mfma_f32_16x16x32_bf16 v[128:131], a[52:55], a[72:75], v[128:131]\n" \
  "v_mfma_f32_16x16x32_bf16 v[132:135], a[52:55], a[76:79], v[132:135]\n" \
  "ds_read_b64_tr_b16 a[18:19], v28 offset:18048\n" \
  "v_mfma_f32_16x16x32_bf16 v[132:135], a[48:51], a[60:63], v[132:135]\n" \
  "buffer_load_dwordx4 v6, s[36:39], s81 offen offset:2048 lds\n" \
  "v_mfma_f32_16x16x32_bf16 v[136:139], a[48:51], a[64:67], v[136:139]\n" \
  "ds_read_b64_tr_b16 a[20:21], v28 offset:21632\n" \
  "v_mfma_f32_16x16x32_bf16 v[136:139], a[52:55], a[80:83], v[136:139]\n" \
  "buffer_load_dwordx4 a[120:123], v7, s[40:43], s8 offen offset:0 sc1\n" \
  "s_add_u32 s81, s81, 458752\n" \
  "v_mfma_f32_16x16x32_bf16 v[140:143], a[52:55], a[84:87], v[140:143]\n" \
  "ds_read_b64_tr_b16 a[22:23], v28 offset:25216\n" \
  "v_mfma_f32_16x16x32_bf16 v[140:143], a[48:51], a[68:71], v[140:143]\n" \
  "s_waitcnt lgkmcnt(8)\n" \
  "v_mfma_f32_16x16x32_bf16 v[144:147], a[0:3], a[56:59], v[144:147]\n" \
  "ds_read_b64_tr_b16 a[24:25], v28 offset:160\n" \
  "v_mfma_f32_16x16x32_bf16 v[144:147], a[4:7], a[72:75], v[144:147]\n" \
  "v_mfma_f32_16x16x32_bf16 v[148:151], a[4:7], a[76:79], v[148:151]\n" \
  "ds_read_b64_tr_b16 a[26:27], v28 offset:3744\n" \
  "v_mfma_f32_16x16x32_bf16 v[148:151], a[0:3], a[60:63], v[148:151]\n" \
  "buffer_load_dwordx4 a[124:127], v7, s[40:43], s8 offen offset:512 sc1\n" \
  "v_mfma_f32_16x16x32_bf16 v[152:155], a[0:3], a[64:67], v[152:155]\n" \
  "ds_read_b64_tr_b16 a[28:29], v28 offset:7328\n" \
  "v_mfma_f32_16x16x32_bf16 v[152:155], a[4:7], a[80:83], v[152:155]\n" \
  "buffer_store_dwordx4 v[16:19], v12, s[52:55], s11 offen sc1\n" \
  "v_mfma_f32_16x16x32_bf16 v[156:159], a[4:7], a[84:87], v[156:159]\n" \
  "ds_read_b64_tr_b16 a[30:31], v28 offset:10912\n" \
  "s_add_u32 s11, s11, 2048\n" \
  "v_mfma_f32_16x16x32_bf16 v[156:159], a[0:3], a[68:71], v[156:159]\n" \
  "s_waitcnt lgkmcnt(8)\n" \
  "v_mfma_f32_16x16x32_bf16 v[160:163], a[8:11], a[56:59], v[160:163]\n" \
  "ds_read_b64_tr_b16 a[32:33], v28 offset:14496\n" \
  "v_mfma_f32_16x16x32_bf16 v[160:163], a[12:15], a[72:75], v[160:163]\n" \
  "s_add_u32 s80, s80, 28672\n" \
  "v_mfma_f32_16x16x32_bf16 v[164:167], a[12:15], a[76:79], v[164:167]\n" \
  "ds_read_b64_tr_b16 a[34:35], v28 offset:18080\n" \
  "v_mfma_f32_16x16x32_bf16 v[164:167], a[8:11], a[60:63], v[164:167]\n" \
  "buffer_load_dwordx4 a[128:131], v8, s[40:43], s8 offen offset:0 sc1\n" \
  "v_mfma_f32_16x16x32_bf16 v[168:171], a[8:11], a[64:67], v[168:171]\n" \
  "ds_read_b64_tr_b16 a[36:37], v28 offset:21664\n" \
  "s_cmp_ge_u32 s80, 114688\n" \
  "v_mfma_f32_16x16x32_bf16 v[168:171], a[12:15], a[80:83], v[168:171]\n" \
  "s_cselect_b32 s80, s82, s80\n" \
  "v_mfma_f32_16x16x32_bf16 v[172:175], a[12:15], a[84:87], v[172:175]\n" \
  "ds_read_b64_tr_b16 a[38:39], v28 offset:25248\n" \
  "v_mfma_f32_16x16x32_bf16 v[172:175], a[8:11], a[68:71], v[172:175]\n" \
  "s_waitcnt lgkmcnt(8)\n" \
  "v_mfma_f32_16x16x32_bf16 v[176:179], a[16:19], a[56:59], v[176:179]\n" \
  "ds_read_b64_tr_b16 a[40:41], v28 offset:192\n" \
  "v_mfma_f32_16x16x32_bf16 v[176:179], a[20:23], a[72:75], v[176:179]\n" \
  "s_add_u32 s9, s9, 28672\n" \
  "v_mfma_f32_16x16x32_bf16 v[180:183], a[20:23], a[76:79], v[180:183]\n" \
  "ds_read_b64_tr_b16 a[42:43], v28 offset:3776\n" \
  "v_mfma_f32_16x16x32_bf16 v[180:183], a[16:19], a[60:63], v[180:183]\n" \
  "buffer_load_dwordx4 a[132:135], v8, s[40:43], s8 offen offset:512 sc1\n" \
  "v_mfma_f32_16x16x32_bf16 v[184:187], a[16:19], a[64:67], v[184:187]\n" \
  "ds_read_b64_tr_b16 a[44:45], v28 offset:7360\n" \
  "s_cmp_eq_u32 s9, 114688\n" \
  "v_mfma_f32_16x16x32_bf16 v[184:187], a[20:23], a[80:83], v[184:187]\n" \
  "s_cselect_b32 s9, 0, s9\n" \
  "v_mfma_f32_16x16x32_bf16 v[188:191], a[20:23], a[84:87], v[188:191]\n" \
  "ds_read_b64_tr_b16 a[46:47], v28 offset:10944\n" \
  "v_mfma_f32_16x16x32_bf16 v[188:191], a[16:19], a[68:71], v[188:191]\n" \
  "s_waitcnt lgkmcnt(8)\n" \
  "v_mfma_f32_16x16x32_bf16 v[192:195], a[24:27], a[56:59], v[192:195]\n" \
  "ds_read_b64_tr_b16 a[48:49], v28 offset:14528\n" \
  "v_mfma_f32_16x16x32_bf16 v[192:195], a[28:31], a[72:75], v[192:195]\n" \
  "v_mfma_f32_16x16x32_bf16 v[196:199], a[28:31], a[76:79], v[196:199]\n" \
  "ds_read_b64_tr_b16 a[50:51], v28 offset:18112\n" \
  "v_mfma_f32_16x16x32_bf16 v[196:199], a[24:27], a[60:63], v[196:199]\n" \
  "buffer_load_dwordx4 a[136:139], v7, s[40:43], s8 offen offset:2048 sc1\n" \
  "v_mfma_f32_16x16x32_bf16 v[200:203], a[24:27], a[64:67], v[200:203]\n" \
  "ds_read_b64_tr_b16 a[52:53], v28 offset:21696\n" \
  "v_mfma_f32_16x16x32_bf16 v[200:203], a[28:31], a[80:83], v[200:203]\n" \
  "v_mfma_f32_16x16x32_bf16 v[204:207], a[28:31], a[84:87], v[204:207]\n" \
  "ds_read_b64_tr_b16 a[54:55], v28 offset:25280\n" \
  "v_add_u32 v28, s9, v9\n" \
  "v_mfma_f32_16x16x32_bf16 v[204:207], a[24:27], a[68:71], v[204:207]\n" \
  "s_waitcnt vmcnt(23) lgkmcnt(8)\n" \
  "s_barrier\n" \
  "v_mfma_f32_16x16x32_bf16 v[208:211], a[32:35], a[56:59], v[208:211]\n" \
  "ds_read_b64_tr_b16 a[0:1], v28 offset:0\n" \
  "v_mfma_f32_16x16x32_bf16 v[208:211], a[36:39], a[72:75], v[208:211]\n" \
  "v_mfma_f32_16x16x32_bf16 v[212:215], a[36:39], a[76:79], v[212:215]\n" \
  "ds_read_b64_tr_b16 a[2:3], v28 offset:3584\n" \
  "v_mfma_f32_16x16x32_bf16 v[212:215], a[32:35], a[60:63], v[212:215]\n" \
  "buffer_load_dwordx4 a[140:143], v7, s[40:43], s8 offen offset:2560 sc1\n" \
  "v_mfma_f32_16x16x32_bf16 v[216:219], a[32:35], a[64:67], v[216:219]\n" \
  "ds_read_b64_tr_b16 a[4:5], v28 offset:7168\n" \
  "v_mfma_f32_16x16x32_bf16 v[216:219], a[36:39], a[80:83], v[216:219]\n" \
  "v_mfma_f32_16x16x32_bf16 v[220:223], a[36:39], a[84:87], v[220:223]\n" \
  "ds_read_b64_tr_b16 a[6:7], v28 offset:10752\n" \
  "v_mfma_f32_16x16x32_bf16 v[220:223], a[32:35], a[68:71], v[220:223]\n" \
  "s_waitcnt lgkmcnt(8)\n" \
  "v_mfma_f32_16x16x32_bf16 v[224:227], a[40:43], a[56:59], v[224:227]\n" \
  "ds_read_b64_tr_b16 a[8:9], v28 offset:14336\n" \
  "v_mfma_f32_16x16x32_bf16 v[224:227], a[44:47], a[72:75], v[224:227]\n" \
  "v_mfma_f32_16x16x32_bf16 v[228:231], a[44:47], a[76:79], v[228:231]\n" \
  "ds_read_b64_tr_b16 a[10:11], v28 offset:17920\n" \
  "v_mfma_f32_16x16x32_bf16 v[228:231], a[40:43], a[60:63], v[228:231]\n" \
  "buffer_load_dwordx4 a[144:147], v8, s[40:43], s8 offen offset:2048 sc1\n" \
  "v_mfma_f32_16x16x32_bf16 v[232:235], a[40:43], a[64:67], v[232:235]\n" \
  "ds_read_b64_tr_b16 a[12:13], v28 offset:21504\n" \
  "v_mfma_f32_16x16x32_bf16 v[232:235], a[44:47], a[80:83], v[232:235]\n" \
  "global_load_dword v30, v21, s[30:31]\n" \
  "v_mfma_f32_16x16x32_bf16 v[236:239], a[44:47], a[84:87], v[236:239]\n" \
  "ds_read_b64_tr_b16 a[14:15], v28 offset:25088\n" \
  "v_mfma_f32_16x16x32_bf16 v[236:239], a[40:43], a[68:71], v[236:239]\n" \
  "s_waitcnt lgkmcnt(8)\n" \
  "v_mfma_f32_16x16x32_bf16 v[240:243], a[48:51], a[56:59], v[240:243]\n" \
  "ds_read_b64_tr_b16 a[16:17], v28 offset:32\n" \
  "v_mfma_f32_16x16x32_bf16 v[240:243], a[52:55], a[72:75], v[240:243]\n" \
  "v_mfma_f32_16x16x32_bf16 v[244:247], a[52:55], a[76:79], v[244:247]\n" \
  "ds_read_b64_tr_b16 a[18:19], v28 offset:3616\n" \
  "v_mfma_f32_16x16x32_bf16 v[244:247], a[48:51], a[60:63], v[244:247]\n" \
  "buffer_load_dwordx4 a[148:151], v8, s[40:43], s8 offen offset:2560 sc1\n" \
  "v_mfma_f32_16x16x32_bf16 v[248:251], a[48:51], a[64:67], v[248:251]\n" \
  "ds_read_b64_tr_b16 a[20:21], v28 offset:7200\n" \
  "v_mfma_f32_16x16x32_bf16 v[248:251], a[52:55], a[80:83], v[248:251]\n" \
  "v_mfma_f32_16x16x32_bf16 v[252:255], a[52:55], a[84:87], v[252:255]\n" \
  "ds_read_b64_tr_b16 a[22:23], v28 offset:10784\n" \
  "s_add_u32 s8, s8, 4096\n" \
  "v_mfma_f32_16x16x32_bf16 v[252:255], a[48:51], a[68:71], v[252:255]\n" \
  "s_waitcnt vmcnt(17) lgkmcnt(8)\n" \
  "v_mfma_f32_16x16x32_bf16 v[32:35], a[0:3], a[88:91], v[32:35]\n" \
  "ds_read_b64_tr_b16 a[24:25], v28 offset:14368\n" \
  "v_mfma_f32_16x16x32_bf16 v[32:35], a[4:7], a[104:107], v[32:35]\n" \
  "s_mov_b32 m0, s80\n" \
  "v_mfma_f32_16x16x32_bf16 v[36:39], a[4:7], a[108:111], v[36:39]\n" \
  "ds_read_b64_tr_b16 a[26:27], v28 offset:17952\n" \
  "v_mfma_f32_16x16x32_bf16 v[36:39], a[0:3], a[92:95], v[36:39]\n" \
  "buffer_load_dwordx4 v0, s[36:39], s81 offen offset:0 lds\n" \
  "v_mfma_f32_16x16x32_bf16 v[40:43], a[0:3], a[96:99], v[40:43]\n" \
  "ds_read_b64_tr_b16 a[28:29], v28 offset:21536\n" \
  "v_mfma_f32_16x16x32_bf16 v[40:43], a[4:7], a[112:115], v[40:43]\n" \
  "v_mfma_f32_16x16x32_bf16 v[44:47], a[4:7], a[116:119], v[44:47]\n" \
  "ds_read_b64_tr_b16 a[30:31], v28 offset:25120\n" \
  "v_mfma_f32_16x16x32_bf16 v[44:47], a[0:3], a[100:103], v[44:47]\n" \
  "s_waitcnt lgkmcnt(8)\n" \
  "v_mfma_f32_16x16x32_bf16 v[48:51], a[8:11], a[88:91], v[48:51]\n" \
  "ds_read_b64_tr_b16 a[32:33], v28 offset:64\n" \
  "v_mfma_f32_16x16x32_bf16 v[48:51], a[12:15], a[104:107], v[48:51]\n" \
  "v_mfma_f32_16x16x32_bf16 v[52:55], a[12:15], a[108:111], v[52:55]\n" \
  "ds_read_b64_tr_b16 a[34:35], v28 offset:3648\n" \
  "v_mfma_f32_16x16x32_bf16 v[52:55], a[8:11], a[92:95], v[52:55]\n" \
  "buffer_load_dwordx4 v1, s[36:39], s81 offen offset:1024 lds\n" \
  "v_mfma_f32_16x16x32_bf16 v[56:59], a[8:11], a[96:99], v[56:59]\n" \
  "ds_read_b64_tr_b16 a[36:37], v28 offset:7232\n" \
  "v_mfma_f32_16x16x32_bf16 v[56:59], a[12:15], a[112:115], v[56:59]\n" \
  "v_mfma_f32_16x16x32_bf16 v[60:63], a[12:15], a[116:119], v[60:63]\n" \
  "ds_read_b64_tr_b16 a[38:39], v28 offset:10816\n" \
  "v_mfma_f32_16x16x32_bf16 v[60:63], a[8:11], a[100:103], v[60:63]\n" \
  "s_waitcnt lgkmcnt(8)\n" \
  "v_mfma_f32_16x16x32_bf16 v[64:67], a[16:19], a[88:91], v[64:67]\n" \
  "ds_read_b64_tr_b16 a[40:41], v28 offset:14400\n" \
  "v_mfma_f32_16x16x32_bf16 v[64:67], a[20:23], a[104:107], v[64:67]\n" \
  "v_mfma_f32_16x16x32_bf16 v[68:71], a[20:23], a[108:111], v[68:71]\n" \
  "ds_read_b64_tr_b16 a[42:43], v28 offset:17984\n" \
  "v_mfma_f32_16x16x32_bf16 v[68:71], a[16:19], a[92:95], v[68:71]\n" \
  "buffer_load_dwordx4 v2, s[36:39], s81 offen offset:2048 lds\n" \
  "v_mfma_f32_16x16x32_bf16 v[72:75], a[16:19], a[96:99], v[72:75]\n" \
  "ds_read_b64_tr_b16 a[44:45], v28 offset:21568\n" \
  "v_mfma_f32_16x16x32_bf16 v[72:75], a[20:23], a[112:115], v[72:75]\n" \
  "v_mfma_f32_16x16x32_bf16 v[76:79], a[20:23], a[116:119], v[76:79]\n" \
  "ds_read_b64_tr_b16 a[46:47], v28 offset:25152\n" \
  "v_mfma_f32_16x16x32_bf16 v[76:79], a[16:19], a[100:103], v[76:79]\n" \
  "s_waitcnt lgkmcnt(8)\n" \
  "v_mfma_f32_16x16x32_bf16 v[80:83], a[24:27], a[88:91], v[80:83]\n" \
  "ds_read_b64_tr_b16 a[48:49], v28 offset:96\n" \
  "v_mfma_f32_16x16x32_bf16 v[80:83], a[28:31], a[104:107], v[80:83]\n" \
  "v_mfma_f32_16x16x32_bf16 v[84:87], a[28:31], a[108:111], v[84:87]\n" \
  "ds_read_b64_tr_b16 a[50:51], v28 offset:3680\n" \
  "v_mfma_f32_16x16x32_bf16 v[84:87], a[24:27], a[92:95], v[84:87]\n" \
  "buffer_load_dwordx4 v3, s[36:39], s81 offen offset:3072 lds\n" \
  "v_mfma_f32_16x16x32_bf16 v[88:91], a[24:27], a[96:99], v[88:91]\n" \
  "ds_read_b64_tr_b16 a[52:53], v28 offset:7264\n" \
  "v_mfma_f32_16x16x32_bf16 v[88:91], a[28:31], a[112:115], v[88:91]\n" \
  "v_mfma_f32_16x16x32_bf16 v[92:95], a[28:31], a[116:119], v[92:95]\n" \
  "ds_read_b64_tr_b16 a[54:55], v28 offset:10848\n" \
  "v_mfma_f32_16x16x32_bf16 v[92:95], a[24:27], a[100:103], v[92:95]\n" \
  "s_waitcnt lgkmcnt(8)\n" \
  "v_mfma_f32_16x16x32_bf16 v[96:99], a[32:35], a[88:91], v[96:99]\n" \
  "ds_read_b64_tr_b16 a[0:1], v28 offset:14432\n" \
  "v_mfma_f32_16x16x32_bf16 v[96:99], a[36:39], a[104:107], v[96:99]\n" \
  "s_add_u32 m0, s80, 4096\n" \
  "v_mfma_f32_16x16x32_bf16 v[100:103], a[36:39], a[108:111], v[100:103]\n" \
  "ds_read_b64_tr_b16 a[2:3], v28 offset:18016\n" \
  "v_mfma_f32_16x16x32_bf16 v[100:103], a[32:35], a[92:95], v[100:103]\n" \
  "buffer_load_dwordx4 v4, s[36:39], s81 offen offset:0 lds\n" \
  "v_mfma_f32_16x16x32_bf16 v[104:107], a[32:35], a[96:99], v[104:107]\n" \
  "ds_read_b64_tr_b16 a[4:5], v28 offset:21600\n" \
  "v_mfma_f32_16x16x32_bf16 v[104:107], a[36:39], a[112:115], v[104:107]\n" \
  "v_mfma_f32_16x16x32_bf16 v[108:111], a[36:39], a[116:119], v[108:111]\n" \
  "ds_read_b64_tr_b16 a[6:7], v28 offset:25184\n" \
  "v_mfma_f32_16x16x32_bf16 v[108:111], a[32:35], a[100:103], v[108:111]\n" \
  "s_waitcnt lgkmcnt(8)\n" \
  "v_mfma_f32_16x16x32_bf16 v[112:115], a[40:43], a[88:91], v[112:115]\n" \
  "ds_read_b64_tr_b16 a[8:9], v28 offset:128\n" \
  "v_mfma_f32_16x16x32_bf16 v[112:115], a[44:47], a[104:107], v[112:115]\n" \
  "v_mfma_f32_16x16x32_bf16 v[116:119], a[44:47], a[108:111], v[116:119]\n" \
  "ds_read_b64_tr_b16 a[10:11], v28 offset:3712\n" \
  "v_mfma_f32_16x16x32_bf16 v[116:119], a[40:43], a[92:95], v[116:119]\n" \
  "buffer_load_dwordx4 v5, s[36:39], s81 offen offset:1024 lds\n" \
  "v_mfma_f32_16x16x32_bf16 v[120:123], a[40:43], a[96:99], v[120:123]\n" \
  "ds_read_b64_tr_b16 a[12:13], v28 offset:7296\n" \
  "v_mfma_f32_16x16x32_bf16 v[120:123], a[44:47], a[112:115], v[120:123]\n" \
  "v_mfma_f32_16x16x32_bf16 v[124:127], a[44:47], a[116:119], v[124:127]\n" \
  "ds_read_b64_tr_b16 a[14:15], v28 offset:10880\n" \
  "v_mfma_f32_16x16x32_bf16 v[124:127], a[40:43], a[100:103], v[124:127]\n" \
  "s_waitcnt lgkmcnt(8)\n" \
  "v_mfma_f32_16x16x32_bf16 v[128:131], a[48:51], a[88:91], v[128:131]\n" \
  "ds_read_b64_tr_b16 a[16:17], v28 offset:14464\n" \
  "v_mfma_f32_16x16x32_bf16 v[128:131], a[52:55], a[104:107], v[128:131]\n" \
  "v_mfma_f32_16x16x32_bf16 v[132:135], a[52:55], a[108:111], v[132:135]\n" \
  "ds_read_b64_tr_b16 a[18:19], v28 offset:18048\n" \
  "v_mfma_f32_16x16x32_bf16 v[132:135], a[48:51], a[92:95], v[132:135]\n" \
  "buffer_load_dwordx4 v6, s[36:39], s81 offen offset:2048 lds\n" \
  "v_mfma_f32_16x16x32_bf16 v[136:139], a[48:51], a[96:99], v[136:139]\n" \
  "ds_read_b64_tr_b16 a[20:21], v28 offset:21632\n" \
  "v_mfma_f32_16x16x32_bf16 v[136:139], a[52:55], a[112:115], v[136:139]\n" \
  "buffer_load_dwordx4 a[56:59], v7, s[40:43], s8 offen offset:0 sc1\n" \
  "s_add_u32 s81, s81, 458752\n" \
  "v_mfma_f32_16x16x32_bf16 v[140:143], a[52:55], a[116:119], v[140:143]\n" \
  "ds_read_b64_tr_b16 a[22:23], v28 offset:25216\n" \
  "v_mfma_f32_16x16x32_bf16 v[140:143], a[48:51], a[100:103], v[140:143]\n" \
  "s_waitcnt lgkmcnt(8)\n" \
  "v_mfma_f32_16x16x32_bf16 v[144:147], a[0:3], a[88:91], v[144:147]\n" \
  "ds_read_b64_tr_b16 a[24:25], v28 offset:160\n" \
  "v_mfma_f32_16x16x32_bf16 v[144:147], a[4:7], a[104:107], v[144:147]\n" \
  "v_mfma_f32_16x16x32_bf16 v[148:151], a[4:7], a[108:111], v[148:151]\n" \
  "ds_read_b64_tr_b16 a[26:27], v28 offset:3744\n" \
  "v_mfma_f32_16x16x32_bf16 v[148:151], a[0:3], a[92:95], v[148:151]\n" \
  "buffer_load_dwordx4 a[60:63], v7, s[40:43], s8 offen offset:512 sc1\n" \
  "v_mfma_f32_16x16x32_bf16 v[152:155], a[0:3], a[96:99], v[152:155]\n" \
  "ds_read_b64_tr_b16 a[28:29], v28 offset:7328\n" \
  "v_mfma_f32_16x16x32_bf16 v[152:155], a[4:7], a[112:115], v[152:155]\n" \
  "buffer_load_dword v30, v13, s[56:59], s12 offen\n" \
  "v_mfma_f32_16x16x32_bf16 v[156:159], a[4:7], a[116:119], v[156:159]\n" \
  "ds_read_b64_tr_b16 a[30:31], v28 offset:10912\n" \
  "s_add_u32 s12, s12, 917504\n" \
  "v_mfma_f32_16x16x32_bf16 v[156:159], a[0:3], a[100:103], v[156:159]\n" \
  "s_waitcnt lgkmcnt(8)\n" \
  "v_mfma_f32_16x16x32_bf16 v[160:163], a[8:11], a[88:91], v[160:163]\n" \
  "ds_read_b64_tr_b16 a[32:33], v28 offset:14496\n" \
  "v_mfma_f32_16x16x32_bf16 v[160:163], a[12:15], a[104:107], v[160:163]\n" \
  "s_add_u32 s80, s80, 28672\n" \
  "v_mfma_f32_16x16x32_bf16 v[164:167], a[12:15], a[108:111], v[164:167]\n" \
  "ds_read_b64_tr_b16 a[34:35], v28 offset:18080\n" \
  "v_mfma_f32_16x16x32_bf16 v[164:167], a[8:11], a[92:95], v[164:167]\n" \
  "buffer_load_dwordx4 a[64:67], v8, s[40:43], s8 offen offset:0 sc1\n" \
  "v_mfma_f32_16x16x32_bf16 v[168:171], a[8:11], a[96:99], v[168:171]\n" \
  "ds_read_b64_tr_b16 a[36:37], v28 offset:21664\n" \
  "s_cmp_ge_u32 s80, 114688\n" \
  "v_mfma_f32_16x16x32_bf16 v[168:171], a[12:15], a[112:115], v[168:171]\n" \
  "s_cselect_b32 s80, s82, s80\n" \
  "v_mfma_f32_16x16x32_bf16 v[172:175], a[12:15], a[116:119], v[172:175]\n" \
  "ds_read_b64_tr_b16 a[38:39], v28 offset:25248\n" \
  "v_mfma_f32_16x16x32_bf16 v[172:175], a[8:11], a[100:103], v[172:175]\n" \
  "s_waitcnt lgkmcnt(8)\n" \
  "v_mfma_f32_16x16x32_bf16 v[176:179], a[16:19], a[88:91], v[176:179]\n" \
  "ds_read_b64_tr_b16 a[40:41], v28 offset:192\n" \
  "v_mfma_f32_16x16x32_bf16 v[176:179], a[20:23], a[104:107], v[176:179]\n" \
  "s_add_u32 s9, s9, 28672\n" \
  "v_mfma_f32_16x16x32_bf16 v[180:183], a[20:23], a[108:111], v[180:183]\n" \
  "ds_read_b64_tr_b16 a[42:43], v28 offset:3776\n" \
  "v_mfma_f32_16x16x32_bf16 v[180:183], a[16:19], a[92:95], v[180:183]\n" \
  "buffer_load_dwordx4 a[68:71], v8, s[40:43], s8 offen offset:512 sc1\n" \
  "v_mfma_f32_16x16x32_bf16 v[184:187], a[16:19], a[96:99], v[184:187]\n" \
  "ds_read_b64_tr_b16 a[44:45], v28 offset:7360\n" \
  "s_cmp_eq_u32 s9, 114688\n" \
  "v_mfma_f32_16x16x32_bf16 v[184:187], a[20:23], a[112:115], v[184:187]\n" \
  "s_cselect_b32 s9, 0, s9\n" \
  "v_mfma_f32_16x16x32_bf16 v[188:191], a[20:23], a[116:119], v[188:191]\n" \
  "ds_read_b64_tr_b16 a[46:47], v28 offset:10944\n" \
  "v_mfma_f32_16x16x32_bf16 v[188:191], a[16:19], a[100:103], v[188:191]\n" \
  "s_waitcnt lgkmcnt(8)\n" \
  "v_mfma_f32_16x16x32_bf16 v[192:195], a[24:27], a[88:91], v[192:195]\n" \
  "ds_read_b64_tr_b16 a[48:49], v28 offset:14528\n" \
  "v_mfma_f32_16x16x32_bf16 v[192:195], a[28:31], a[104:107], v[192:195]\n" \
  "v_mfma_f32_16x16x32_bf16 v[196:199], a[28:31], a[108:111], v[196:199]\n" \
  "ds_read_b64_tr_b16 a[50:51], v28 offset:18112\n" \
  "v_mfma_f32_16x16x32_bf16 v[196:199], a[24:27], a[92:95], v[196:199]\n" \
  "buffer_load_dwordx4 a[72:75], v7, s[40:43], s8 offen offset:2048 sc1\n" \
  "v_mfma_f32_16x16x32_bf16 v[200:203], a[24:27], a[96:99], v[200:203]\n" \
  "ds_read_b64_tr_b16 a[52:53], v28 offset:21696\n" \
  "v_mfma_f32_16x16x32_bf16 v[200:203], a[28:31], a[112:115], v[200:203]\n" \
  "v_mfma_f32_16x16x32_bf16 v[204:207], a[28:31], a[116:119], v[204:207]\n" \
  "ds_read_b64_tr_b16 a[54:55], v28 offset:25280\n" \
  "v_add_u32 v28, s9, v9\n" \
  "v_mfma_f32_16x16x32_bf16 v[204:207], a[24:27], a[100:103], v[204:207]\n" \
  "s_waitcnt vmcnt(23) lgkmcnt(8)\n" \
  "s_barrier\n" \
  "v_mfma_f32_16x16x32_bf16 v[208:211], a[32:35], a[88:91], v[208:211]\n" \
  "ds_read_b64_tr_b16 a[0:1], v28 offset:0\n" \
  "v_mfma_f32_16x16x32_bf16 v[208:211], a[36:39], a[104:107], v[208:211]\n" \
  "v_mfma_f32_16x16x32_bf16 v[212:215], a[36:39], a[108:111], v[212:215]\n" \
  "ds_read_b64_tr_b16 a[2:3], v28 offset:3584\n" \
  "v_mfma_f32_16x16x32_bf16 v[212:215], a[32:35], a[92:95], v[212:215]\n" \
  "buffer_load_dwordx4 a[76:79], v7, s[40:43], s8 offen offset:2560 sc1\n" \
  "v_mfma_f32_16x16x32_bf16 v[216:219], a[32:35], a[96:99], v[216:219]\n" \
  "ds_read_b64_tr_b16 a[4:5], v28 offset:7168\n" \
  "v_mfma_f32_16x16x32_bf16 v[216:219], a[36:39], a[112:115], v[216:219]\n" \
  "v_mfma_f32_16x16x32_bf16 v[220:223], a[36:39], a[116:119], v[220:223]\n" \
  "ds_read_b64_tr_b16 a[6:7], v28 offset:10752\n" \
  "v_mfma_f32_16x16x32_bf16 v[220:223], a[32:35], a[100:103], v[220:223]\n" \
  "s_waitcnt lgkmcnt(8)\n" \
  "v_mfma_f32_16x16x32_bf16 v[224:227], a[40:43], a[88:91], v[224:227]\n" \
  "ds_read_b64_tr_b16 a[8:9], v28 offset:14336\n" \
  "v_mfma_f32_16x16x32_bf16 v[224:227], a[44:47], a[104:107], v[224:227]\n" \
  "v_mfma_f32_16x16x32_bf16 v[228:231], a[44:47], a[108:111], v[228:231]\n" \
  "ds_read_b64_tr_b16 a[10:11], v28 offset:17920\n" \
  "v_mfma_f32_16x16x32_bf16 v[228:231], a[40:43], a[92:95], v[228:231]\n" \
  "buffer_load_dwordx4 a[80:83], v8, s[40:43], s8 offen offset:2048 sc1\n" \
  "v_mfma_f32_16x16x32_bf16 v[232:235], a[40:43], a[96:99], v[232:235]\n" \
  "ds_read_b64_tr_b16 a[12:13], v28 offset:21504\n" \
  "v_mfma_f32_16x16x32_bf16 v[232:235], a[44:47], a[112:115], v[232:235]\n" \
  "v_mfma_f32_16x16x32_bf16 v[236:239], a[44:47], a[116:119], v[236:239]\n" \
  "ds_read_b64_tr_b16 a[14:15], v28 offset:25088\n" \
  "v_mfma_f32_16x16x32_bf16 v[236:239], a[40:43], a[100:103], v[236:239]\n" \
  "s_waitcnt lgkmcnt(8)\n" \
  "v_mfma_f32_16x16x32_bf16 v[240:243], a[48:51], a[88:91], v[240:243]\n" \
  "ds_read_b64_tr_b16 a[16:17], v28 offset:32\n" \
  "v_mfma_f32_16x16x32_bf16 v[240:243], a[52:55], a[104:107], v[240:243]\n" \
  "v_mfma_f32_16x16x32_bf16 v[244:247], a[52:55], a[108:111], v[244:247]\n" \
  "ds_read_b64_tr_b16 a[18:19], v28 offset:3616\n" \
  "v_mfma_f32_16x16x32_bf16 v[244:247], a[48:51], a[92:95], v[244:247]\n" \
  "buffer_load_dwordx4 a[84:87], v8, s[40:43], s8 offen offset:2560 sc1\n" \
  "v_mfma_f32_16x16x32_bf16 v[248:251], a[48:51], a[96:99], v[248:251]\n" \
  "ds_read_b64_tr_b16 a[20:21], v28 offset:7200\n" \
  "v_mfma_f32_16x16x32_bf16 v[248:251], a[52:55], a[112:115], v[248:251]\n" \
  "v_mfma_f32_16x16x32_bf16 v[252:255], a[52:55], a[116:119], v[252:255]\n" \
  "ds_read_b64_tr_b16 a[22:23], v28 offset:10784\n" \
  "s_add_u32 s8, s8, 4096\n" \
  "v_mfma_f32_16x16x32_bf16 v[252:255], a[48:51], a[100:103], v[252:255]\n" \
  "s_sub_u32 s10, s10, 1\n" \
  "s_cmp_lg_u32 s10, 0\n" \
  "s_cbranch_scc1 1b\n" \
  ".Lmg_mid:\n" \
  "s_waitcnt vmcnt(16) lgkmcnt(8)\n" \
  "v_mfma_f32_16x16x32_bf16 v[32:35], a[0:3], a[120:123], v[32:35]\n" \
  "ds_read_b64_tr_b16 a[24:25], v28 offset:14368\n" \
  "v_accvgpr_read_b32 v0, a160\n" \
  "v_mfma_f32_16x16x32_bf16 v[32:35], a[4:7], a[136:139], v[32:35]\n" \
  "v_accvgpr_read_b32 v1, a161\n" \
  "v_mfma_f32_16x16x32_bf16 v[36:39], a[4:7], a[140:143], v[36:39]\n" \
  "ds_read_b64_tr_b16 a[26:27], v28 offset:17952\n" \
  "v_accvgpr_read_b32 v2, a162\n" \
  "v_mfma_f32_16x16x32_bf16 v[36:39], a[0:3], a[124:127], v[36:39]\n" \
  "v_accvgpr_read_b32 v3, a163\n" \
  "v_mfma_f32_16x16x32_bf16 v[40:43], a[0:3], a[128:131], v[40:43]\n" \
  "ds_read_b64_tr_b16 a[28:29], v28 offset:21536\n" \
  "v_accvgpr_read_b32 v4, a164\n" \
  "v_mfma_f32_16x16x32_bf16 v[40:43], a[4:7], a[144:147], v[40:43]\n" \
  "v_accvgpr_read_b32 v5, a165\n" \
  "v_mfma_f32_16x16x32_bf16 v[44:47], a[4:7], a[148:151], v[44:47]\n" \
  "ds_read_b64_tr_b16 a[30:31], v28 offset:25120\n" \
  "v_accvgpr_read_b32 v6, a166\n" \
  "v_add_u32 v31, 86016, v9\n" \
  "v_mfma_f32_16x16x32_bf16 v[44:47], a[0:3], a[132:135], v[44:47]\n" \
  "s_waitcnt lgkmcnt(8)\n" \
  "v_mfma_f32_16x16x32_bf16 v[48:51], a[8:11], a[120:123], v[48:51]\n" \
  "ds_read_b64_tr_b16 a[32:33], v28 offset:64\n" \
  "s_add_u32 m0, s86, 114688\n" \
  "v_mfma_f32_16x16x32_bf16 v[48:51], a[12:15], a[136:139], v[48:51]\n" \
  "buffer_load_dwordx4 v0, s[56:59], s13 offen offset:0 lds\n" \
  "v_mfma_f32_16x16x32_bf16 v[52:55], a[12:15], a[140:143], v[52:55]\n" \
  "ds_read_b64_tr_b16 a[34:35], v28 offset:3648\n" \
  "v_mfma_f32_16x16x32_bf16 v[52:55], a[8:11], a[124:127], v[52:55]\n" \
  "v_mfma_f32_16x16x32_bf16 v[56:59], a[8:11], a[128:131], v[56:59]\n" \
  "ds_read_b64_tr_b16 a[36:37], v28 offset:7232\n" \
  "v_mfma_f32_16x16x32_bf16 v[56:59], a[12:15], a[144:147], v[56:59]\n" \
  "buffer_load_dwordx4 v1, s[56:59], s13 offen offset:1024 lds\n" \
  "v_mfma_f32_16x16x32_bf16 v[60:63], a[12:15], a[148:151], v[60:63]\n" \
  "ds_read_b64_tr_b16 a[38:39], v28 offset:10816\n" \
  "s_add_u32 s13, s13, 65536\n" \
  "v_mfma_f32_16x16x32_bf16 v[60:63], a[8:11], a[132:135], v[60:63]\n" \
  "s_waitcnt lgkmcnt(8)\n" \
  "v_mfma_f32_16x16x32_bf16 v[64:67], a[16:19], a[120:123], v[64:67]\n" \
  "ds_read_b64_tr_b16 a[40:41], v28 offset:14400\n" \
  "s_add_u32 m0, s86, 122880\n" \
  "v_mfma_f32_16x16x32_bf16 v[64:67], a[20:23], a[136:139], v[64:67]\n" \
  "buffer_load_dwordx4 v0, s[56:59], s13 offen offset:0 lds\n" \
  "v_mfma_f32_16x16x32_bf16 v[68:71], a[20:23], a[140:143], v[68:71]\n" \
  "ds_read_b64_tr_b16 a[42:43], v28 offset:17984\n" \
  "v_mfma_f32_16x16x32_bf16 v[68:71], a[16:19], a[124:127], v[68:71]\n" \
  "v_mfma_f32_16x16x32_bf16 v[72:75], a[16:19], a[128:131], v[72:75]\n" \
  "ds_read_b64_tr_b16 a[44:45], v28 offset:21568\n" \
  "v_mfma_f32_16x16x32_bf16 v[72:75], a[20:23], a[144:147], v[72:75]\n" \
  "buffer_load_dwordx4 v1, s[56:59], s13 offen offset:1024 lds\n" \
  "v_mfma_f32_16x16x32_bf16 v[76:79], a[20:23], a[148:151], v[76:79]\n" \
  "ds_read_b64_tr_b16 a[46:47], v28 offset:25152\n" \
  "s_add_u32 s13, s13, 65536\n" \
  "v_mfma_f32_16x16x32_bf16 v[76:79], a[16:19], a[132:135], v[76:79]\n" \
  "s_waitcnt vmcnt(13) lgkmcnt(8)\n" \
  "s_barrier\n" \
  "v_mfma_f32_16x16x32_bf16 v[80:83], a[24:27], a[120:123], v[80:83]\n" \
  "ds_read_b64_tr_b16 a[48:49], v31 offset:0\n" \
  "s_add_u32 m0, s86, 131072\n" \
  "v_mfma_f32_16x16x32_bf16 v[80:83], a[28:31], a[136:139], v[80:83]\n" \
  "buffer_load_dwordx4 v0, s[56:59], s13 offen offset:0 lds\n" \
  "v_mfma_f32_16x16x32_bf16 v[84:87], a[28:31], a[140:143], v[84:87]\n" \
  "ds_read_b64_tr_b16 a[50:51], v31 offset:3584\n" \
  "v_mfma_f32_16x16x32_bf16 v[84:87], a[24:27], a[124:127], v[84:87]\n" \
  "v_mfma_f32_16x16x32_bf16 v[88:91], a[24:27], a[128:131], v[88:91]\n" \
  "ds_read_b64_tr_b16 a[52:53], v31 offset:7168\n" \
  "v_mfma_f32_16x16x32_bf16 v[88:91], a[28:31], a[144:147], v[88:91]\n" \
  "buffer_load_dwordx4 v1, s[56:59], s13 offen offset:1024 lds\n" \
  "v_mfma_f32_16x16x32_bf16 v[92:95], a[28:31], a[148:151], v[92:95]\n" \
  "ds_read_b64_tr_b16 a[54:55], v31 offset:10752\n" \
  "s_add_u32 s13, s13, 65536\n" \
  "v_mfma_f32_16x16x32_bf16 v[92:95], a[24:27], a[132:135], v[92:95]\n" \
  "s_waitcnt lgkmcnt(8)\n" \
  "v_mfma_f32_16x16x32_bf16 v[96:99], a[32:35], a[120:123], v[96:99]\n" \
  "ds_read_b64_tr_b16 a[0:1], v31 offset:14336\n" \
  "s_add_u32 m0, s86, 139264\n" \
  "v_mfma_f32_16x16x32_bf16 v[96:99], a[36:39], a[136:139], v[96:99]\n" \
  "buffer_load_dwordx4 v0, s[56:59], s13 offen offset:0 lds\n" \
  "v_mfma_f32_16x16x32_bf16 v[100:103], a[36:39], a[140:143], v[100:103]\n" \
  "ds_read_b64_tr_b16 a[2:3], v31 offset:17920\n" \
  "v_mfma_f32_16x16x32_bf16 v[100:103], a[32:35], a[124:127], v[100:103]\n" \
  "v_mfma_f32_16x16x32_bf16 v[104:107], a[32:35], a[128:131], v[104:107]\n" \
  "ds_read_b64_tr_b16 a[4:5], v31 offset:21504\n" \
  "v_mfma_f32_16x16x32_bf16 v[104:107], a[36:39], a[144:147], v[104:107]\n" \
  "buffer_load_dwordx4 v1, s[56:59], s13 offen offset:1024 lds\n" \
  "v_mfma_f32_16x16x32_bf16 v[108:111], a[36:39], a[148:151], v[108:111]\n" \
  "ds_read_b64_tr_b16 a[6:7], v31 offset:25088\n" \
  "s_add_u32 s13, s13, 65536\n" \
  "v_mfma_f32_16x16x32_bf16 v[108:111], a[32:35], a[132:135], v[108:111]\n" \
  "s_waitcnt lgkmcnt(8)\n" \
  "v_mfma_f32_16x16x32_bf16 v[112:115], a[40:43], a[120:123], v[112:115]\n" \
  "ds_read_b64_tr_b16 a[8:9], v31 offset:32\n" \
  "s_add_u32 m0, s86, 147456\n" \
  "v_mfma_f32_16x16x32_bf16 v[112:115], a[44:47], a[136:139], v[112:115]\n" \
  "buffer_load_dwordx4 v0, s[56:59], s13 offen offset:0 lds\n" \
  "v_mfma_f32_16x16x32_bf16 v[116:119], a[44:47], a[140:143], v[116:119]\n" \
  "ds_read_b64_tr_b16 a[10:11], v31 offset:3616\n" \
  "v_mfma_f32_16x16x32_bf16 v[116:119], a[40:43], a[124:127], v[116:119]\n" \
  "v_mfma_f32_16x16x32_bf16 v[120:123], a[40:43], a[128:131], v[120:123]\n" \
  "ds_read_b64_tr_b16 a[12:13], v31 offset:7200\n" \
  "v_mfma_f32_16x16x32_bf16 v[120:123], a[44:47], a[144:147], v[120:123]\n" \
  "buffer_load_dwordx4 v1, s[56:59], s13 offen offset:1024 lds\n" \
  "v_mfma_f32_16x16x32_bf16 v[124:127], a[44:47], a[148:151], v[124:127]\n" \
  "ds_read_b64_tr_b16 a[14:15], v31 offset:10784\n" \
  "s_add_u32 s13, s13, 65536\n" \
  "v_mfma_f32_16x16x32_bf16 v[124:127], a[40:43], a[132:135], v[124:127]\n" \
  "s_waitcnt vmcnt(10) lgkmcnt(8)\n" \
  "v_mfma_f32_16x16x32_bf16 v[32:35], a[48:51], a[56:59], v[32:35]\n" \
  "ds_read_b64_tr_b16 a[16:17], v31 offset:14368\n" \
  "s_add_u32 m0, s86, 155648\n" \
  "v_mfma_f32_16x16x32_bf16 v[32:35], a[52:55], a[72:75], v[32:35]\n" \
  "buffer_load_dwordx4 v0, s[56:59], s13 offen offset:0 lds\n" \
  "v_mfma_f32_16x16x32_bf16 v[36:39], a[52:55], a[76:79], v[36:39]\n" \
  "ds_read_b64_tr_b16 a[18:19], v31 offset:17952\n" \
  "v_mfma_f32_16x16x32_bf16 v[36:39], a[48:51], a[60:63], v[36:39]\n" \
  "v_mfma_f32_16x16x32_bf16 v[40:43], a[48:51], a[64:67], v[40:43]\n" \
  "ds_read_b64_tr_b16 a[20:21], v31 offset:21536\n" \
  "v_mfma_f32_16x16x32_bf16 v[40:43], a[52:55], a[80:83], v[40:43]\n" \
  "buffer_load_dwordx4 v1, s[56:59], s13 offen offset:1024 lds\n" \
  "v_mfma_f32_16x16x32_bf16 v[44:47], a[52:55], a[84:87], v[44:47]\n" \
  "ds_read_b64_tr_b16 a[22:23], v31 offset:25120\n" \
  "s_add_u32 s13, s13, 65536\n" \
  "v_mfma_f32_16x16x32_bf16 v[44:47], a[48:51], a[68:71], v[44:47]\n" \
  "s_waitcnt lgkmcnt(8)\n" \
  "v_mfma_f32_16x16x32_bf16 v[48:51], a[0:3], a[56:59], v[48:51]\n" \
  "ds_read_b64_tr_b16 a[24:25], v31 offset:64\n" \
  "v_mfma_f32_16x16x32_bf16 v[48:51], a[4:7], a[72:75], v[48:51]\n" \
  "buffer_load_dwordx4 a[168:171], v0, s[56:59], s17 offen offset:0\n" \
  "v_mfma_f32_16x16x32_bf16 v[52:55], a[4:7], a[76:79], v[52:55]\n" \
  "ds_read_b64_tr_b16 a[26:27], v31 offset:3648\n" \
  "v_mfma_f32_16x16x32_bf16 v[52:55], a[0:3], a[60:63], v[52:55]\n" \
  "v_mfma_f32_16x16x32_bf16 v[56:59], a[0:3], a[64:67], v[56:59]\n" \
  "ds_read_b64_tr_b16 a[28:29], v31 offset:7232\n" \
  "v_mfma_f32_16x16x32_bf16 v[56:59], a[4:7], a[80:83], v[56:59]\n" \
  "buffer_load_dwordx4 a[172:175], v1, s[56:59], s17 offen offset:1024\n" \
  "v_mfma_f32_16x16x32_bf16 v[60:63], a[4:7], a[84:87], v[60:63]\n" \
  "ds_read_b64_tr_b16 a[30:31], v31 offset:10816\n" \
  "s_add_u32 s17, s17, 65536\n" \
  "v_mfma_f32_16x16x32_bf16 v[60:63], a[0:3], a[68:71], v[60:63]\n" \
  "s_waitcnt lgkmcnt(8)\n" \
  "v_mfma_f32_16x16x32_bf16 v[64:67], a[8:11], a[56:59], v[64:67]\n" \
  "ds_read_b64_tr_b16 a[32:33], v31 offset:14400\n" \
  "v_mfma_f32_16x16x32_bf16 v[64:67], a[12:15], a[72:75], v[64:67]\n" \
  "buffer_load_dwordx4 a[176:179], v0, s[56:59], s17 offen offset:0\n" \
  "v_mfma_f32_16x16x32_bf16 v[68:71], a[12:15], a[76:79], v[68:71]\n" \
  "ds_read_b64_tr_b16 a[34:35], v31 offset:17984\n" \
  "v_mfma_f32_16x16x32_bf16 v[68:71], a[8:11], a[60:63], v[68:71]\n" \
  "v_mfma_f32_16x16x32_bf16 v[72:75], a[8:11], a[64:67], v[72:75]\n" \
  "ds_read_b64_tr_b16 a[36:37], v31 offset:21568\n" \
  "v_mfma_f32_16x16x32_bf16 v[72:75], a[12:15], a[80:83], v[72:75]\n" \
  "buffer_load_dwordx4 a[180:183], v1, s[56:59], s17 offen offset:1024\n" \
  "v_mfma_f32_16x16x32_bf16 v[76:79], a[12:15], a[84:87], v[76:79]\n" \
  "ds_read_b64_tr_b16 a[38:39], v31 offset:25152\n" \
  "s_add_u32 s17, s17, 65536\n" \
  "v_mfma_f32_16x16x32_bf16 v[76:79], a[8:11], a[68:71], v[76:79]\n" \
  "s_waitcnt lgkmcnt(8)\n" \
  "v_mfma_f32_16x16x32_bf16 v[80:83], a[16:19], a[56:59], v[80:83]\n" \
  "ds_read_b64_tr_b16 a[40:41], v28 offset:96\n" \
  "v_mfma_f32_16x16x32_bf16 v[80:83], a[20:23], a[72:75], v[80:83]\n" \
  "buffer_load_dwordx4 a[184:187], v0, s[56:59], s17 offen offset:0\n" \
  "v_mfma_f32_16x16x32_bf16 v[84:87], a[20:23], a[76:79], v[84:87]\n" \
  "ds_read_b64_tr_b16 a[42:43], v28 offset:3680\n" \
  "v_mfma_f32_16x16x32_bf16 v[84:87], a[16:19], a[60:63], v[84:87]\n" \
  "v_mfma_f32_16x16x32_bf16 v[88:91], a[16:19], a[64:67], v[88:91]\n" \
  "ds_read_b64_tr_b16 a[44:45], v28 offset:7264\n" \
  "v_mfma_f32_16x16x32_bf16 v[88:91], a[20:23], a[80:83], v[88:91]\n" \
  "buffer_load_dwordx4 a[188:191], v1, s[56:59], s17 offen offset:1024\n" \
  "v_mfma_f32_16x16x32_bf16 v[92:95], a[20:23], a[84:87], v[92:95]\n" \
  "ds_read_b64_tr_b16 a[46:47], v28 offset:10848\n" \
  "s_add_u32 s17, s17, 65536\n" \
  "v_mfma_f32_16x16x32_bf16 v[92:95], a[16:19], a[68:71], v[92:95]\n" \
  "s_waitcnt lgkmcnt(8)\n" \
  "v_mfma_f32_16x16x32_bf16 v[96:99], a[24:27], a[56:59], v[96:99]\n" \
  "ds_read_b64_tr_b16 a[48:49], v28 offset:14432\n" \
  "v_mfma_f32_16x16x32_bf16 v[96:99], a[28:31], a[72:75], v[96:99]\n" \
  "buffer_load_dwordx4 a[192:195], v0, s[56:59], s17 offen offset:0\n" \
  "v_mfma_f32_16x16x32_bf16 v[100:103], a[28:31], a[76:79], v[100:103]\n" \
  "ds_read_b64_tr_b16 a[50:51], v28 offset:18016\n" \
  "v_mfma_f32_16x16x32_bf16 v[100:103], a[24:27], a[60:63], v[100:103]\n" \
  "v_mfma_f32_16x16x32_bf16 v[104:107], a[24:27], a[64:67], v[104:107]\n" \
  "ds_read_b64_tr_b16 a[52:53], v28 offset:21600\n" \
  "v_mfma_f32_16x16x32_bf16 v[104:107], a[28:31], a[80:83], v[104:107]\n" \
  "buffer_load_dwordx4 a[196:199], v1, s[56:59], s17 offen offset:1024\n" \
  "v_mfma_f32_16x16x32_bf16 v[108:111], a[28:31], a[84:87], v[108:111]\n" \
  "ds_read_b64_tr_b16 a[54:55], v28 offset:25184\n" \
  "s_mov_b32 s17, s90\n" \
  "v_mfma_f32_16x16x32_bf16 v[108:111], a[24:27], a[68:71], v[108:111]\n" \
  "s_waitcnt lgkmcnt(8)\n" \
  "v_mfma_f32_16x16x32_bf16 v[112:115], a[32:35], a[56:59], v[112:115]\n" \
  "ds_read_b64_tr_b16 a[0:1], v28 offset:128\n" \
  "v_mfma_f32_16x16x32_bf16 v[112:115], a[36:39], a[72:75], v[112:115]\n" \
  "buffer_load_dwordx4 a[200:203], v0, s[56:59], s17 offen offset:0\n" \
  "v_mfma_f32_16x16x32_bf16 v[116:119], a[36:39], a[76:79], v[116:119]\n" \
  "ds_read_b64_tr_b16 a[2:3], v28 offset:3712\n" \
  "v_mfma_f32_16x16x32_bf16 v[116:119], a[32:35], a[60:63], v[116:119]\n" \
  "v_mfma_f32_16x16x32_bf16 v[120:123], a[32:35], a[64:67], v[120:123]\n" \
  "ds_read_b64_tr_b16 a[4:5], v28 offset:7296\n" \
  "v_mfma_f32_16x16x32_bf16 v[120:123], a[36:39], a[80:83], v[120:123]\n" \
  "buffer_load_dwordx4 a[204:207], v1, s[56:59], s17 offen offset:1024\n" \
  "v_mfma_f32_16x16x32_bf16 v[124:127], a[36:39], a[84:87], v[124:127]\n" \
  "ds_read_b64_tr_b16 a[6:7], v28 offset:10880\n" \
  "s_add_u32 s17, s17, 65536\n" \
  "v_mfma_f32_16x16x32_bf16 v[124:127], a[32:35], a[68:71], v[124:127]\n" \
  "v_pk_mul_f32 v[16:17], v[32:33], v[32:33]\n" \
  "v_pk_mul_f32 v[18:19], v[34:35], v[34:35]\n" \
  "v_pk_mul_f32 v[20:21], v[36:37], v[36:37]\n" \
  "v_pk_mul_f32 v[22:23], v[38:39], v[38:39]\n" \
  "v_pk_fma_f32 v[16:17], v[16:17], v[24:25], v[26:27]\n" \
  "v_pk_fma_f32 v[18:19], v[18:19], v[24:25], v[26:27]\n" \
  "v_pk_fma_f32 v[20:21], v[20:21], v[24:25], v[26:27]\n" \
  "v_pk_fma_f32 v[22:23], v[22:23], v[24:25], v[26:27]\n" \
  "v_pk_mul_f32 v[16:17], v[16:17], v[32:33]\n" \
  "v_pk_mul_f32 v[18:19], v[18:19], v[34:35]\n" \
  "v_pk_mul_f32 v[20:21], v[20:21], v[36:37]\n" \
  "v_pk_mul_f32 v[22:23], v[22:23], v[38:39]\n" \
  "v_exp_f32 v16, v16\n" \
  "v_exp_f32 v17, v17\n" \
  "v_exp_f32 v18, v18\n" \
  "v_exp_f32 v19, v19\n" \
  "v_exp_f32 v20, v20\n" \
  "v_exp_f32 v21, v21\n" \
  "v_exp_f32 v22, v22\n" \
  "v_exp_f32 v23, v23\n" \
  "v_pk_mul_f32 v[48:49], v[48:49], v[32:33]\n" \
  "v_pk_mul_f32 v[50:51], v[50:51], v[34:35]\n" \
  "v_pk_mul_f32 v[52:53], v[52:53], v[36:37]\n" \
  "v_pk_mul_f32 v[54:55], v[54:55], v[38:39]\n" \
  "v_pk_add_f32 v[16:17], v[16:17], 1.0 op_sel_hi:[1,0]\n" \
  "v_pk_add_f32 v[18:19], v[18:19], 1.0 op_sel_hi:[1,0]\n" \
  "v_pk_add_f32 v[20:21], v[20:21], 1.0 op_sel_hi:[1,0]\n" \
  "v_pk_add_f32 v[22:23], v[22:23], 1.0 op_sel_hi:[1,0]\n" \
  "v_rcp_f32 v16, v16\n" \
  "v_rcp_f32 v17, v17\n" \
  "v_rcp_f32 v18, v18\n" \
  "v_rcp_f32 v19, v19\n" \
  "v_rcp_f32 v20, v20\n" \
  "v_rcp_f32 v21, v21\n" \
  "v_rcp_f32 v22, v22\n" \
  "v_rcp_f32 v23, v23\n" \
  "v_pk_mul_f32 v[48:49], v[48:49], v[16:17]\n" \
  "v_pk_mul_f32 v[50:51], v[50:51], v[18:19]\n" \
  "v_pk_mul_f32 v[52:53], v[52:53], v[20:21]\n" \
  "v_pk_mul_f32 v[54:55], v[54:55], v[22:23]\n" \
  "v_cvt_pk_bf16_f32 v32, v48, v49\n" \
  "v_cvt_pk_bf16_f32 v33, v50, v51\n" \
  "v_cvt_pk_bf16_f32 v34, v52, v53\n" \
  "v_cvt_pk_bf16_f32 v35, v54, v55\n" \
  "v_pk_mul_f32 v[16:17], v[40:41], v[40:41]\n" \
  "v_pk_mul_f32 v[18:19], v[42:43], v[42:43]\n" \
  "v_permlane16_swap_b32 v32, v34\n" \
  "v_permlane16_swap_b32 v33, v35\n" \
  "buffer_store_dwordx4 v[32:35], v10, s[44:47], s84 offen offset:0 sc1\n" \
  "v_pk_mul_f32 v[20:21], v[44:45], v[44:45]\n" \
  "v_pk_mul_f32 v[22:23], v[46:47], v[46:47]\n" \
  "v_pk_fma_f32 v[16:17], v[16:17], v[24:25], v[26:27]\n" \
  "v_pk_fma_f32 v[18:19], v[18:19], v[24:25], v[26:27]\n" \
  "v_pk_fma_f32 v[20:21], v[20:21], v[24:25], v[26:27]\n" \
  "v_pk_fma_f32 v[22:23], v[22:23], v[24:25], v[26:27]\n" \
  "v_pk_mul_f32 v[16:17], v[16:17], v[40:41]\n" \
  "v_pk_mul_f32 v[18:19], v[18:19], v[42:43]\n" \
  "v_pk_mul_f32 v[20:21], v[20:21], v[44:45]\n" \
  "v_pk_mul_f32 v[22:23], v[22:23], v[46:47]\n" \
  "v_exp_f32 v16, v16\n" \
  "v_exp_f32 v17, v17\n" \
  "v_exp_f32 v18, v18\n" \
  "v_exp_f32 v19, v19\n" \
  "v_exp_f32 v20, v20\n" \
  "v_exp_f32 v21, v21\n" \
  "v_exp_f32 v22, v22\n" \
  "v_exp_f32 v23, v23\n" \
  "v_pk_mul_f32 v[56:57], v[56:57], v[40:41]\n" \
  "v_pk_mul_f32 v[58:59], v[58:59], v[42:43]\n" \
  "v_pk_mul_f32 v[60:61], v[60:61], v[44:45]\n" \
  "v_pk_mul_f32 v[62:63], v[62:63], v[46:47]\n" \
  "v_pk_add_f32 v[16:17], v[16:17], 1.0 op_sel_hi:[1,0]\n" \
  "v_pk_add_f32 v[18:19], v[18:19], 1.0 op_sel_hi:[1,0]\n" \
  "v_pk_add_f32 v[20:21], v[20:21], 1.0 op_sel_hi:[1,0]\n" \
  "v_pk_add_f32 v[22:23], v[22:23], 1.0 op_sel_hi:[1,0]\n" \
  "v_rcp_f32 v16, v16\n" \
  "v_rcp_f32 v17, v17\n" \
  "v_rcp_f32 v18, v18\n" \
  "v_rcp_f32 v19, v19\n" \
  "v_rcp_f32 v20, v20\n" \
  "v_rcp_f32 v21, v21\n" \
  "v_rcp_f32 v22, v22\n" \
  "v_rcp_f32 v23, v23\n" \
  "v_pk_mul_f32 v[56:57], v[56:57], v[16:17]\n" \
  "v_pk_mul_f32 v[58:59], v[58:59], v[18:19]\n" \
  "v_pk_mul_f32 v[60:61], v[60:61], v[20:21]\n" \
  "v_pk_mul_f32 v[62:63], v[62:63], v[22:23]\n" \
  "v_cvt_pk_bf16_f32 v40, v56, v57\n" \
  "v_cvt_pk_bf16_f32 v41, v58, v59\n" \
  "v_cvt_pk_bf16_f32 v42, v60, v61\n" \
  "v_cvt_pk_bf16_f32 v43, v62, v63\n" \
  "v_pk_mul_f32 v[16:17], v[64:65], v[64:65]\n" \
  "v_pk_mul_f32 v[18:19], v[66:67], v[66:67]\n" \
  "v_permlane16_swap_b32 v40, v42\n" \
  "v_permlane16_swap_b32 v41, v43\n" \
  "buffer_store_dwordx4 v[40:43], v11, s[44:47], s84 offen offset:0 sc1\n" \
  "v_pk_mul_f32 v[20:21], v[68:69], v[68:69]\n" \
  "v_pk_mul_f32 v[22:23], v[70:71], v[70:71]\n" \
  "v_pk_fma_f32 v[16:17], v[16:17], v[24:25], v[26:27]\n" \
  "v_pk_fma_f32 v[18:19], v[18:19], v[24:25], v[26:27]\n" \
  "v_pk_fma_f32 v[20:21], v[20:21], v[24:25], v[26:27]\n" \
  "v_pk_fma_f32 v[22:23], v[22:23], v[24:25], v[26:27]\n" \
  "v_pk_mul_f32 v[16:17], v[16:17], v[64:65]\n" \
  "v_pk_mul_f32 v[18:19], v[18:19], v[66:67]\n" \
  "v_pk_mul_f32 v[20:21], v[20:21], v[68:69]\n" \
  "v_pk_mul_f32 v[22:23], v[22:23], v[70:71]\n" \
  "v_exp_f32 v16, v16\n" \
  "v_exp_f32 v17, v17\n" \
  "v_exp_f32 v18, v18\n" \
  "v_exp_f32 v19, v19\n" \
  "v_exp_f32 v20, v20\n" \
  "v_exp_f32 v21, v21\n" \
  "v_exp_f32 v22, v22\n" \
  "v_exp_f32 v23, v23\n" \
  "v_pk_mul_f32 v[80:81], v[80:81], v[64:65]\n" \
  "v_pk_mul_f32 v[82:83], v[82:83], v[66:67]\n" \
  "v_pk_mul_f32 v[84:85], v[84:85], v[68:69]\n" \
  "v_pk_mul_f32 v[86:87], v[86:87], v[70:71]\n" \
  "v_pk_add_f32 v[16:17], v[16:17], 1.0 op_sel_hi:[1,0]\n" \
  "v_pk_add_f32 v[18:19], v[18:19], 1.0 op_sel_hi:[1,0]\n" \
  "v_pk_add_f32 v[20:21], v[20:21], 1.0 op_sel_hi:[1,0]\n" \
  "v_pk_add_f32 v[22:23], v[22:23], 1.0 op_sel_hi:[1,0]\n" \
  "v_rcp_f32 v16, v16\n" \
  "v_rcp_f32 v17, v17\n" \
  "v_rcp_f32 v18, v18\n" \
  "v_rcp_f32 v19, v19\n" \
  "v_rcp_f32 v20, v20\n" \
  "v_rcp_f32 v21, v21\n" \
  "v_rcp_f32 v22, v22\n" \
  "v_rcp_f32 v23, v23\n" \
  "v_pk_mul_f32 v[80:81], v[80:81], v[16:17]\n" \
  "v_pk_mul_f32 v[82:83], v[82:83], v[18:19]\n" \
  "v_pk_mul_f32 v[84:85], v[84:85], v[20:21]\n" \
  "v_pk_mul_f32 v[86:87], v[86:87], v[22:23]\n" \
  "v_cvt_pk_bf16_f32 v64, v80, v81\n" \
  "v_cvt_pk_bf16_f32 v65, v82, v83\n" \
  "v_cvt_pk_bf16_f32 v66, v84, v85\n" \
  "v_cvt_pk_bf16_f32 v67, v86, v87\n" \
  "v_pk_mul_f32 v[16:17], v[72:73], v[72:73]\n" \
  "v_pk_mul_f32 v[18:19], v[74:75], v[74:75]\n" \
  "v_permlane16_swap_b32 v64, v66\n" \
  "v_permlane16_swap_b32 v65, v67\n" \
  "buffer_store_dwordx4 v[64:67], v10, s[44:47], s84 offen offset:1024 sc1\n" \
  "v_pk_mul_f32 v[20:21], v[76:77], v[76:77]\n" \
  "v_pk_mul_f32 v[22:23], v[78:79], v[78:79]\n" \
  "v_pk_fma_f32 v[16:17], v[16:17], v[24:25], v[26:27]\n" \
  "v_pk_fma_f32 v[18:19], v[18:19], v[24:25], v[26:27]\n" \
  "v_pk_fma_f32 v[20:21], v[20:21], v[24:25], v[26:27]\n" \
  "v_pk_fma_f32 v[22:23], v[22:23], v[24:25], v[26:27]\n" \
  "v_pk_mul_f32 v[16:17], v[16:17], v[72:73]\n" \
  "v_pk_mul_f32 v[18:19], v[18:19], v[74:75]\n" \
  "v_pk_mul_f32 v[20:21], v[20:21], v[76:77]\n" \
  "v_pk_mul_f32 v[22:23], v[22:23], v[78:79]\n" \
  "v_exp_f32 v16, v16\n" \
  "v_exp_f32 v17, v17\n" \
  "v_exp_f32 v18, v18\n" \
  "v_exp_f32 v19, v19\n" \
  "v_exp_f32 v20, v20\n" \
  "v_exp_f32 v21, v21\n" \
  "v_exp_f32 v22, v22\n" \
  "v_exp_f32 v23, v23\n" \
  "v_pk_mul_f32 v[88:89], v[88:89], v[72:73]\n" \
  "v_pk_mul_f32 v[90:91], v[90:91], v[74:75]\n" \
  "v_pk_mul_f32 v[92:93], v[92:93], v[76:77]\n" \
  "v_pk_mul_f32 v[94:95], v[94:95], v[78:79]\n" \
  "v_pk_add_f32 v[16:17], v[16:17], 1.0 op_sel_hi:[1,0]\n" \
  "v_pk_add_f32 v[18:19], v[18:19], 1.0 op_sel_hi:[1,0]\n" \
  "v_pk_add_f32 v[20:21], v[20:21], 1.0 op_sel_hi:[1,0]\n" \
  "v_pk_add_f32 v[22:23], v[22:23], 1.0 op_sel_hi:[1,0]\n" \
  "v_rcp_f32 v16, v16\n" \
  "v_rcp_f32 v17, v17\n" \
  "v_rcp_f32 v18, v18\n" \
  "v_rcp_f32 v19, v19\n" \
  "v_rcp_f32 v20, v20\n" \
  "v_rcp_f32 v21, v21\n" \
  "v_rcp_f32 v22, v22\n" \
  "v_rcp_f32 v23, v23\n" \
  "v_pk_mul_f32 v[88:89], v[88:89], v[16:17]\n" \
  "v_pk_mul_f32 v[90:91], v[90:91], v[18:19]\n" \
  "v_pk_mul_f32 v[92:93], v[92:93], v[20:21]\n" \
  "v_pk_mul_f32 v[94:95], v[94:95], v[22:23]\n" \
  "v_cvt_pk_bf16_f32 v72, v88, v89\n" \
  "v_cvt_pk_bf16_f32 v73, v90, v91\n" \
  "v_cvt_pk_bf16_f32 v74, v92, v93\n" \
  "v_cvt_pk_bf16_f32 v75, v94, v95\n" \
  "v_pk_mul_f32 v[16:17], v[96:97], v[96:97]\n" \
  "v_pk_mul_f32 v[18:19], v[98:99], v[98:99]\n" \
  "v_permlane16_swap_b32 v72, v74\n" \
  "v_permlane16_swap_b32 v73, v75\n" \
  "buffer_store_dwordx4 v[72:75], v11, s[44:47], s84 offen offset:1024 sc1\n" \
  "v_pk_mul_f32 v[20:21], v[100:101], v[100:101]\n" \
  "v_pk_mul_f32 v[22:23], v[102:103], v[102:103]\n" \
  "v_pk_fma_f32 v[16:17], v[16:17], v[24:25], v[26:27]\n" \
  "v_pk_fma_f32 v[18:19], v[18:19], v[24:25], v[26:27]\n" \
  "v_pk_fma_f32 v[20:21], v[20:21], v[24:25], v[26:27]\n" \
  "v_pk_fma_f32 v[22:23], v[22:23], v[24:25], v[26:27]\n" \
  "v_pk_mul_f32 v[16:17], v[16:17], v[96:97]\n" \
  "v_pk_mul_f32 v[18:19], v[18:19], v[98:99]\n" \
  "v_pk_mul_f32 v[20:21], v[20:21], v[100:101]\n" \
  "v_pk_mul_f32 v[22:23], v[22:23], v[102:103]\n" \
  "v_exp_f32 v16, v16\n" \
  "v_exp_f32 v17, v17\n" \
  "v_exp_f32 v18, v18\n" \
  "v_exp_f32 v19, v19\n" \
  "v_exp_f32 v20, v20\n" \
  "v_exp_f32 v21, v21\n" \
  "v_exp_f32 v22, v22\n" \
  "v_exp_f32 v23, v23\n" \
  "v_pk_mul_f32 v[112:113], v[112:113], v[96:97]\n" \
  "v_pk_mul_f32 v[114:115], v[114:115], v[98:99]\n" \
  "v_pk_mul_f32 v[116:117], v[116:117], v[100:101]\n" \
  "v_pk_mul_f32 v[118:119], v[118:119], v[102:103]\n" \
  "v_pk_add_f32 v[16:17], v[16:17], 1.0 op_sel_hi:[1,0]\n" \
  "v_pk_add_f32 v[18:19], v[18:19], 1.0 op_sel_hi:[1,0]\n" \
  "v_pk_add_f32 v[20:21], v[20:21], 1.0 op_sel_hi:[1,0]\n" \
  "v_pk_add_f32 v[22:23], v[22:23], 1.0 op_sel_hi:[1,0]\n" \
  "v_rcp_f32 v16, v16\n" \
  "v_rcp_f32 v17, v17\n" \
  "v_rcp_f32 v18, v18\n" \
  "v_rcp_f32 v19, v19\n" \
  "v_rcp_f32 v20, v20\n" \
  "v_rcp_f32 v21, v21\n" \
  "v_rcp_f32 v22, v22\n" \
  "v_rcp_f32 v23, v23\n" \
  "v_pk_mul_f32 v[112:113], v[112:113], v[16:17]\n" \
  "v_pk_mul_f32 v[114:115], v[114:115], v[18:19]\n" \
  "v_pk_mul_f32 v[116:117], v[116:117], v[20:21]\n" \
  "v_pk_mul_f32 v[118:119], v[118:119], v[22:23]\n" \
  "v_cvt_pk_bf16_f32 v96, v112, v113\n" \
  "v_cvt_pk_bf16_f32 v97, v114, v115\n" \
  "v_cvt_pk_bf16_f32 v98, v116, v117\n" \
  "v_cvt_pk_bf16_f32 v99, v118, v119\n" \
  "v_pk_mul_f32 v[16:17], v[104:105], v[104:105]\n" \
  "v_pk_mul_f32 v[18:19], v[106:107], v[106:107]\n" \
  "v_permlane16_swap_b32 v96, v98\n" \
  "v_permlane16_swap_b32 v97, v99\n" \
  "buffer_store_dwordx4 v[96:99], v10, s[44:47], s84 offen offset:2048 sc1\n" \
  "v_pk_mul_f32 v[20:21], v[108:109], v[108:109]\n" \
  "v_pk_mul_f32 v[22:23], v[110:111], v[110:111]\n" \
  "v_pk_fma_f32 v[16:17], v[16:17], v[24:25], v[26:27]\n" \
  "v_pk_fma_f32 v[18:19], v[18:19], v[24:25], v[26:27]\n" \
  "v_pk_fma_f32 v[20:21], v[20:21], v[24:25], v[26:27]\n" \
  "v_pk_fma_f32 v[22:23], v[22:23], v[24:25], v[26:27]\n" \
  "v_pk_mul_f32 v[16:17], v[16:17], v[104:105]\n" \
  "v_pk_mul_f32 v[18:19], v[18:19], v[106:107]\n" \
  "v_pk_mul_f32 v[20:21], v[20:21], v[108:109]\n" \
  "v_pk_mul_f32 v[22:23], v[22:23], v[110:111]\n" \
  "v_exp_f32 v16, v16\n" \
  "v_exp_f32 v17, v17\n" \
  "v_exp_f32 v18, v18\n" \
  "v_exp_f32 v19, v19\n" \
  "v_exp_f32 v20, v20\n" \
  "v_exp_f32 v21, v21\n" \
  "v_exp_f32 v22, v22\n" \
  "v_exp_f32 v23, v23\n" \
  "v_pk_mul_f32 v[120:121], v[120:121], v[104:105]\n" \
  "v_pk_mul_f32 v[122:123], v[122:123], v[106:107]\n" \
  "v_pk_mul_f32 v[124:125], v[124:125], v[108:109]\n" \
  "v_pk_mul_f32 v[126:127], v[126:127], v[110:111]\n" \
  "v_pk_add_f32 v[16:17], v[16:17], 1.0 op_sel_hi:[1,0]\n" \
  "v_pk_add_f32 v[18:19], v[18:19], 1.0 op_sel_hi:[1,0]\n" \
  "v_pk_add_f32 v[20:21], v[20:21], 1.0 op_sel_hi:[1,0]\n" \
  "v_pk_add_f32 v[22:23], v[22:23], 1.0 op_sel_hi:[1,0]\n" \
  "v_rcp_f32 v16, v16\n" \
  "v_rcp_f32 v17, v17\n" \
  "v_rcp_f32 v18, v18\n" \
  "v_rcp_f32 v19, v19\n" \
  "v_rcp_f32 v20, v20\n" \
  "v_rcp_f32 v21, v21\n" \
  "v_rcp_f32 v22, v22\n" \
  "v_rcp_f32 v23, v23\n" \
  "v_pk_mul_f32 v[120:121], v[120:121], v[16:17]\n" \
  "v_pk_mul_f32 v[122:123], v[122:123], v[18:19]\n" \
  "v_pk_mul_f32 v[124:125], v[124:125], v[20:21]\n" \
  "v_pk_mul_f32 v[126:127], v[126:127], v[22:23]\n" \
  "v_cvt_pk_bf16_f32 v104, v120, v121\n" \
  "v_cvt_pk_bf16_f32 v105, v122, v123\n" \
  "v_cvt_pk_bf16_f32 v106, v124, v125\n" \
  "v_cvt_pk_bf16_f32 v107, v126, v127\n" \
  "s_nop 1\n" \
  "v_permlane16_swap_b32 v104, v106\n" \
  "v_permlane16_swap_b32 v105, v107\n" \
  "buffer_store_dwordx4 v[104:107], v11, s[44:47], s84 offen offset:2048 sc1\n" \
  "s_waitcnt lgkmcnt(8)\n" \
  "v_mfma_f32_16x16x32_bf16 v[128:131], a[40:43], a[120:123], v[128:131]\n" \
  "ds_read_b64_tr_b16 a[8:9], v28 offset:14464\n" \
  "v_mfma_f32_16x16x32_bf16 v[128:131], a[44:47], a[136:139], v[128:131]\n" \
  "buffer_load_dwordx4 a[208:211], v0, s[56:59], s17 offen offset:0\n" \
  "v_mfma_f32_16x16x32_bf16 v[132:135], a[44:47], a[140:143], v[132:135]\n" \
  "ds_read_b64_tr_b16 a[10:11], v28 offset:18048\n" \
  "v_mfma_f32_16x16x32_bf16 v[132:135], a[40:43], a[124:127], v[132:135]\n" \
  "v_mfma_f32_16x16x32_bf16 v[136:139], a[40:43], a[128:131], v[136:139]\n" \
  "ds_read_b64_tr_b16 a[12:13], v28 offset:21632\n" \
  "v_mfma_f32_16x16x32_bf16 v[136:139], a[44:47], a[144:147], v[136:139]\n" \
  "buffer_load_dwordx4 a[212:215], v1, s[56:59], s17 offen offset:1024\n" \
  "v_mfma_f32_16x16x32_bf16 v[140:143], a[44:47], a[148:151], v[140:143]\n" \
  "ds_read_b64_tr_b16 a[14:15], v28 offset:25216\n" \
  "s_add_u32 s17, s17, 65536\n" \
  "v_mfma_f32_16x16x32_bf16 v[140:143], a[40:43], a[132:135], v[140:143]\n" \
  "s_waitcnt lgkmcnt(8)\n" \
  "v_mfma_f32_16x16x32_bf16 v[144:147], a[48:51], a[120:123], v[144:147]\n" \
  "ds_read_b64_tr_b16 a[16:17], v28 offset:160\n" \
  "v_mfma_f32_16x16x32_bf16 v[144:147], a[52:55], a[136:139], v[144:147]\n" \
  "buffer_load_dwordx4 a[216:219], v0, s[56:59], s17 offen offset:0\n" \
  "v_mfma_f32_16x16x32_bf16 v[148:151], a[52:55], a[140:143], v[148:151]\n" \
  "ds_read_b64_tr_b16 a[18:19], v28 offset:3744\n" \
  "v_mfma_f32_16x16x32_bf16 v[148:151], a[48:51], a[124:127], v[148:151]\n" \
  "v_mfma_f32_16x16x32_bf16 v[152:155], a[48:51], a[128:131], v[152:155]\n" \
  "ds_read_b64_tr_b16 a[20:21], v28 offset:7328\n" \
  "v_mfma_f32_16x16x32_bf16 v[152:155], a[52:55], a[144:147], v[152:155]\n" \
  "buffer_load_dwordx4 a[220:223], v1, s[56:59], s17 offen offset:1024\n" \
  "v_mfma_f32_16x16x32_bf16 v[156:159], a[52:55], a[148:151], v[156:159]\n" \
  "ds_read_b64_tr_b16 a[22:23], v28 offset:10912\n" \
  "s_add_u32 s17, s17, 65536\n" \
  "v_mfma_f32_16x16x32_bf16 v[156:159], a[48:51], a[132:135], v[156:159]\n" \
  "s_waitcnt lgkmcnt(8)\n" \
  "v_mfma_f32_16x16x32_bf16 v[160:163], a[0:3], a[120:123], v[160:163]\n" \
  "ds_read_b64_tr_b16 a[24:25], v28 offset:14496\n" \
  "s_add_u32 m0, s86, 0\n" \
  "v_mfma_f32_16x16x32_bf16 v[160:163], a[4:7], a[136:139], v[160:163]\n" \
  "buffer_load_dwordx4 v0, s[56:59], s13 offen offset:0 lds\n" \
  "v_mfma_f32_16x16x32_bf16 v[164:167], a[4:7], a[140:143], v[164:167]\n" \
  "ds_read_b64_tr_b16 a[26:27], v28 offset:18080\n" \
  "v_mfma_f32_16x16x32_bf16 v[164:167], a[0:3], a[124:127], v[164:167]\n" \
  "v_mfma_f32_16x16x32_bf16 v[168:171], a[0:3], a[128:131], v[168:171]\n" \
  "ds_read_b64_tr_b16 a[28:29], v28 offset:21664\n" \
  "v_mfma_f32_16x16x32_bf16 v[168:171], a[4:7], a[144:147], v[168:171]\n" \
  "buffer_load_dwordx4 v1, s[56:59], s13 offen offset:1024 lds\n" \
  "v_mfma_f32_16x16x32_bf16 v[172:175], a[4:7], a[148:151], v[172:175]\n" \
  "ds_read_b64_tr_b16 a[30:31], v28 offset:25248\n" \
  "s_add_u32 s13, s13, 65536\n" \
  "v_mfma_f32_16x16x32_bf16 v[172:175], a[0:3], a[132:135], v[172:175]\n" \
  "s_waitcnt lgkmcnt(8)\n" \
  "v_mfma_f32_16x16x32_bf16 v[176:179], a[8:11], a[120:123], v[176:179]\n" \
  "ds_read_b64_tr_b16 a[32:33], v28 offset:192\n" \
  "v_mfma_f32_16x16x32_bf16 v[176:179], a[12:15], a[136:139], v[176:179]\n" \
  "v_mfma_f32_16x16x32_bf16 v[180:183], a[12:15], a[140:143], v[180:183]\n" \
  "ds_read_b64_tr_b16 a[34:35], v28 offset:3776\n" \
  "s_add_u32 m0, s86, 8192\n" \
  "v_mfma_f32_16x16x32_bf16 v[180:183], a[8:11], a[124:127], v[180:183]\n" \
  "buffer_load_dwordx4 v0, s[56:59], s13 offen offset:0 lds\n" \
  "v_mfma_f32_16x16x32_bf16 v[184:187], a[8:11], a[128:131], v[184:187]\n" \
  "ds_read_b64_tr_b16 a[36:37], v28 offset:7360\n" \
  "v_mfma_f32_16x16x32_bf16 v[184:187], a[12:15], a[144:147], v[184:187]\n" \
  "v_mfma_f32_16x16x32_bf16 v[188:191], a[12:15], a[148:151], v[188:191]\n" \
  "ds_read_b64_tr_b16 a[38:39], v28 offset:10944\n" \
  "v_mfma_f32_16x16x32_bf16 v[188:191], a[8:11], a[132:135], v[188:191]\n" \
  "s_waitcnt lgkmcnt(8)\n" \
  "v_mfma_f32_16x16x32_bf16 v[192:195], a[16:19], a[120:123], v[192:195]\n" \
  "ds_read_b64_tr_b16 a[40:41], v28 offset:14528\n" \
  "v_mfma_f32_16x16x32_bf16 v[192:195], a[20:23], a[136:139], v[192:195]\n" \
  "v_mfma_f32_16x16x32_bf16 v[196:199], a[20:23], a[140:143], v[196:199]\n" \
  "ds_read_b64_tr_b16 a[42:43], v28 offset:18112\n" \
  "v_mfma_f32_16x16x32_bf16 v[196:199], a[16:19], a[124:127], v[196:199]\n" \
  "buffer_load_dwordx4 v1, s[56:59], s13 offen offset:1024 lds\n" \
  "v_mfma_f32_16x16x32_bf16 v[200:203], a[16:19], a[128:131], v[200:203]\n" \
  "ds_read_b64_tr_b16 a[44:45], v28 offset:21696\n" \
  "s_add_u32 s13, s13, 65536\n" \
  "v_mfma_f32_16x16x32_bf16 v[200:203], a[20:23], a[144:147], v[200:203]\n" \
  "v_mfma_f32_16x16x32_bf16 v[204:207], a[20:23], a[148:151], v[204:207]\n" \
  "ds_read_b64_tr_b16 a[46:47], v28 offset:25280\n" \
  "v_mfma_f32_16x16x32_bf16 v[204:207], a[16:19], a[132:135], v[204:207]\n" \
  "s_waitcnt lgkmcnt(8)\n" \
  "v_mfma_f32_16x16x32_bf16 v[208:211], a[24:27], a[120:123], v[208:211]\n" \
  "ds_read_b64_tr_b16 a[48:49], v31 offset:96\n" \
  "v_mfma_f32_16x16x32_bf16 v[208:211], a[28:31], a[136:139], v[208:211]\n" \
  "v_mfma_f32_16x16x32_bf16 v[212:215], a[28:31], a[140:143], v[212:215]\n" \
  "ds_read_b64_tr_b16 a[50:51], v31 offset:3680\n" \
  "s_add_u32 m0, s86, 16384\n" \
  "v_mfma_f32_16x16x32_bf16 v[212:215], a[24:27], a[124:127], v[212:215]\n" \
  "buffer_load_dwordx4 v0, s[56:59], s13 offen offset:0 lds\n" \
  "v_mfma_f32_16x16x32_bf16 v[216:219], a[24:27], a[128:131], v[216:219]\n" \
  "ds_read_b64_tr_b16 a[52:53], v31 offset:7264\n" \
  "v_mfma_f32_16x16x32_bf16 v[216:219], a[28:31], a[144:147], v[216:219]\n" \
  "v_mfma_f32_16x16x32_bf16 v[220:223], a[28:31], a[148:151], v[220:223]\n" \
  "ds_read_b64_tr_b16 a[54:55], v31 offset:10848\n" \
  "v_mfma_f32_16x16x32_bf16 v[220:223], a[24:27], a[132:135], v[220:223]\n" \
  "s_waitcnt lgkmcnt(8)\n" \
  "v_mfma_f32_16x16x32_bf16 v[224:227], a[32:35], a[120:123], v[224:227]\n" \
  "ds_read_b64_tr_b16 a[0:1], v31 offset:14432\n" \
  "v_mfma_f32_16x16x32_bf16 v[224:227], a[36:39], a[136:139], v[224:227]\n" \
  "v_mfma_f32_16x16x32_bf16 v[228:231], a[36:39], a[140:143], v[228:231]\n" \
  "ds_read_b64_tr_b16 a[2:3], v31 offset:18016\n" \
  "v_mfma_f32_16x16x32_bf16 v[228:231], a[32:35], a[124:127], v[228:231]\n" \
  "buffer_load_dwordx4 v1, s[56:59], s13 offen offset:1024 lds\n" \
  "v_mfma_f32_16x16x32_bf16 v[232:235], a[32:35], a[128:131], v[232:235]\n" \
  "ds_read_b64_tr_b16 a[4:5], v31 offset:21600\n" \
  "s_add_u32 s13, s13, 65536\n" \
  "v_mfma_f32_16x16x32_bf16 v[232:235], a[36:39], a[144:147], v[232:235]\n" \
  "v_mfma_f32_16x16x32_bf16 v[236:239], a[36:39], a[148:151], v[236:239]\n" \
  "ds_read_b64_tr_b16 a[6:7], v31 offset:25184\n" \
  "v_mfma_f32_16x16x32_bf16 v[236:239], a[32:35], a[132:135], v[236:239]\n" \
  "s_waitcnt lgkmcnt(8)\n" \
  "v_mfma_f32_16x16x32_bf16 v[240:243], a[40:43], a[120:123], v[240:243]\n" \
  "ds_read_b64_tr_b16 a[8:9], v31 offset:128\n" \
  "v_mfma_f32_16x16x32_bf16 v[240:243], a[44:47], a[136:139], v[240:243]\n" \
  "v_mfma_f32_16x16x32_bf16 v[244:247], a[44:47], a[140:143], v[244:247]\n" \
  "ds_read_b64_tr_b16 a[10:11], v31 offset:3712\n" \
  "s_add_u32 m0, s86, 24576\n" \
  "v_mfma_f32_16x16x32_bf16 v[244:247], a[40:43], a[124:127], v[244:247]\n" \
  "buffer_load_dwordx4 v0, s[56:59], s13 offen offset:0 lds\n" \
  "v_mfma_f32_16x16x32_bf16 v[248:251], a[40:43], a[128:131], v[248:251]\n" \
  "ds_read_b64_tr_b16 a[12:13], v31 offset:7296\n" \
  "v_mfma_f32_16x16x32_bf16 v[248:251], a[44:47], a[144:147], v[248:251]\n" \
  "v_mfma_f32_16x16x32_bf16 v[252:255], a[44:47], a[148:151], v[252:255]\n" \
  "ds_read_b64_tr_b16 a[14:15], v31 offset:10880\n" \
  "v_mfma_f32_16x16x32_bf16 v[252:255], a[40:43], a[132:135], v[252:255]\n" \
  "s_waitcnt lgkmcnt(8)\n" \
  "v_mfma_f32_16x16x32_bf16 v[128:131], a[48:51], a[56:59], v[128:131]\n" \
  "ds_read_b64_tr_b16 a[16:17], v31 offset:14464\n" \
  "v_mfma_f32_16x16x32_bf16 v[128:131], a[52:55], a[72:75], v[128:131]\n" \
  "v_mfma_f32_16x16x32_bf16 v[132:135], a[52:55], a[76:79], v[132:135]\n" \
  "ds_read_b64_tr_b16 a[18:19], v31 offset:18048\n" \
  "v_mfma_f32_16x16x32_bf16 v[132:135], a[48:51], a[60:63], v[132:135]\n" \
  "buffer_load_dwordx4 v1, s[56:59], s13 offen offset:1024 lds\n" \
  "v_mfma_f32_16x16x32_bf16 v[136:139], a[48:51], a[64:67], v[136:139]\n" \
  "ds_read_b64_tr_b16 a[20:21], v31 offset:21632\n" \
  "s_add_u32 s13, s13, 65536\n" \
  "v_mfma_f32_16x16x32_bf16 v[136:139], a[52:55], a[80:83], v[136:139]\n" \
  "v_mfma_f32_16x16x32_bf16 v[140:143], a[52:55], a[84:87], v[140:143]\n" \
  "ds_read_b64_tr_b16 a[22:23], v31 offset:25216\n" \
  "v_mfma_f32_16x16x32_bf16 v[140:143], a[48:51], a[68:71], v[140:143]\n" \
  "s_waitcnt lgkmcnt(8)\n" \
  "v_mfma_f32_16x16x32_bf16 v[144:147], a[0:3], a[56:59], v[144:147]\n" \
  "ds_read_b64_tr_b16 a[24:25], v31 offset:160\n" \
  "v_mfma_f32_16x16x32_bf16 v[144:147], a[4:7], a[72:75], v[144:147]\n" \
  "v_mfma_f32_16x16x32_bf16 v[148:151], a[4:7], a[76:79], v[148:151]\n" \
  "ds_read_b64_tr_b16 a[26:27], v31 offset:3744\n" \
  "s_add_u32 m0, s86, 32768\n" \
  "v_mfma_f32_16x16x32_bf16 v[148:151], a[0:3], a[60:63], v[148:151]\n" \
  "buffer_load_dwordx4 v0, s[56:59], s13 offen offset:0 lds\n" \
  "v_mfma_f32_16x16x32_bf16 v[152:155], a[0:3], a[64:67], v[152:155]\n" \
  "ds_read_b64_tr_b16 a[28:29], v31 offset:7328\n" \
  "v_mfma_f32_16x16x32_bf16 v[152:155], a[4:7], a[80:83], v[152:155]\n" \
  "v_mfma_f32_16x16x32_bf16 v[156:159], a[4:7], a[84:87], v[156:159]\n" \
  "ds_read_b64_tr_b16 a[30:31], v31 offset:10912\n" \
  "v_mfma_f32_16x16x32_bf16 v[156:159], a[0:3], a[68:71], v[156:159]\n" \
  "s_waitcnt lgkmcnt(8)\n" \
  "v_mfma_f32_16x16x32_bf16 v[160:163], a[8:11], a[56:59], v[160:163]\n" \
  "ds_read_b64_tr_b16 a[32:33], v31 offset:14496\n" \
  "v_mfma_f32_16x16x32_bf16 v[160:163], a[12:15], a[72:75], v[160:163]\n" \
  "v_mfma_f32_16x16x32_bf16 v[164:167], a[12:15], a[76:79], v[164:167]\n" \
  "ds_read_b64_tr_b16 a[34:35], v31 offset:18080\n" \
  "v_mfma_f32_16x16x32_bf16 v[164:167], a[8:11], a[60:63], v[164:167]\n" \
  "buffer_load_dwordx4 v1, s[56:59], s13 offen offset:1024 lds\n" \
  "v_mfma_f32_16x16x32_bf16 v[168:171], a[8:11], a[64:67], v[168:171]\n" \
  "ds_read_b64_tr_b16 a[36:37], v31 offset:21664\n" \
  "s_add_u32 s13, s13, 65536\n" \
  "v_mfma_f32_16x16x32_bf16 v[168:171], a[12:15], a[80:83], v[168:171]\n" \
  "v_mfma_f32_16x16x32_bf16 v[172:175], a[12:15], a[84:87], v[172:175]\n" \
  "ds_read_b64_tr_b16 a[38:39], v31 offset:25248\n" \
  "v_mfma_f32_16x16x32_bf16 v[172:175], a[8:11], a[68:71], v[172:175]\n" \
  "s_waitcnt lgkmcnt(8)\n" \
  "v_mfma_f32_16x16x32_bf16 v[176:179], a[16:19], a[56:59], v[176:179]\n" \
  "ds_read_b64_tr_b16 a[40:41], v31 offset:192\n" \
  "v_mfma_f32_16x16x32_bf16 v[176:179], a[20:23], a[72:75], v[176:179]\n" \
  "v_mfma_f32_16x16x32_bf16 v[180:183], a[20:23], a[76:79], v[180:183]\n" \
  "ds_read_b64_tr_b16 a[42:43], v31 offset:3776\n" \
  "s_add_u32 m0, s86, 40960\n" \
  "v_mfma_f32_16x16x32_bf16 v[180:183], a[16:19], a[60:63], v[180:183]\n" \
  "buffer_load_dwordx4 v0, s[56:59], s13 offen offset:0 lds\n" \
  "v_mfma_f32_16x16x32_bf16 v[184:187], a[16:19], a[64:67], v[184:187]\n" \
  "ds_read_b64_tr_b16 a[44:45], v31 offset:7360\n" \
  "v_mfma_f32_16x16x32_bf16 v[184:187], a[20:23], a[80:83], v[184:187]\n" \
  "v_mfma_f32_16x16x32_bf16 v[188:191], a[20:23], a[84:87], v[188:191]\n" \
  "ds_read_b64_tr_b16 a[46:47], v31 offset:10944\n" \
  "v_mfma_f32_16x16x32_bf16 v[188:191], a[16:19], a[68:71], v[188:191]\n" \
  "s_waitcnt lgkmcnt(8)\n" \
  "v_mfma_f32_16x16x32_bf16 v[192:195], a[24:27], a[56:59], v[192:195]\n" \
  "ds_read_b64_tr_b16 a[48:49], v31 offset:14528\n" \
  "v_mfma_f32_16x16x32_bf16 v[192:195], a[28:31], a[72:75], v[192:195]\n" \
  "v_mfma_f32_16x16x32_bf16 v[196:199], a[28:31], a[76:79], v[196:199]\n" \
  "ds_read_b64_tr_b16 a[50:51], v31 offset:18112\n" \
  "v_mfma_f32_16x16x32_bf16 v[196:199], a[24:27], a[60:63], v[196:199]\n" \
  "buffer_load_dwordx4 v1, s[56:59], s13 offen offset:1024 lds\n" \
  "v_mfma_f32_16x16x32_bf16 v[200:203], a[24:27], a[64:67], v[200:203]\n" \
  "ds_read_b64_tr_b16 a[52:53], v31 offset:21696\n" \
  "s_add_u32 s13, s13, 65536\n" \
  "v_mfma_f32_16x16x32_bf16 v[200:203], a[28:31], a[80:83], v[200:203]\n" \
  "v_mfma_f32_16x16x32_bf16 v[204:207], a[28:31], a[84:87], v[204:207]\n" \
  "ds_read_b64_tr_b16 a[54:55], v31 offset:25280\n" \
  "v_mfma_f32_16x16x32_bf16 v[204:207], a[24:27], a[68:71], v[204:207]\n" \
  "s_waitcnt lgkmcnt(8)\n" \
  "v_mfma_f32_16x16x32_bf16 v[208:211], a[32:35], a[56:59], v[208:211]\n" \
  "v_mfma_f32_16x16x32_bf16 v[208:211], a[36:39], a[72:75], v[208:211]\n" \
  "v_mfma_f32_16x16x32_bf16 v[212:215], a[36:39], a[76:79], v[212:215]\n" \
  "s_add_u32 m0, s86, 49152\n" \
  "v_mfma_f32_16x16x32_bf16 v[212:215], a[32:35], a[60:63], v[212:215]\n" \
  "buffer_load_dwordx4 v0, s[56:59], s13 offen offset:0 lds\n" \
  "v_mfma_f32_16x16x32_bf16 v[216:219], a[32:35], a[64:67], v[216:219]\n" \
  "v_mfma_f32_16x16x32_bf16 v[216:219], a[36:39], a[80:83], v[216:219]\n" \
  "v_mfma_f32_16x16x32_bf16 v[220:223], a[36:39], a[84:87], v[220:223]\n" \
  "v_mfma_f32_16x16x32_bf16 v[220:223], a[32:35], a[68:71], v[220:223]\n" \
  "s_waitcnt lgkmcnt(4)\n" \
  "v_mfma_f32_16x16x32_bf16 v[224:227], a[40:43], a[56:59], v[224:227]\n" \
  "v_mfma_f32_16x16x32_bf16 v[224:227], a[44:47], a[72:75], v[224:227]\n" \
  "v_mfma_f32_16x16x32_bf16 v[228:231], a[44:47], a[76:79], v[228:231]\n" \
  "v_mfma_f32_16x16x32_bf16 v[228:231], a[40:43], a[60:63], v[228:231]\n" \
  "buffer_load_dwordx4 v1, s[56:59], s13 offen offset:1024 lds\n" \
  "v_mfma_f32_16x16x32_bf16 v[232:235], a[40:43], a[64:67], v[232:235]\n" \
  "s_add_u32 s13, s13, 65536\n" \
  "v_mfma_f32_16x16x32_bf16 v[232:235], a[44:47], a[80:83], v[232:235]\n" \
  "v_mfma_f32_16x16x32_bf16 v[236:239], a[44:47], a[84:87], v[236:239]\n" \
  "v_mfma_f32_16x16x32_bf16 v[236:239], a[40:43], a[68:71], v[236:239]\n" \
  "s_waitcnt lgkmcnt(0)\n" \
  "v_mfma_f32_16x16x32_bf16 v[240:243], a[48:51], a[56:59], v[240:243]\n" \
  "v_mfma_f32_16x16x32_bf16 v[240:243], a[52:55], a[72:75], v[240:243]\n" \
  "v_mfma_f32_16x16x32_bf16 v[244:247], a[52:55], a[76:79], v[244:247]\n" \
  "v_mfma_f32_16x16x32_bf16 v[244:247], a[48:51], a[60:63], v[244:247]\n" \
  "v_mfma_f32_16x16x32_bf16 v[248:251], a[48:51], a[64:67], v[248:251]\n" \
  "v_mfma_f32_16x16x32_bf16 v[248:251], a[52:55], a[80:83], v[248:251]\n" \
  "v_mfma_f32_16x16x32_bf16 v[252:255], a[52:55], a[84:87], v[252:255]\n" \
  "v_mfma_f32_16x16x32_bf16 v[252:255], a[48:51], a[68:71], v[252:255]\n" \
  "s_waitcnt vmcnt(18)\n" \
  "s_barrier\n" \
  "buffer_store_dword v24, v3, s[68:71], 0 offen sc1\n" \
  "v_pk_mul_f32 v[16:17], v[128:129], v[128:129]\n" \
  "v_pk_mul_f32 v[18:19], v[130:131], v[130:131]\n" \
  "v_pk_mul_f32 v[20:21], v[132:133], v[132:133]\n" \
  "v_pk_mul_f32 v[22:23], v[134:135], v[134:135]\n" \
  "v_pk_fma_f32 v[16:17], v[16:17], v[24:25], v[26:27]\n" \
  "v_pk_fma_f32 v[18:19], v[18:19], v[24:25], v[26:27]\n" \
  "v_pk_fma_f32 v[20:21], v[20:21], v[24:25], v[26:27]\n" \
  "v_pk_fma_f32 v[22:23], v[22:23], v[24:25], v[26:27]\n" \
  "v_pk_mul_f32 v[16:17], v[16:17], v[128:129]\n" \
  "v_pk_mul_f32 v[18:19], v[18:19], v[130:131]\n" \
  "v_pk_mul_f32 v[20:21], v[20:21], v[132:133]\n" \
  "v_pk_mul_f32 v[22:23], v[22:23], v[134:135]\n" \
  "v_exp_f32 v16, v16\n" \
  "v_exp_f32 v17, v17\n" \
  "v_exp_f32 v18, v18\n" \
  "v_exp_f32 v19, v19\n" \
  "v_exp_f32 v20, v20\n" \
  "v_exp_f32 v21, v21\n" \
  "v_exp_f32 v22, v22\n" \
  "v_exp_f32 v23, v23\n" \
  "v_pk_mul_f32 v[144:145], v[144:145], v[128:129]\n" \
  "v_pk_mul_f32 v[146:147], v[146:147], v[130:131]\n" \
  "v_pk_mul_f32 v[148:149], v[148:149], v[132:133]\n" \
  "v_pk_mul_f32 v[150:151], v[150:151], v[134:135]\n" \
  "v_pk_add_f32 v[16:17], v[16:17], 1.0 op_sel_hi:[1,0]\n" \
  "v_pk_add_f32 v[18:19], v[18:19], 1.0 op_sel_hi:[1,0]\n" \
  "v_pk_add_f32 v[20:21], v[20:21], 1.0 op_sel_hi:[1,0]\n" \
  "v_pk_add_f32 v[22:23], v[22:23], 1.0 op_sel_hi:[1,0]\n" \
  "v_rcp_f32 v16, v16\n" \
  "v_rcp_f32 v17, v17\n" \
  "v_rcp_f32 v18, v18\n" \
  "v_rcp_f32 v19, v19\n" \
  "v_rcp_f32 v20, v20\n" \
  "v_rcp_f32 v21, v21\n" \
  "v_rcp_f32 v22, v22\n" \
  "v_rcp_f32 v23, v23\n" \
  "v_pk_mul_f32 v[144:145], v[144:145], v[16:17]\n" \
  "v_pk_mul_f32 v[146:147], v[146:147], v[18:19]\n" \
  "v_pk_mul_f32 v[148:149], v[148:149], v[20:21]\n" \
  "v_pk_mul_f32 v[150:151], v[150:151], v[22:23]\n" \
  "v_cvt_pk_bf16_f32 v128, v144, v145\n" \
  "v_cvt_pk_bf16_f32 v129, v146, v147\n" \
  "v_cvt_pk_bf16_f32 v130, v148, v149\n" \
  "v_cvt_pk_bf16_f32 v131, v150, v151\n" \
  "v_pk_mul_f32 v[16:17], v[136:137], v[136:137]\n" \
  "v_pk_mul_f32 v[18:19], v[138:139], v[138:139]\n" \
  "v_permlane16_swap_b32 v128, v130\n" \
  "v_permlane16_swap_b32 v129, v131\n" \
  "buffer_store_dwordx4 v[128:131], v10, s[44:47], s85 offen offset:0 sc1\n" \
  "v_pk_mul_f32 v[20:21], v[140:141], v[140:141]\n" \
  "v_pk_mul_f32 v[22:23], v[142:143], v[142:143]\n" \
  "v_pk_fma_f32 v[16:17], v[16:17], v[24:25], v[26:27]\n" \
  "v_pk_fma_f32 v[18:19], v[18:19], v[24:25], v[26:27]\n" \
  "v_pk_fma_f32 v[20:21], v[20:21], v[24:25], v[26:27]\n" \
  "v_pk_fma_f32 v[22:23], v[22:23], v[24:25], v[26:27]\n" \
  "v_pk_mul_f32 v[16:17], v[16:17], v[136:137]\n" \
  "v_pk_mul_f32 v[18:19], v[18:19], v[138:139]\n" \
  "v_pk_mul_f32 v[20:21], v[20:21], v[140:141]\n" \
  "v_pk_mul_f32 v[22:23], v[22:23], v[142:143]\n" \
  "v_exp_f32 v16, v16\n" \
  "v_exp_f32 v17, v17\n" \
  "v_exp_f32 v18, v18\n" \
  "v_exp_f32 v19, v19\n" \
  "v_exp_f32 v20, v20\n" \
  "v_exp_f32 v21, v21\n" \
  "v_exp_f32 v22, v22\n" \
  "v_exp_f32 v23, v23\n" \
  "v_pk_mul_f32 v[152:153], v[152:153], v[136:137]\n" \
  "v_pk_mul_f32 v[154:155], v[154:155], v[138:139]\n" \
  "v_pk_mul_f32 v[156:157], v[156:157], v[140:141]\n" \
  "v_pk_mul_f32 v[158:159], v[158:159], v[142:143]\n" \
  "v_pk_add_f32 v[16:17], v[16:17], 1.0 op_sel_hi:[1,0]\n" \
  "v_pk_add_f32 v[18:19], v[18:19], 1.0 op_sel_hi:[1,0]\n" \
  "v_pk_add_f32 v[20:21], v[20:21], 1.0 op_sel_hi:[1,0]\n" \
  "v_pk_add_f32 v[22:23], v[22:23], 1.0 op_sel_hi:[1,0]\n" \
  "v_rcp_f32 v16, v16\n" \
  "v_rcp_f32 v17, v17\n" \
  "v_rcp_f32 v18, v18\n" \
  "v_rcp_f32 v19, v19\n" \
  "v_rcp_f32 v20, v20\n" \
  "v_rcp_f32 v21, v21\n" \
  "v_rcp_f32 v22, v22\n" \
  "v_rcp_f32 v23, v23\n" \
  "v_pk_mul_f32 v[152:153], v[152:153], v[16:17]\n" \
  "v_pk_mul_f32 v[154:155], v[154:155], v[18:19]\n" \
  "v_pk_mul_f32 v[156:157], v[156:157], v[20:21]\n" \
  "v_pk_mul_f32 v[158:159], v[158:159], v[22:23]\n" \
  "v_cvt_pk_bf16_f32 v136, v152, v153\n" \
  "v_cvt_pk_bf16_f32 v137, v154, v155\n" \
  "v_cvt_pk_bf16_f32 v138, v156, v157\n" \
  "v_cvt_pk_bf16_f32 v139, v158, v159\n" \
  "s_nop 1\n" \
  "v_permlane16_swap_b32 v136, v138\n" \
  "v_permlane16_swap_b32 v137, v139\n" \
  "buffer_store_dwordx4 v[136:139], v11, s[44:47], s85 offen offset:0 sc1\n" \
  "v_pk_mul_f32 v[16:17], v[160:161], v[160:161]\n" \
  "v_pk_mul_f32 v[18:19], v[162:163], v[162:163]\n" \
  "v_pk_mul_f32 v[20:21], v[164:165], v[164:165]\n" \
  "v_pk_mul_f32 v[22:23], v[166:167], v[166:167]\n" \
  "v_pk_fma_f32 v[16:17], v[16:17], v[24:25], v[26:27]\n" \
  "v_pk_fma_f32 v[18:19], v[18:19], v[24:25], v[26:27]\n" \
  "v_pk_fma_f32 v[20:21], v[20:21], v[24:25], v[26:27]\n" \
  "v_pk_fma_f32 v[22:23], v[22:23], v[24:25], v[26:27]\n" \
  "v_pk_mul_f32 v[16:17], v[16:17], v[160:161]\n" \
  "v_pk_mul_f32 v[18:19], v[18:19], v[162:163]\n" \
  "v_pk_mul_f32 v[20:21], v[20:21], v[164:165]\n" \
  "v_pk_mul_f32 v[22:23], v[22:23], v[166:167]\n" \
  "v_exp_f32 v16, v16\n" \
  "v_exp_f32 v17, v17\n" \
  "v_exp_f32 v18, v18\n" \
  "v_exp_f32 v19, v19\n" \
  "v_exp_f32 v20, v20\n" \
  "v_exp_f32 v21, v21\n" \
  "v_exp_f32 v22, v22\n" \
  "v_exp_f32 v23, v23\n" \
  "v_pk_mul_f32 v[176:177], v[176:177], v[160:161]\n" \
  "v_pk_mul_f32 v[178:179], v[178:179], v[162:163]\n" \
  "v_pk_mul_f32 v[180:181], v[180:181], v[164:165]\n" \
  "v_pk_mul_f32 v[182:183], v[182:183], v[166:167]\n" \
  "v_pk_add_f32 v[16:17], v[16:17], 1.0 op_sel_hi:[1,0]\n" \
  "v_pk_add_f32 v[18:19], v[18:19], 1.0 op_sel_hi:[1,0]\n" \
  "v_pk_add_f32 v[20:21], v[20:21], 1.0 op_sel_hi:[1,0]\n" \
  "v_pk_add_f32 v[22:23], v[22:23], 1.0 op_sel_hi:[1,0]\n" \
  "v_rcp_f32 v16, v16\n" \
  "v_rcp_f32 v17, v17\n" \
  "v_rcp_f32 v18, v18\n" \
  "v_rcp_f32 v19, v19\n" \
  "v_rcp_f32 v20, v20\n" \
  "v_rcp_f32 v21, v21\n" \
  "v_rcp_f32 v22, v22\n" \
  "v_rcp_f32 v23, v23\n" \
  "v_pk_mul_f32 v[176:177], v[176:177], v[16:17]\n" \
  "v_pk_mul_f32 v[178:179], v[178:179], v[18:19]\n" \
  "v_pk_mul_f32 v[180:181], v[180:181], v[20:21]\n" \
  "v_pk_mul_f32 v[182:183], v[182:183], v[22:23]\n" \
  "v_cvt_pk_bf16_f32 v160, v176, v177\n" \
  "v_cvt_pk_bf16_f32 v161, v178, v179\n" \
  "v_cvt_pk_bf16_f32 v162, v180, v181\n" \
  "v_cvt_pk_bf16_f32 v163, v182, v183\n" \
  "v_pk_mul_f32 v[16:17], v[168:169], v[168:169]\n" \
  "v_pk_mul_f32 v[18:19], v[170:171], v[170:171]\n" \
  "v_permlane16_swap_b32 v160, v162\n" \
  "v_permlane16_swap_b32 v161, v163\n" \
  "v_pk_mul_f32 v[20:21], v[172:173], v[172:173]\n" \
  "v_pk_mul_f32 v[22:23], v[174:175], v[174:175]\n" \
  "v_pk_fma_f32 v[16:17], v[16:17], v[24:25], v[26:27]\n" \
  "v_pk_fma_f32 v[18:19], v[18:19], v[24:25], v[26:27]\n" \
  "v_pk_fma_f32 v[20:21], v[20:21], v[24:25], v[26:27]\n" \
  "v_pk_fma_f32 v[22:23], v[22:23], v[24:25], v[26:27]\n" \
  "v_pk_mul_f32 v[16:17], v[16:17], v[168:169]\n" \
  "v_pk_mul_f32 v[18:19], v[18:19], v[170:171]\n" \
  "v_pk_mul_f32 v[20:21], v[20:21], v[172:173]\n" \
  "v_pk_mul_f32 v[22:23], v[22:23], v[174:175]\n" \
  "v_exp_f32 v16, v16\n" \
  "v_exp_f32 v17, v17\n" \
  "v_exp_f32 v18, v18\n" \
  "v_exp_f32 v19, v19\n" \
  "v_exp_f32 v20, v20\n" \
  "v_exp_f32 v21, v21\n" \
  "v_exp_f32 v22, v22\n" \
  "v_exp_f32 v23, v23\n" \
  "v_pk_mul_f32 v[184:185], v[184:185], v[168:169]\n" \
  "v_pk_mul_f32 v[186:187], v[186:187], v[170:171]\n" \
  "v_pk_mul_f32 v[188:189], v[188:189], v[172:173]\n" \
  "v_pk_mul_f32 v[190:191], v[190:191], v[174:175]\n" \
  "v_pk_add_f32 v[16:17], v[16:17], 1.0 op_sel_hi:[1,0]\n" \
  "v_pk_add_f32 v[18:19], v[18:19], 1.0 op_sel_hi:[1,0]\n" \
  "v_pk_add_f32 v[20:21], v[20:21], 1.0 op_sel_hi:[1,0]\n" \
  "v_pk_add_f32 v[22:23], v[22:23], 1.0 op_sel_hi:[1,0]\n" \
  "v_rcp_f32 v16, v16\n" \
  "v_rcp_f32 v17, v17\n" \
  "v_rcp_f32 v18, v18\n" \
  "v_rcp_f32 v19, v19\n" \
  "v_rcp_f32 v20, v20\n" \
  "v_rcp_f32 v21, v21\n" \
  "v_rcp_f32 v22, v22\n" \
  "v_rcp_f32 v23, v23\n" \
  "v_pk_mul_f32 v[184:185], v[184:185], v[16:17]\n" \
  "v_pk_mul_f32 v[186:187], v[186:187], v[18:19]\n" \
  "v_pk_mul_f32 v[188:189], v[188:189], v[20:21]\n" \
  "v_pk_mul_f32 v[190:191], v[190:191], v[22:23]\n" \
  "v_cvt_pk_bf16_f32 v168, v184, v185\n" \
  "v_cvt_pk_bf16_f32 v169, v186, v187\n" \
  "v_cvt_pk_bf16_f32 v170, v188, v189\n" \
  "v_cvt_pk_bf16_f32 v171, v190, v191\n" \
  "s_nop 1\n" \
  "v_permlane16_swap_b32 v168, v170\n" \
  "v_permlane16_swap_b32 v169, v171\n" \
  "v_pk_mul_f32 v[16:17], v[192:193], v[192:193]\n" \
  "v_pk_mul_f32 v[18:19], v[194:195], v[194:195]\n" \
  "v_pk_mul_f32 v[20:21], v[196:197], v[196:197]\n" \
  "v_pk_mul_f32 v[22:23], v[198:199], v[198:199]\n" \
  "v_pk_fma_f32 v[16:17], v[16:17], v[24:25], v[26:27]\n" \
  "v_pk_fma_f32 v[18:19], v[18:19], v[24:25], v[26:27]\n" \
  "v_pk_fma_f32 v[20:21], v[20:21], v[24:25], v[26:27]\n" \
  "v_pk_fma_f32 v[22:23], v[22:23], v[24:25], v[26:27]\n" \
  "v_pk_mul_f32 v[16:17], v[16:17], v[192:193]\n" \
  "v_pk_mul_f32 v[18:19], v[18:19], v[194:195]\n" \
  "v_pk_mul_f32 v[20:21], v[20:21], v[196:197]\n" \
  "v_pk_mul_f32 v[22:23], v[22:23], v[198:199]\n" \
  "v_exp_f32 v16, v16\n" \
  "v_exp_f32 v17, v17\n" \
  "v_exp_f32 v18, v18\n" \
  "v_exp_f32 v19, v19\n" \
  "v_exp_f32 v20, v20\n" \
  "v_exp_f32 v21, v21\n" \
  "v_exp_f32 v22, v22\n" \
  "v_exp_f32 v23, v23\n" \
  "v_pk_mul_f32 v[208:209], v[208:209], v[192:193]\n" \
  "v_pk_mul_f32 v[210:211], v[210:211], v[194:195]\n" \
  "v_pk_mul_f32 v[212:213], v[212:213], v[196:197]\n" \
  "v_pk_mul_f32 v[214:215], v[214:215], v[198:199]\n" \
  "v_pk_add_f32 v[16:17], v[16:17], 1.0 op_sel_hi:[1,0]\n" \
  "v_pk_add_f32 v[18:19], v[18:19], 1.0 op_sel_hi:[1,0]\n" \
  "v_pk_add_f32 v[20:21], v[20:21], 1.0 op_sel_hi:[1,0]\n" \
  "v_pk_add_f32 v[22:23], v[22:23], 1.0 op_sel_hi:[1,0]\n" \
  "v_rcp_f32 v16, v16\n" \
  "v_rcp_f32 v17, v17\n" \
  "v_rcp_f32 v18, v18\n" \
  "v_rcp_f32 v19, v19\n" \
  "v_rcp_f32 v20, v20\n" \
  "v_rcp_f32 v21, v21\n" \
  "v_rcp_f32 v22, v22\n" \
  "v_rcp_f32 v23, v23\n" \
  "v_pk_mul_f32 v[208:209], v[208:209], v[16:17]\n" \
  "v_pk_mul_f32 v[210:211], v[210:211], v[18:19]\n" \
  "v_pk_mul_f32 v[212:213], v[212:213], v[20:21]\n" \
  "v_pk_mul_f32 v[214:215], v[214:215], v[22:23]\n" \
  "v_cvt_pk_bf16_f32 v192, v208, v209\n" \
  "v_cvt_pk_bf16_f32 v193, v210, v211\n" \
  "v_cvt_pk_bf16_f32 v194, v212, v213\n" \
  "v_cvt_pk_bf16_f32 v195, v214, v215\n" \
  "v_pk_mul_f32 v[16:17], v[200:201], v[200:201]\n" \
  "v_pk_mul_f32 v[18:19], v[202:203], v[202:203]\n" \
  "v_permlane16_swap_b32 v192, v194\n" \
  "v_permlane16_swap_b32 v193, v195\n" \
  "v_pk_mul_f32 v[20:21], v[204:205], v[204:205]\n" \
  "v_pk_mul_f32 v[22:23], v[206:207], v[206:207]\n" \
  "v_pk_fma_f32 v[16:17], v[16:17], v[24:25], v[26:27]\n" \
  "v_pk_fma_f32 v[18:19], v[18:19], v[24:25], v[26:27]\n" \
  "v_pk_fma_f32 v[20:21], v[20:21], v[24:25], v[26:27]\n" \
  "v_pk_fma_f32 v[22:23], v[22:23], v[24:25], v[26:27]\n" \
  "v_pk_mul_f32 v[16:17], v[16:17], v[200:201]\n" \
  "v_pk_mul_f32 v[18:19], v[18:19], v[202:203]\n" \
  "v_pk_mul_f32 v[20:21], v[20:21], v[204:205]\n" \
  "v_pk_mul_f32 v[22:23], v[22:23], v[206:207]\n" \
  "v_exp_f32 v16, v16\n" \
  "v_exp_f32 v17, v17\n" \
  "v_exp_f32 v18, v18\n" \
  "v_exp_f32 v19, v19\n" \
  "v_exp_f32 v20, v20\n" \
  "v_exp_f32 v21, v21\n" \
  "v_exp_f32 v22, v22\n" \
  "v_exp_f32 v23, v23\n" \
  "v_pk_mul_f32 v[216:217], v[216:217], v[200:201]\n" \
  "v_pk_mul_f32 v[218:219], v[218:219], v[202:203]\n" \
  "v_pk_mul_f32 v[220:221], v[220:221], v[204:205]\n" \
  "v_pk_mul_f32 v[222:223], v[222:223], v[206:207]\n" \
  "v_pk_add_f32 v[16:17], v[16:17], 1.0 op_sel_hi:[1,0]\n" \
  "v_pk_add_f32 v[18:19], v[18:19], 1.0 op_sel_hi:[1,0]\n" \
  "v_pk_add_f32 v[20:21], v[20:21], 1.0 op_sel_hi:[1,0]\n" \
  "v_pk_add_f32 v[22:23], v[22:23], 1.0 op_sel_hi:[1,0]\n" \
  "v_rcp_f32 v16, v16\n" \
  "v_rcp_f32 v17, v17\n" \
  "v_rcp_f32 v18, v18\n" \
  "v_rcp_f32 v19, v19\n" \
  "v_rcp_f32 v20, v20\n" \
  "v_rcp_f32 v21, v21\n" \
  "v_rcp_f32 v22, v22\n" \
  "v_rcp_f32 v23, v23\n" \
  "v_pk_mul_f32 v[216:217], v[216:217], v[16:17]\n" \
  "v_pk_mul_f32 v[218:219], v[218:219], v[18:19]\n" \
  "v_pk_mul_f32 v[220:221], v[220:221], v[20:21]\n" \
  "v_pk_mul_f32 v[222:223], v[222:223], v[22:23]\n" \
  "v_cvt_pk_bf16_f32 v200, v216, v217\n" \
  "v_cvt_pk_bf16_f32 v201, v218, v219\n" \
  "buffer_load_dword v30, v5, s[68:71], 0 offen sc1\n" \
  "v_cvt_pk_bf16_f32 v202, v220, v221\n" \
  "v_cvt_pk_bf16_f32 v203, v222, v223\n" \
  "s_nop 1\n" \
  "v_permlane16_swap_b32 v200, v202\n" \
  "v_permlane16_swap_b32 v201, v203\n" \
  "v_pk_mul_f32 v[16:17], v[224:225], v[224:225]\n" \
  "v_pk_mul_f32 v[18:19], v[226:227], v[226:227]\n" \
  "v_pk_mul_f32 v[20:21], v[228:229], v[228:229]\n" \
  "v_pk_mul_f32 v[22:23], v[230:231], v[230:231]\n" \
  "v_pk_fma_f32 v[16:17], v[16:17], v[24:25], v[26:27]\n" \
  "v_pk_fma_f32 v[18:19], v[18:19], v[24:25], v[26:27]\n" \
  "buffer_load_dword v31, v5, s[68:71], 0 offen sc1\n" \
  "buffer_store_dwordx4 v[160:163], v10, s[44:47], s85 offen offset:1024 sc1\n" \
  "buffer_store_dwordx4 v[168:171], v11, s[44:47], s85 offen offset:1024 sc1\n" \
  "buffer_store_dwordx4 v[192:195], v10, s[44:47], s85 offen offset:2048 sc1\n" \
  "buffer_store_dwordx4 v[200:203], v11, s[44:47], s85 offen offset:2048 sc1\n" \
  "v_pk_fma_f32 v[20:21], v[20:21], v[24:25], v[26:27]\n" \
  "v_pk_fma_f32 v[22:23], v[22:23], v[24:25], v[26:27]\n" \
  "v_pk_mul_f32 v[16:17], v[16:17], v[224:225]\n" \
  "v_pk_mul_f32 v[18:19], v[18:19], v[226:227]\n" \
  "v_pk_mul_f32 v[20:21], v[20:21], v[228:229]\n" \
  "v_pk_mul_f32 v[22:23], v[22:23], v[230:231]\n" \
  "v_xor_b32 v96, 0, v2\n" \
  "v_xor_b32 v97, 32, v2\n" \
  "v_exp_f32 v16, v16\n" \
  "v_exp_f32 v17, v17\n" \
  "v_exp_f32 v18, v18\n" \
  "v_exp_f32 v19, v19\n" \
  "v_exp_f32 v20, v20\n" \
  "v_exp_f32 v21, v21\n" \
  "v_xor_b32 v98, 64, v2\n" \
  "v_xor_b32 v99, 96, v2\n" \
  "v_exp_f32 v22, v22\n" \
  "v_exp_f32 v23, v23\n" \
  "v_pk_mul_f32 v[240:241], v[240:241], v[224:225]\n" \
  "v_pk_mul_f32 v[242:243], v[242:243], v[226:227]\n" \
  "v_pk_mul_f32 v[244:245], v[244:245], v[228:229]\n" \
  "v_pk_mul_f32 v[246:247], v[246:247], v[230:231]\n" \
  "v_xor_b32 v100, 128, v2\n" \
  "v_xor_b32 v101, 160, v2\n" \
  "v_pk_add_f32 v[16:17], v[16:17], 1.0 op_sel_hi:[1,0]\n" \
  "v_pk_add_f32 v[18:19], v[18:19], 1.0 op_sel_hi:[1,0]\n" \
  "v_pk_add_f32 v[20:21], v[20:21], 1.0 op_sel_hi:[1,0]\n" \
  "v_pk_add_f32 v[22:23], v[22:23], 1.0 op_sel_hi:[1,0]\n" \
  "v_rcp_f32 v16, v16\n" \
  "v_rcp_f32 v17, v17\n" \
  "v_xor_b32 v102, 192, v2\n" \
  "v_xor_b32 v103, 224, v2\n" \
  "v_rcp_f32 v18, v18\n" \
  "v_rcp_f32 v19, v19\n" \
  "v_rcp_f32 v20, v20\n" \
  "v_rcp_f32 v21, v21\n" \
  "v_rcp_f32 v22, v22\n" \
  "v_rcp_f32 v23, v23\n" \
  "v_xor_b32 v104, 65536, v2\n" \
  "v_xor_b32 v105, 65568, v2\n" \
  "v_pk_mul_f32 v[240:241], v[240:241], v[16:17]\n" \
  "v_pk_mul_f32 v[242:243], v[242:243], v[18:19]\n" \
  "v_pk_mul_f32 v[244:245], v[244:245], v[20:21]\n" \
  "v_pk_mul_f32 v[246:247], v[246:247], v[22:23]\n" \
  "v_cvt_pk_bf16_f32 v224, v240, v241\n" \
  "v_cvt_pk_bf16_f32 v225, v242, v243\n" \
  "v_xor_b32 v106, 65600, v2\n" \
  "v_xor_b32 v107, 65632, v2\n" \
  "v_cvt_pk_bf16_f32 v226, v244, v245\n" \
  "v_cvt_pk_bf16_f32 v227, v246, v247\n" \
  "v_pk_mul_f32 v[16:17], v[232:233], v[232:233]\n" \
  "v_pk_mul_f32 v[18:19], v[234:235], v[234:235]\n" \
  "v_permlane16_swap_b32 v224, v226\n" \
  "v_permlane16_swap_b32 v225, v227\n" \
  "buffer_store_dwordx4 v[224:227], v10, s[44:47], s85 offen offset:3072 sc1\n" \
  "v_xor_b32 v108, 65664, v2\n" \
  "v_xor_b32 v109, 65696, v2\n" \
  "v_pk_mul_f32 v[20:21], v[236:237], v[236:237]\n" \
  "v_pk_mul_f32 v[22:23], v[238:239], v[238:239]\n" \
  "v_pk_fma_f32 v[16:17], v[16:17], v[24:25], v[26:27]\n" \
  "v_pk_fma_f32 v[18:19], v[18:19], v[24:25], v[26:27]\n" \
  "v_pk_fma_f32 v[20:21], v[20:21], v[24:25], v[26:27]\n" \
  "v_pk_fma_f32 v[22:23], v[22:23], v[24:25], v[26:27]\n" \
  "v_xor_b32 v110, 65728, v2\n" \
  "v_xor_b32 v111, 65760, v2\n" \
  "v_pk_mul_f32 v[16:17], v[16:17], v[232:233]\n" \
  "v_pk_mul_f32 v[18:19], v[18:19], v[234:235]\n" \
  "v_pk_mul_f32 v[20:21], v[20:21], v[236:237]\n" \
  "v_pk_mul_f32 v[22:23], v[22:23], v[238:239]\n" \
  "v_exp_f32 v16, v16\n" \
  "v_exp_f32 v17, v17\n" \
  "v_xor_b32 v112, 131072, v2\n" \
  "v_xor_b32 v113, 131104, v2\n" \
  "v_exp_f32 v18, v18\n" \
  "v_exp_f32 v19, v19\n" \
  "v_exp_f32 v20, v20\n" \
  "v_exp_f32 v21, v21\n" \
  "v_exp_f32 v22, v22\n" \
  "v_exp_f32 v23, v23\n" \
  "v_xor_b32 v114, 131136, v2\n" \
  "v_xor_b32 v115, 131168, v2\n" \
  "v_pk_mul_f32 v[248:249], v[248:249], v[232:233]\n" \
  "v_pk_mul_f32 v[250:251], v[250:251], v[234:235]\n" \
  "v_pk_mul_f32 v[252:253], v[252:253], v[236:237]\n" \
  "v_pk_mul_f32 v[254:255], v[254:255], v[238:239]\n" \
  "v_pk_add_f32 v[16:17], v[16:17], 1.0 op_sel_hi:[1,0]\n" \
  "v_pk_add_f32 v[18:19], v[18:19], 1.0 op_sel_hi:[1,0]\n" \
  "v_xor_b32 v116, 131200, v2\n" \
  "v_xor_b32 v117, 131232, v2\n" \
  "v_pk_add_f32 v[20:21], v[20:21], 1.0 op_sel_hi:[1,0]\n" \
  "v_pk_add_f32 v[22:23], v[22:23], 1.0 op_sel_hi:[1,0]\n" \
  "v_rcp_f32 v16, v16\n" \
  "v_rcp_f32 v17, v17\n" \
  "v_rcp_f32 v18, v18\n" \
  "v_rcp_f32 v19, v19\n" \
  "v_xor_b32 v118, 131264, v2\n" \
  "v_xor_b32 v119, 131296, v2\n" \
  "v_rcp_f32 v20, v20\n" \
  "v_rcp_f32 v21, v21\n" \
  "v_rcp_f32 v22, v22\n" \
  "v_rcp_f32 v23, v23\n" \
  "v_pk_mul_f32 v[248:249], v[248:249], v[16:17]\n" \
  "v_pk_mul_f32 v[250:251], v[250:251], v[18:19]\n" \
  "v_add_u32 v120, 0, v6\n" \
  "v_add_u32 v121, 65536, v6\n" \
  "v_add_u32 v122, 131072, v6\n" \
  "v_pk_mul_f32 v[252:253], v[252:253], v[20:21]\n" \
  "v_pk_mul_f32 v[254:255], v[254:255], v[22:23]\n" \
  "v_cvt_pk_bf16_f32 v232, v248, v249\n" \
  "v_cvt_pk_bf16_f32 v233, v250, v251\n" \
  "v_cvt_pk_bf16_f32 v234, v252, v253\n" \
  "v_cvt_pk_bf16_f32 v235, v254, v255\n" \
  "s_nop 1\n" \
  "v_permlane16_swap_b32 v232, v234\n" \
  "v_permlane16_swap_b32 v233, v235\n" \
  "buffer_store_dwordx4 v[232:235], v11, s[44:47], s85 offen offset:3072 sc1\n" \
  "s_waitcnt vmcnt(7)\n" \
  "v_cmp_eq_u32 vcc, 0, v30\n" \
  "s_cbranch_vccz 4f\n" \
  "s_waitcnt vmcnt(6)\n" \
  "v_cmp_eq_u32 vcc, 0, v31\n" \
  "s_cbranch_vccz 4f\n" \
  "s_mov_b32 s15, 0x400000\n" \
  "3:\n" \
  "buffer_load_dword v30, v5, s[68:71], 0 offen sc1\n" \
  "s_waitcnt vmcnt(0)\n" \
  "v_cmp_eq_u32 vcc, 0, v30\n" \
  "s_cbranch_vccz 4f\n" \
  "s_sub_u32 s15, s15, 1\n" \
  "s_cmp_lg_u32 s15, 0\n" \
  "s_cbranch_scc1 3b\n" \
  "4:\n" \
  "buffer_load_dwordx4 a[0:3], v7, s[60:63], s14 offen offset:0 sc1\n" \
  "buffer_load_dwordx4 a[4:7], v7, s[60:63], s14 offen offset:512 sc1\n" \
  "s_add_u32 s14, s14, 2048\n" \
  "buffer_load_dwordx4 a[8:11], v7, s[60:63], s14 offen offset:0 sc1\n" \
  "buffer_load_dwordx4 a[12:15], v7, s[60:63], s14 offen offset:512 sc1\n" \
  "s_add_u32 s14, s14, 2048\n" \
  "buffer_load_dwordx4 a[16:19], v7, s[60:63], s14 offen offset:0 sc1\n" \
  "buffer_load_dwordx4 a[20:23], v7, s[60:63], s14 offen offset:512 sc1\n" \
  "s_add_u32 s14, s14, 2048\n" \
  "buffer_load_dwordx4 a[24:27], v7, s[60:63], s14 offen offset:0 sc1\n" \
  "buffer_load_dwordx4 a[28:31], v7, s[60:63], s14 offen offset:512 sc1\n" \
  "s_add_u32 s14, s14, 2048\n" \
  "ds_read_b64_tr_b16 a[112:113], v104 offset:49152\n" \
  "ds_read_b64_tr_b16 a[114:115], v104 offset:53248\n" \
  "ds_read_b64_tr_b16 a[116:117], v105 offset:49152\n" \
  "ds_read_b64_tr_b16 a[118:119], v105 offset:53248\n" \
  "ds_read_b64_tr_b16 a[120:121], v106 offset:49152\n" \
  "ds_read_b64_tr_b16 a[122:123], v106 offset:53248\n" \
  "ds_read_b64_tr_b16 a[124:125], v107 offset:49152\n" \
  "ds_read_b64_tr_b16 a[126:127], v107 offset:53248\n" \
  "ds_read_b64_tr_b16 a[128:129], v108 offset:49152\n" \
  "ds_read_b64_tr_b16 a[130:131], v108 offset:53248\n" \
  "ds_read_b64_tr_b16 a[132:133], v109 offset:49152\n" \
  "ds_read_b64_tr_b16 a[134:135], v109 offset:53248\n" \
  "buffer_load_dwordx4 a[32:35], v7, s[60:63], s14 offen offset:0 sc1\n" \
  "buffer_load_dwordx4 a[36:39], v7, s[60:63], s14 offen offset:512 sc1\n" \
  "s_add_u32 s14, s14, 2048\n" \
  "s_waitcnt vmcnt(8) lgkmcnt(8)\n" \
  "v_mfma_f32_16x16x32_bf16 v[32:35], a[112:115], a[0:3], 0\n" \
  "ds_read_b64_tr_b16 a[136:137], v110 offset:49152\n" \
  "v_mfma_f32_16x16x32_bf16 v[36:39], a[112:115], a[4:7], 0\n" \
  "ds_read_b64_tr_b16 a[138:139], v110 offset:53248\n" \
  "buffer_load_dwordx4 a[40:43], v7, s[60:63], s14 offen offset:0 sc1\n" \
  "v_mfma_f32_16x16x32_bf16 v[40:43], a[116:119], a[0:3], 0\n" \
  "ds_read_b64_tr_b16 a[140:141], v111 offset:49152\n" \
  "v_mfma_f32_16x16x32_bf16 v[44:47], a[116:119], a[4:7], 0\n" \
  "ds_read_b64_tr_b16 a[142:143], v111 offset:53248\n" \
  "s_waitcnt lgkmcnt(8)\n" \
  "v_mfma_f32_16x16x32_bf16 v[48:51], a[120:123], a[0:3], 0\n" \
  "ds_read_b64_tr_b16 a[112:113], v104 offset:57344\n" \
  "s_add_u32 m0, s86, 57344\n" \
  "v_mfma_f32_16x16x32_bf16 v[52:55], a[120:123], a[4:7], 0\n" \
  "ds_read_b64_tr_b16 a[114:115], v104 offset:61440\n" \
  "buffer_load_dwordx4 v0, s[56:59], s13 offen offset:0 lds\n" \
  "v_mfma_f32_16x16x32_bf16 v[56:59], a[124:127], a[0:3], 0\n" \
  "ds_read_b64_tr_b16 a[116:117], v105 offset:57344\n" \
  "v_mfma_f32_16x16x32_bf16 v[60:63], a[124:127], a[4:7], 0\n" \
  "ds_read_b64_tr_b16 a[118:119], v105 offset:61440\n" \
  "s_waitcnt lgkmcnt(8)\n" \
  "v_mfma_f32_16x16x32_bf16 v[64:67], a[128:131], a[0:3], 0\n" \
  "ds_read_b64_tr_b16 a[120:121], v106 offset:57344\n" \
  "v_mfma_f32_16x16x32_bf16 v[68:71], a[128:131], a[4:7], 0\n" \
  "ds_read_b64_tr_b16 a[122:123], v106 offset:61440\n" \
  "buffer_load_dwordx4 v1, s[56:59], s13 offen offset:1024 lds\n" \
  "v_mfma_f32_16x16x32_bf16 v[72:75], a[132:135], a[0:3], 0\n" \
  "ds_read_b64_tr_b16 a[124:125], v107 offset:57344\n" \
  "s_add_u32 s13, s13, 65536\n" \
  "v_mfma_f32_16x16x32_bf16 v[76:79], a[132:135], a[4:7], 0\n" \
  "ds_read_b64_tr_b16 a[126:127], v107 offset:61440\n" \
  "s_waitcnt lgkmcnt(8)\n" \
  "v_mfma_f32_16x16x32_bf16 v[80:83], a[136:139], a[0:3], 0\n" \
  "ds_read_b64_tr_b16 a[128:129], v108 offset:57344\n" \
  "v_mfma_f32_16x16x32_bf16 v[84:87], a[136:139], a[4:7], 0\n" \
  "ds_read_b64_tr_b16 a[130:131], v108 offset:61440\n" \
  "v_mfma_f32_16x16x32_bf16 v[88:91], a[140:143], a[0:3], 0\n" \
  "ds_read_b64_tr_b16 a[132:133], v109 offset:57344\n" \
  "v_mfma_f32_16x16x32_bf16 v[92:95], a[140:143], a[4:7], 0\n" \
  "ds_read_b64_tr_b16 a[134:135], v109 offset:61440\n" \
  "s_waitcnt vmcnt(9) lgkmcnt(8)\n" \
  "v_mfma_f32_16x16x32_bf16 v[32:35], a[112:115], a[8:11], v[32:35]\n" \
  "ds_read_b64_tr_b16 a[136:137], v110 offset:57344\n" \
  "v_mfma_f32_16x16x32_bf16 v[36:39], a[112:115], a[12:15], v[36:39]\n" \
  "ds_read_b64_tr_b16 a[138:139], v110 offset:61440\n" \
  "buffer_load_dwordx4 a[44:47], v7, s[60:63], s14 offen offset:512 sc1\n" \
  "v_mfma_f32_16x16x32_bf16 v[40:43], a[116:119], a[8:11], v[40:43]\n" \
  "ds_read_b64_tr_b16 a[140:141], v111 offset:57344\n" \
  "s_add_u32 s14, s14, 2048\n" \
  "v_mfma_f32_16x16x32_bf16 v[44:47], a[116:119], a[12:15], v[44:47]\n" \
  "ds_read_b64_tr_b16 a[142:143], v111 offset:61440\n" \
  "s_waitcnt lgkmcnt(8)\n" \
  "v_mfma_f32_16x16x32_bf16 v[48:51], a[120:123], a[8:11], v[48:51]\n" \
  "ds_read_b64_tr_b16 a[112:113], v112 offset:0\n" \
  "s_add_u32 m0, s86, 65536\n" \
  "v_mfma_f32_16x16x32_bf16 v[52:55], a[120:123], a[12:15], v[52:55]\n" \
  "ds_read_b64_tr_b16 a[114:115], v112 offset:4096\n" \
  "buffer_load_dwordx4 v0, s[56:59], s13 offen offset:0 lds\n" \
  "v_mfma_f32_16x16x32_bf16 v[56:59], a[124:127], a[8:11], v[56:59]\n" \
  "ds_read_b64_tr_b16 a[116:117], v113 offset:0\n" \
  "v_mfma_f32_16x16x32_bf16 v[60:63], a[124:127], a[12:15], v[60:63]\n" \
  "ds_read_b64_tr_b16 a[118:119], v113 offset:4096\n" \
  "s_waitcnt lgkmcnt(8)\n" \
  "v_mfma_f32_16x16x32_bf16 v[64:67], a[128:131], a[8:11], v[64:67]\n" \
  "ds_read_b64_tr_b16 a[120:121], v114 offset:0\n" \
  "v_mfma_f32_16x16x32_bf16 v[68:71], a[128:131], a[12:15], v[68:71]\n" \
  "ds_read_b64_tr_b16 a[122:123], v114 offset:4096\n" \
  "buffer_load_dwordx4 a[48:51], v7, s[60:63], s14 offen offset:0 sc1\n" \
  "v_mfma_f32_16x16x32_bf16 v[72:75], a[132:135], a[8:11], v[72:75]\n" \
  "ds_read_b64_tr_b16 a[124:125], v115 offset:0\n" \
  "v_mfma_f32_16x16x32_bf16 v[76:79], a[132:135], a[12:15], v[76:79]\n" \
  "ds_read_b64_tr_b16 a[126:127], v115 offset:4096\n" \
  "s_waitcnt lgkmcnt(8)\n" \
  "v_mfma_f32_16x16x32_bf16 v[80:83], a[136:139], a[8:11], v[80:83]\n" \
  "ds_read_b64_tr_b16 a[128:129], v116 offset:0\n" \
  "v_mfma_f32_16x16x32_bf16 v[84:87], a[136:139], a[12:15], v[84:87]\n" \
  "ds_read_b64_tr_b16 a[130:131], v116 offset:4096\n" \
  "buffer_load_dwordx4 v1, s[56:59], s13 offen offset:1024 lds\n" \
  "v_mfma_f32_16x16x32_bf16 v[88:91], a[140:143], a[8:11], v[88:91]\n" \
  "ds_read_b64_tr_b16 a[132:133], v117 offset:0\n" \
  "s_add_u32 s13, s13, 65536\n" \
  "v_mfma_f32_16x16x32_bf16 v[92:95], a[140:143], a[12:15], v[92:95]\n" \
  "ds_read_b64_tr_b16 a[134:135], v117 offset:4096\n" \
  "s_waitcnt vmcnt(11) lgkmcnt(8)\n" \
  "v_mfma_f32_16x16x32_bf16 v[32:35], a[112:115], a[16:19], v[32:35]\n" \
  "ds_read_b64_tr_b16 a[136:137], v118 offset:0\n" \
  "v_mfma_f32_16x16x32_bf16 v[36:39], a[112:115], a[20:23], v[36:39]\n" \
  "ds_read_b64_tr_b16 a[138:139], v118 offset:4096\n" \
  "buffer_load_dwordx4 a[52:55], v7, s[60:63], s14 offen offset:512 sc1\n" \
  "v_mfma_f32_16x16x32_bf16 v[40:43], a[116:119], a[16:19], v[40:43]\n" \
  "ds_read_b64_tr_b16 a[140:141], v119 offset:0\n" \
  "s_add_u32 s14, s14, 2048\n" \
  "v_mfma_f32_16x16x32_bf16 v[44:47], a[116:119], a[20:23], v[44:47]\n" \
  "ds_read_b64_tr_b16 a[142:143], v119 offset:4096\n" \
  "s_waitcnt lgkmcnt(8)\n" \
  "v_mfma_f32_16x16x32_bf16 v[48:51], a[120:123], a[16:19], v[48:51]\n" \
  "ds_read_b64_tr_b16 a[112:113], v112 offset:8192\n" \
  "s_add_u32 m0, s86, 73728\n" \
  "v_mfma_f32_16x16x32_bf16 v[52:55], a[120:123], a[20:23], v[52:55]\n" \
  "ds_read_b64_tr_b16 a[114:115], v112 offset:12288\n" \
  "buffer_load_dwordx4 v0, s[56:59], s13 offen offset:0 lds\n" \
  "v_mfma_f32_16x16x32_bf16 v[56:59], a[124:127], a[16:19], v[56:59]\n" \
  "ds_read_b64_tr_b16 a[116:117], v113 offset:8192\n" \
  "v_mfma_f32_16x16x32_bf16 v[60:63], a[124:127], a[20:23], v[60:63]\n" \
  "ds_read_b64_tr_b16 a[118:119], v113 offset:12288\n" \
  "s_waitcnt lgkmcnt(8)\n" \
  "v_mfma_f32_16x16x32_bf16 v[64:67], a[128:131], a[16:19], v[64:67]\n" \
  "ds_read_b64_tr_b16 a[120:121], v114 offset:8192\n" \
  "v_mfma_f32_16x16x32_bf16 v[68:71], a[128:131], a[20:23], v[68:71]\n" \
  "ds_read_b64_tr_b16 a[122:123], v114 offset:12288\n" \
  "buffer_load_dwordx4 v1, s[56:59], s13 offen offset:1024 lds\n" \
  "v_mfma_f32_16x16x32_bf16 v[72:75], a[132:135], a[16:19], v[72:75]\n" \
  "ds_read_b64_tr_b16 a[124:125], v115 offset:8192\n" \
  "s_add_u32 s13, s13, 65536\n" \
  "v_mfma_f32_16x16x32_bf16 v[76:79], a[132:135], a[20:23], v[76:79]\n" \
  "ds_read_b64_tr_b16 a[126:127], v115 offset:12288\n" \
  "s_waitcnt lgkmcnt(8)\n" \
  "v_mfma_f32_16x16x32_bf16 v[80:83], a[136:139], a[16:19], v[80:83]\n" \
  "ds_read_b64_tr_b16 a[128:129], v116 offset:8192\n" \
  "v_mfma_f32_16x16x32_bf16 v[84:87], a[136:139], a[20:23], v[84:87]\n" \
  "ds_read_b64_tr_b16 a[130:131], v116 offset:12288\n" \
  "v_mfma_f32_16x16x32_bf16 v[88:91], a[140:143], a[16:19], v[88:91]\n" \
  "ds_read_b64_tr_b16 a[132:133], v117 offset:8192\n" \
  "v_mfma_f32_16x16x32_bf16 v[92:95], a[140:143], a[20:23], v[92:95]\n" \
  "ds_read_b64_tr_b16 a[134:135], v117 offset:12288\n" \
  "s_waitcnt vmcnt(12) lgkmcnt(8)\n" \
  "v_mfma_f32_16x16x32_bf16 v[32:35], a[112:115], a[24:27], v[32:35]\n" \
  "ds_read_b64_tr_b16 a[136:137], v118 offset:8192\n" \
  "v_mfma_f32_16x16x32_bf16 v[36:39], a[112:115], a[28:31], v[36:39]\n" \
  "ds_read_b64_tr_b16 a[138:139], v118 offset:12288\n" \
  "buffer_load_dwordx4 a[56:59], v7, s[60:63], s14 offen offset:0 sc1\n" \
  "v_mfma_f32_16x16x32_bf16 v[40:43], a[116:119], a[24:27], v[40:43]\n" \
  "ds_read_b64_tr_b16 a[140:141], v119 offset:8192\n" \
  "v_mfma_f32_16x16x32_bf16 v[44:47], a[116:119], a[28:31], v[44:47]\n" \
  "ds_read_b64_tr_b16 a[142:143], v119 offset:12288\n" \
  "s_waitcnt lgkmcnt(8)\n" \
  "s_barrier\n" \
  "buffer_store_dword v24, v4, s[68:71], 0 offen sc1\n" \
  "v_mfma_f32_16x16x32_bf16 v[48:51], a[120:123], a[24:27], v[48:51]\n" \
  "ds_read_b64_tr_b16 a[112:113], v112 offset:16384\n" \
  "s_add_u32 m0, s86, 81920\n" \
  "v_mfma_f32_16x16x32_bf16 v[52:55], a[120:123], a[28:31], v[52:55]\n" \
  "ds_read_b64_tr_b16 a[114:115], v112 offset:20480\n" \
  "buffer_load_dwordx4 v0, s[56:59], s13 offen offset:0 lds\n" \
  "v_mfma_f32_16x16x32_bf16 v[56:59], a[124:127], a[24:27], v[56:59]\n" \
  "ds_read_b64_tr_b16 a[116:117], v113 offset:16384\n" \
  "v_mfma_f32_16x16x32_bf16 v[60:63], a[124:127], a[28:31], v[60:63]\n" \
  "ds_read_b64_tr_b16 a[118:119], v113 offset:20480\n" \
  "s_waitcnt lgkmcnt(8)\n" \
  "v_mfma_f32_16x16x32_bf16 v[64:67], a[128:131], a[24:27], v[64:67]\n" \
  "ds_read_b64_tr_b16 a[120:121], v114 offset:16384\n" \
  "v_mfma_f32_16x16x32_bf16 v[68:71], a[128:131], a[28:31], v[68:71]\n" \
  "ds_read_b64_tr_b16 a[122:123], v114 offset:20480\n" \
  "buffer_load_dwordx4 a[60:63], v7, s[60:63], s14 offen offset:512 sc1\n" \
  "v_mfma_f32_16x16x32_bf16 v[72:75], a[132:135], a[24:27], v[72:75]\n" \
  "ds_read_b64_tr_b16 a[124:125], v115 offset:16384\n" \
  "s_add_u32 s14, s14, 2048\n" \
  "v_mfma_f32_16x16x32_bf16 v[76:79], a[132:135], a[28:31], v[76:79]\n" \
  "ds_read_b64_tr_b16 a[126:127], v115 offset:20480\n" \
  "s_waitcnt lgkmcnt(8)\n" \
  "v_mfma_f32_16x16x32_bf16 v[80:83], a[136:139], a[24:27], v[80:83]\n" \
  "ds_read_b64_tr_b16 a[128:129], v116 offset:16384\n" \
  "v_mfma_f32_16x16x32_bf16 v[84:87], a[136:139], a[28:31], v[84:87]\n" \
  "ds_read_b64_tr_b16 a[130:131], v116 offset:20480\n" \
  "buffer_load_dwordx4 v1, s[56:59], s13 offen offset:1024 lds\n" \
  "v_mfma_f32_16x16x32_bf16 v[88:91], a[140:143], a[24:27], v[88:91]\n" \
  "ds_read_b64_tr_b16 a[132:133], v117 offset:16384\n" \
  "s_add_u32 s13, s13, 65536\n" \
  "v_mfma_f32_16x16x32_bf16 v[92:95], a[140:143], a[28:31], v[92:95]\n" \
  "ds_read_b64_tr_b16 a[134:135], v117 offset:20480\n" \
  "s_waitcnt vmcnt(15) lgkmcnt(8)\n" \
  "v_mfma_f32_16x16x32_bf16 v[32:35], a[112:115], a[32:35], v[32:35]\n" \
  "ds_read_b64_tr_b16 a[136:137], v118 offset:16384\n" \
  "v_mfma_f32_16x16x32_bf16 v[36:39], a[112:115], a[36:39], v[36:39]\n" \
  "ds_read_b64_tr_b16 a[138:139], v118 offset:20480\n" \
  "buffer_load_dwordx4 a[64:67], v7, s[60:63], s14 offen offset:0 sc1\n" \
  "v_mfma_f32_16x16x32_bf16 v[40:43], a[116:119], a[32:35], v[40:43]\n" \
  "ds_read_b64_tr_b16 a[140:141], v119 offset:16384\n" \
  "v_mfma_f32_16x16x32_bf16 v[44:47], a[116:119], a[36:39], v[44:47]\n" \
  "ds_read_b64_tr_b16 a[142:143], v119 offset:20480\n" \
  "s_waitcnt lgkmcnt(8)\n" \
  "v_mfma_f32_16x16x32_bf16 v[48:51], a[120:123], a[32:35], v[48:51]\n" \
  "ds_read_b64_tr_b16 a[112:113], v112 offset:24576\n" \
  "s_add_u32 m0, s86, 90112\n" \
  "v_mfma_f32_16x16x32_bf16 v[52:55], a[120:123], a[36:39], v[52:55]\n" \
  "ds_read_b64_tr_b16 a[114:115], v112 offset:28672\n" \
  "buffer_load_dwordx4 v0, s[56:59], s13 offen offset:0 lds\n" \
  "v_mfma_f32_16x16x32_bf16 v[56:59], a[124:127], a[32:35], v[56:59]\n" \
  "ds_read_b64_tr_b16 a[116:117], v113 offset:24576\n" \
  "v_mfma_f32_16x16x32_bf16 v[60:63], a[124:127], a[36:39], v[60:63]\n" \
  "ds_read_b64_tr_b16 a[118:119], v113 offset:28672\n" \
  "s_waitcnt lgkmcnt(8)\n" \
  "v_mfma_f32_16x16x32_bf16 v[64:67], a[128:131], a[32:35], v[64:67]\n" \
  "ds_read_b64_tr_b16 a[120:121], v114 offset:24576\n" \
  "v_mfma_f32_16x16x32_bf16 v[68:71], a[128:131], a[36:39], v[68:71]\n" \
  "ds_read_b64_tr_b16 a[122:123], v114 offset:28672\n" \
  "buffer_load_dwordx4 a[68:71], v7, s[60:63], s14 offen offset:512 sc1\n" \
  "v_mfma_f32_16x16x32_bf16 v[72:75], a[132:135], a[32:35], v[72:75]\n" \
  "ds_read_b64_tr_b16 a[124:125], v115 offset:24576\n" \
  "s_add_u32 s14, s14, 2048\n" \
  "v_mfma_f32_16x16x32_bf16 v[76:79], a[132:135], a[36:39], v[76:79]\n" \
  "ds_read_b64_tr_b16 a[126:127], v115 offset:28672\n" \
  "s_waitcnt lgkmcnt(8)\n" \
  "v_mfma_f32_16x16x32_bf16 v[80:83], a[136:139], a[32:35], v[80:83]\n" \
  "ds_read_b64_tr_b16 a[128:129], v116 offset:24576\n" \
  "v_mfma_f32_16x16x32_bf16 v[84:87], a[136:139], a[36:39], v[84:87]\n" \
  "ds_read_b64_tr_b16 a[130:131], v116 offset:28672\n" \
  "buffer_load_dwordx4 v1, s[56:59], s13 offen offset:1024 lds\n" \
  "v_mfma_f32_16x16x32_bf16 v[88:91], a[140:143], a[32:35], v[88:91]\n" \
  "ds_read_b64_tr_b16 a[132:133], v117 offset:24576\n" \
  "s_add_u32 s13, s13, 65536\n" \
  "v_mfma_f32_16x16x32_bf16 v[92:95], a[140:143], a[36:39], v[92:95]\n" \
  "ds_read_b64_tr_b16 a[134:135], v117 offset:28672\n" \
  "s_waitcnt vmcnt(15) lgkmcnt(8)\n" \
  "v_mfma_f32_16x16x32_bf16 v[32:35], a[112:115], a[40:43], v[32:35]\n" \
  "ds_read_b64_tr_b16 a[136:137], v118 offset:24576\n" \
  "v_mfma_f32_16x16x32_bf16 v[36:39], a[112:115], a[44:47], v[36:39]\n" \
  "ds_read_b64_tr_b16 a[138:139], v118 offset:28672\n" \
  "buffer_load_dwordx4 a[72:75], v7, s[60:63], s14 offen offset:0 sc1\n" \
  "v_mfma_f32_16x16x32_bf16 v[40:43], a[116:119], a[40:43], v[40:43]\n" \
  "ds_read_b64_tr_b16 a[140:141], v119 offset:24576\n" \
  "s_add_u32 m0, s86, 98304\n" \
  "v_mfma_f32_16x16x32_bf16 v[44:47], a[116:119], a[44:47], v[44:47]\n" \
  "ds_read_b64_tr_b16 a[142:143], v119 offset:28672\n" \
  "buffer_load_dwordx4 v0, s[56:59], s13 offen offset:0 lds\n" \
  "s_waitcnt lgkmcnt(8)\n" \
  "v_mfma_f32_16x16x32_bf16 v[48:51], a[120:123], a[40:43], v[48:51]\n" \
  "ds_read_b64_tr_b16 a[112:113], v96 offset:0\n" \
  "v_mfma_f32_16x16x32_bf16 v[52:55], a[120:123], a[44:47], v[52:55]\n" \
  "ds_read_b64_tr_b16 a[114:115], v96 offset:4096\n" \
  "buffer_load_dwordx4 a[76:79], v7, s[60:63], s14 offen offset:512 sc1\n" \
  "v_mfma_f32_16x16x32_bf16 v[56:59], a[124:127], a[40:43], v[56:59]\n" \
  "ds_read_b64_tr_b16 a[116:117], v97 offset:0\n" \
  "s_add_u32 s14, s14, 2048\n" \
  "v_mfma_f32_16x16x32_bf16 v[60:63], a[124:127], a[44:47], v[60:63]\n" \
  "ds_read_b64_tr_b16 a[118:119], v97 offset:4096\n" \
  "s_waitcnt lgkmcnt(8)\n" \
  "v_mfma_f32_16x16x32_bf16 v[64:67], a[128:131], a[40:43], v[64:67]\n" \
  "ds_read_b64_tr_b16 a[120:121], v98 offset:0\n" \
  "v_mfma_f32_16x16x32_bf16 v[68:71], a[128:131], a[44:47], v[68:71]\n" \
  "ds_read_b64_tr_b16 a[122:123], v98 offset:4096\n" \
  "buffer_load_dwordx4 v1, s[56:59], s13 offen offset:1024 lds\n" \
  "v_mfma_f32_16x16x32_bf16 v[72:75], a[132:135], a[40:43], v[72:75]\n" \
  "ds_read_b64_tr_b16 a[124:125], v99 offset:0\n" \
  "s_add_u32 s13, s13, 65536\n" \
  "v_mfma_f32_16x16x32_bf16 v[76:79], a[132:135], a[44:47], v[76:79]\n" \
  "ds_read_b64_tr_b16 a[126:127], v99 offset:4096\n" \
  "ds_write_b128 v121, a[168:171] offset:49152\n" \
  "s_waitcnt lgkmcnt(9)\n" \
  "v_mfma_f32_16x16x32_bf16 v[80:83], a[136:139], a[40:43], v[80:83]\n" \
  "ds_read_b64_tr_b16 a[128:129], v100 offset:0\n" \
  "v_mfma_f32_16x16x32_bf16 v[84:87], a[136:139], a[44:47], v[84:87]\n" \
  "ds_read_b64_tr_b16 a[130:131], v100 offset:4096\n" \
  "ds_write_b128 v121, a[172:175] offset:50176\n" \
  "v_mfma_f32_16x16x32_bf16 v[88:91], a[140:143], a[40:43], v[88:91]\n" \
  "ds_read_b64_tr_b16 a[132:133], v101 offset:0\n" \
  "v_mfma_f32_16x16x32_bf16 v[92:95], a[140:143], a[44:47], v[92:95]\n" \
  "ds_read_b64_tr_b16 a[134:135], v101 offset:4096\n" \
  "s_waitcnt vmcnt(15) lgkmcnt(10)\n" \
  "v_mfma_f32_16x16x32_bf16 v[32:35], a[112:115], a[48:51], v[32:35]\n" \
  "ds_read_b64_tr_b16 a[136:137], v102 offset:0\n" \
  "v_mfma_f32_16x16x32_bf16 v[36:39], a[112:115], a[52:55], v[36:39]\n" \
  "ds_read_b64_tr_b16 a[138:139], v102 offset:4096\n" \
  "buffer_load_dwordx4 a[80:83], v7, s[60:63], s14 offen offset:0 sc1\n" \
  "v_mfma_f32_16x16x32_bf16 v[40:43], a[116:119], a[48:51], v[40:43]\n" \
  "ds_read_b64_tr_b16 a[140:141], v103 offset:0\n" \
  "s_add_u32 m0, s86, 106496\n" \
  "v_mfma_f32_16x16x32_bf16 v[44:47], a[116:119], a[52:55], v[44:47]\n" \
  "ds_read_b64_tr_b16 a[142:143], v103 offset:4096\n" \
  "buffer_load_dwordx4 v0, s[56:59], s13 offen offset:0 lds\n" \
  "s_waitcnt lgkmcnt(10)\n" \
  "v_mfma_f32_16x16x32_bf16 v[48:51], a[120:123], a[48:51], v[48:51]\n" \
  "ds_read_b64_tr_b16 a[112:113], v96 offset:8192\n" \
  "v_mfma_f32_16x16x32_bf16 v[52:55], a[120:123], a[52:55], v[52:55]\n" \
  "ds_read_b64_tr_b16 a[114:115], v96 offset:12288\n" \
  "buffer_load_dwordx4 a[84:87], v7, s[60:63], s14 offen offset:512 sc1\n" \
  "v_mfma_f32_16x16x32_bf16 v[56:59], a[124:127], a[48:51], v[56:59]\n" \
  "ds_read_b64_tr_b16 a[116:117], v97 offset:8192\n" \
  "s_add_u32 s14, s14, 2048\n" \
  "v_mfma_f32_16x16x32_bf16 v[60:63], a[124:127], a[52:55], v[60:63]\n" \
  "ds_read_b64_tr_b16 a[118:119], v97 offset:12288\n" \
  "s_waitcnt lgkmcnt(8)\n" \
  "v_mfma_f32_16x16x32_bf16 v[64:67], a[128:131], a[48:51], v[64:67]\n" \
  "ds_read_b64_tr_b16 a[120:121], v98 offset:8192\n" \
  "v_mfma_f32_16x16x32_bf16 v[68:71], a[128:131], a[52:55], v[68:71]\n" \
  "ds_read_b64_tr_b16 a[122:123], v98 offset:12288\n" \
  "buffer_load_dwordx4 v1, s[56:59], s13 offen offset:1024 lds\n" \
  "v_mfma_f32_16x16x32_bf16 v[72:75], a[132:135], a[48:51], v[72:75]\n" \
  "ds_read_b64_tr_b16 a[124:125], v99 offset:8192\n" \
  "s_add_u32 s13, s90, 196608\n" \
  "v_mfma_f32_16x16x32_bf16 v[76:79], a[132:135], a[52:55], v[76:79]\n" \
  "ds_read_b64_tr_b16 a[126:127], v99 offset:12288\n" \
  "ds_write_b128 v121, a[176:179] offset:57344\n" \
  "s_waitcnt lgkmcnt(9)\n" \
  "v_mfma_f32_16x16x32_bf16 v[80:83], a[136:139], a[48:51], v[80:83]\n" \
  "ds_read_b64_tr_b16 a[128:129], v100 offset:8192\n" \
  "v_mfma_f32_16x16x32_bf16 v[84:87], a[136:139], a[52:55], v[84:87]\n" \
  "ds_read_b64_tr_b16 a[130:131], v100 offset:12288\n" \
  "ds_write_b128 v121, a[180:183] offset:58368\n" \
  "v_mfma_f32_16x16x32_bf16 v[88:91], a[140:143], a[48:51], v[88:91]\n" \
  "ds_read_b64_tr_b16 a[132:133], v101 offset:8192\n" \
  "v_mfma_f32_16x16x32_bf16 v[92:95], a[140:143], a[52:55], v[92:95]\n" \
  "ds_read_b64_tr_b16 a[134:135], v101 offset:12288\n" \
  "s_waitcnt vmcnt(13) lgkmcnt(10)\n" \
  "v_mfma_f32_16x16x32_bf16 v[32:35], a[112:115], a[56:59], v[32:35]\n" \
  "ds_read_b64_tr_b16 a[136:137], v102 offset:8192\n" \
  "v_mfma_f32_16x16x32_bf16 v[36:39], a[112:115], a[60:63], v[36:39]\n" \
  "ds_read_b64_tr_b16 a[138:139], v102 offset:12288\n" \
  "buffer_load_dwordx4 a[88:91], v7, s[60:63], s14 offen offset:0 sc1\n" \
  "v_mfma_f32_16x16x32_bf16 v[40:43], a[116:119], a[56:59], v[40:43]\n" \
  "ds_read_b64_tr_b16 a[140:141], v103 offset:8192\n" \
  "v_mfma_f32_16x16x32_bf16 v[44:47], a[116:119], a[60:63], v[44:47]\n" \
  "ds_read_b64_tr_b16 a[142:143], v103 offset:12288\n" \
  "ds_write_b128 v122, a[184:187] offset:0\n" \
  "s_waitcnt lgkmcnt(11)\n" \
  "s_barrier\n" \
  "v_mfma_f32_16x16x32_bf16 v[48:51], a[120:123], a[56:59], v[48:51]\n" \
  "ds_read_b64_tr_b16 a[112:113], v96 offset:16384\n" \
  "v_mfma_f32_16x16x32_bf16 v[52:55], a[120:123], a[60:63], v[52:55]\n" \
  "ds_read_b64_tr_b16 a[114:115], v96 offset:20480\n" \
  "buffer_load_dwordx4 a[92:95], v7, s[60:63], s14 offen offset:512 sc1\n" \
  "v_mfma_f32_16x16x32_bf16 v[56:59], a[124:127], a[56:59], v[56:59]\n" \
  "ds_read_b64_tr_b16 a[116:117], v97 offset:16384\n" \
  "s_add_u32 s14, s14, 2048\n" \
  "v_mfma_f32_16x16x32_bf16 v[60:63], a[124:127], a[60:63], v[60:63]\n" \
  "ds_read_b64_tr_b16 a[118:119], v97 offset:20480\n" \
  "s_waitcnt lgkmcnt(9)\n" \
  "v_mfma_f32_16x16x32_bf16 v[64:67], a[128:131], a[56:59], v[64:67]\n" \
  "ds_read_b64_tr_b16 a[120:121], v98 offset:16384\n" \
  "v_mfma_f32_16x16x32_bf16 v[68:71], a[128:131], a[60:63], v[68:71]\n" \
  "ds_read_b64_tr_b16 a[122:123], v98 offset:20480\n" \
  "ds_write_b128 v122, a[188:191] offset:1024\n" \
  "v_mfma_f32_16x16x32_bf16 v[72:75], a[132:135], a[56:59], v[72:75]\n" \
  "ds_read_b64_tr_b16 a[124:125], v99 offset:16384\n" \
  "v_mfma_f32_16x16x32_bf16 v[76:79], a[132:135], a[60:63], v[76:79]\n" \
  "ds_read_b64_tr_b16 a[126:127], v99 offset:20480\n" \
  "buffer_load_dwordx4 a[96:99], v7, s[60:63], s14 offen offset:0 sc1\n" \
  "s_waitcnt lgkmcnt(10)\n" \
  "v_mfma_f32_16x16x32_bf16 v[80:83], a[136:139], a[56:59], v[80:83]\n" \
  "ds_read_b64_tr_b16 a[128:129], v100 offset:16384\n" \
  "v_mfma_f32_16x16x32_bf16 v[84:87], a[136:139], a[60:63], v[84:87]\n" \
  "ds_read_b64_tr_b16 a[130:131], v100 offset:20480\n" \
  "buffer_load_dwordx4 a[100:103], v7, s[60:63], s14 offen offset:512 sc1\n" \
  "v_mfma_f32_16x16x32_bf16 v[88:91], a[140:143], a[56:59], v[88:91]\n" \
  "ds_read_b64_tr_b16 a[132:133], v101 offset:16384\n" \
  "s_add_u32 s14, s14, 2048\n" \
  "v_mfma_f32_16x16x32_bf16 v[92:95], a[140:143], a[60:63], v[92:95]\n" \
  "ds_read_b64_tr_b16 a[134:135], v101 offset:20480\n" \
  "s_waitcnt vmcnt(13) lgkmcnt(9)\n" \
  "v_mfma_f32_16x16x32_bf16 v[32:35], a[112:115], a[64:67], v[32:35]\n" \
  "ds_read_b64_tr_b16 a[136:137], v102 offset:16384\n" \
  "v_mfma_f32_16x16x32_bf16 v[36:39], a[112:115], a[68:71], v[36:39]\n" \
  "ds_read_b64_tr_b16 a[138:139], v102 offset:20480\n" \
  "buffer_load_dwordx4 a[104:107], v7, s[60:63], s14 offen offset:0 sc1\n" \
  "v_mfma_f32_16x16x32_bf16 v[40:43], a[116:119], a[64:67], v[40:43]\n" \
  "ds_read_b64_tr_b16 a[140:141], v103 offset:16384\n" \
  "v_mfma_f32_16x16x32_bf16 v[44:47], a[116:119], a[68:71], v[44:47]\n" \
  "ds_read_b64_tr_b16 a[142:143], v103 offset:20480\n" \
  "ds_write_b128 v122, a[192:195] offset:8192\n" \
  "s_waitcnt lgkmcnt(9)\n" \
  "v_mfma_f32_16x16x32_bf16 v[48:51], a[120:123], a[64:67], v[48:51]\n" \
  "ds_read_b64_tr_b16 a[112:113], v96 offset:24576\n" \
  "v_mfma_f32_16x16x32_bf16 v[52:55], a[120:123], a[68:71], v[52:55]\n" \
  "ds_read_b64_tr_b16 a[114:115], v96 offset:28672\n" \
  "buffer_load_dwordx4 a[108:111], v7, s[60:63], s14 offen offset:512 sc1\n" \
  "v_mfma_f32_16x16x32_bf16 v[56:59], a[124:127], a[64:67], v[56:59]\n" \
  "ds_read_b64_tr_b16 a[116:117], v97 offset:24576\n" \
  "s_add_u32 s14, s14, 2048\n" \
  "v_mfma_f32_16x16x32_bf16 v[60:63], a[124:127], a[68:71], v[60:63]\n" \
  "ds_read_b64_tr_b16 a[118:119], v97 offset:28672\n" \
  "s_waitcnt lgkmcnt(9)\n" \
  "v_mfma_f32_16x16x32_bf16 v[64:67], a[128:131], a[64:67], v[64:67]\n" \
  "ds_read_b64_tr_b16 a[120:121], v98 offset:24576\n" \
  "v_mfma_f32_16x16x32_bf16 v[68:71], a[128:131], a[68:71], v[68:71]\n" \
  "ds_read_b64_tr_b16 a[122:123], v98 offset:28672\n" \
  "ds_write_b128 v122, a[196:199] offset:9216\n" \
  "v_mfma_f32_16x16x32_bf16 v[72:75], a[132:135], a[64:67], v[72:75]\n" \
  "ds_read_b64_tr_b16 a[124:125], v99 offset:24576\n" \
  "v_mfma_f32_16x16x32_bf16 v[76:79], a[132:135], a[68:71], v[76:79]\n" \
  "ds_read_b64_tr_b16 a[126:127], v99 offset:28672\n" \
  "buffer_load_dwordx4 a[0:3], v7, s[60:63], s14 offen offset:0 sc1\n" \
  "s_waitcnt lgkmcnt(10)\n" \
  "v_mfma_f32_16x16x32_bf16 v[80:83], a[136:139], a[64:67], v[80:83]\n" \
  "ds_read_b64_tr_b16 a[128:129], v100 offset:24576\n" \
  "v_mfma_f32_16x16x32_bf16 v[84:87], a[136:139], a[68:71], v[84:87]\n" \
  "ds_read_b64_tr_b16 a[130:131], v100 offset:28672\n" \
  "v_mfma_f32_16x16x32_bf16 v[88:91], a[140:143], a[64:67], v[88:91]\n" \
  "ds_read_b64_tr_b16 a[132:133], v101 offset:24576\n" \
  "v_mfma_f32_16x16x32_bf16 v[92:95], a[140:143], a[68:71], v[92:95]\n" \
  "ds_read_b64_tr_b16 a[134:135], v101 offset:28672\n" \
  "s_waitcnt vmcnt(12) lgkmcnt(9)\n" \
  "v_mfma_f32_16x16x32_bf16 v[32:35], a[112:115], a[72:75], v[32:35]\n" \
  "ds_read_b64_tr_b16 a[136:137], v102 offset:24576\n" \
  "v_mfma_f32_16x16x32_bf16 v[36:39], a[112:115], a[76:79], v[36:39]\n" \
  "ds_read_b64_tr_b16 a[138:139], v102 offset:28672\n" \
  "buffer_load_dwordx4 a[4:7], v7, s[60:63], s14 offen offset:512 sc1\n" \
  "v_mfma_f32_16x16x32_bf16 v[40:43], a[116:119], a[72:75], v[40:43]\n" \
  "ds_read_b64_tr_b16 a[140:141], v103 offset:24576\n" \
  "s_add_u32 s14, s14, 2048\n" \
  "v_mfma_f32_16x16x32_bf16 v[44:47], a[116:119], a[76:79], v[44:47]\n" \
  "ds_read_b64_tr_b16 a[142:143], v103 offset:28672\n" \
  "ds_write_b128 v122, a[200:203] offset:16384\n" \
  "s_waitcnt lgkmcnt(9)\n" \
  "v_mfma_f32_16x16x32_bf16 v[48:51], a[120:123], a[72:75], v[48:51]\n" \
  "ds_read_b64_tr_b16 a[112:113], v96 offset:32768\n" \
  "v_mfma_f32_16x16x32_bf16 v[52:55], a[120:123], a[76:79], v[52:55]\n" \
  "ds_read_b64_tr_b16 a[114:115], v96 offset:36864\n" \
  "buffer_load_dwordx4 a[8:11], v7, s[60:63], s14 offen offset:0 sc1\n" \
  "v_mfma_f32_16x16x32_bf16 v[56:59], a[124:127], a[72:75], v[56:59]\n" \
  "ds_read_b64_tr_b16 a[116:117], v97 offset:32768\n" \
  "v_mfma_f32_16x16x32_bf16 v[60:63], a[124:127], a[76:79], v[60:63]\n" \
  "ds_read_b64_tr_b16 a[118:119], v97 offset:36864\n" \
  "s_waitcnt lgkmcnt(9)\n" \
  "v_mfma_f32_16x16x32_bf16 v[64:67], a[128:131], a[72:75], v[64:67]\n" \
  "ds_read_b64_tr_b16 a[120:121], v98 offset:32768\n" \
  "v_mfma_f32_16x16x32_bf16 v[68:71], a[128:131], a[76:79], v[68:71]\n" \
  "ds_read_b64_tr_b16 a[122:123], v98 offset:36864\n" \
  "ds_write_b128 v122, a[204:207] offset:17408\n" \
  "v_mfma_f32_16x16x32_bf16 v[72:75], a[132:135], a[72:75], v[72:75]\n" \
  "ds_read_b64_tr_b16 a[124:125], v99 offset:32768\n" \
  "v_mfma_f32_16x16x32_bf16 v[76:79], a[132:135], a[76:79], v[76:79]\n" \
  "ds_read_b64_tr_b16 a[126:127], v99 offset:36864\n" \
  "buffer_load_dwordx4 a[12:15], v7, s[60:63], s14 offen offset:512 sc1\n" \
  "s_waitcnt lgkmcnt(10)\n" \
  "v_mfma_f32_16x16x32_bf16 v[80:83], a[136:139], a[72:75], v[80:83]\n" \
  "ds_read_b64_tr_b16 a[128:129], v100 offset:32768\n" \
  "s_add_u32 s14, s14, 2048\n" \
  "v_mfma_f32_16x16x32_bf16 v[84:87], a[136:139], a[76:79], v[84:87]\n" \
  "ds_read_b64_tr_b16 a[130:131], v100 offset:36864\n" \
  "buffer_load_dwordx4 a[16:19], v7, s[60:63], s14 offen offset:0 sc1\n" \
  "v_mfma_f32_16x16x32_bf16 v[88:91], a[140:143], a[72:75], v[88:91]\n" \
  "ds_read_b64_tr_b16 a[132:133], v101 offset:32768\n" \
  "v_mfma_f32_16x16x32_bf16 v[92:95], a[140:143], a[76:79], v[92:95]\n" \
  "ds_read_b64_tr_b16 a[134:135], v101 offset:36864\n" \
  "s_waitcnt vmcnt(12) lgkmcnt(9)\n" \
  "v_mfma_f32_16x16x32_bf16 v[32:35], a[112:115], a[80:83], v[32:35]\n" \
  "ds_read_b64_tr_b16 a[136:137], v102 offset:32768\n" \
  "v_mfma_f32_16x16x32_bf16 v[36:39], a[112:115], a[84:87], v[36:39]\n" \
  "ds_read_b64_tr_b16 a[138:139], v102 offset:36864\n" \
  "buffer_load_dword v30, v5, s[68:71], 0 offen offset:128 sc1\n" \
  "v_mfma_f32_16x16x32_bf16 v[40:43], a[116:119], a[80:83], v[40:43]\n" \
  "ds_read_b64_tr_b16 a[140:141], v103 offset:32768\n" \
  "v_mfma_f32_16x16x32_bf16 v[44:47], a[116:119], a[84:87], v[44:47]\n" \
  "ds_read_b64_tr_b16 a[142:143], v103 offset:36864\n" \
  "buffer_load_dwordx4 a[20:23], v7, s[60:63], s14 offen offset:512 sc1\n" \
  "s_waitcnt lgkmcnt(8)\n" \
  "v_mfma_f32_16x16x32_bf16 v[48:51], a[120:123], a[80:83], v[48:51]\n" \
  "ds_read_b64_tr_b16 a[112:113], v96 offset:40960\n" \
  "s_add_u32 s14, s14, 2048\n" \
  "v_mfma_f32_16x16x32_bf16 v[52:55], a[120:123], a[84:87], v[52:55]\n" \
  "ds_read_b64_tr_b16 a[114:115], v96 offset:45056\n" \
  "ds_write_b128 v122, a[208:211] offset:24576\n" \
  "v_mfma_f32_16x16x32_bf16 v[56:59], a[124:127], a[80:83], v[56:59]\n" \
  "ds_read_b64_tr_b16 a[116:117], v97 offset:40960\n" \
  "v_mfma_f32_16x16x32_bf16 v[60:63], a[124:127], a[84:87], v[60:63]\n" \
  "ds_read_b64_tr_b16 a[118:119], v97 offset:45056\n" \
  "s_waitcnt lgkmcnt(9)\n" \
  "v_mfma_f32_16x16x32_bf16 v[64:67], a[128:131], a[80:83], v[64:67]\n" \
  "ds_read_b64_tr_b16 a[120:121], v98 offset:40960\n" \
  "v_mfma_f32_16x16x32_bf16 v[68:71], a[128:131], a[84:87], v[68:71]\n" \
  "ds_read_b64_tr_b16 a[122:123], v98 offset:45056\n" \
  "buffer_load_dwordx4 a[24:27], v7, s[60:63], s14 offen offset:0 sc1\n" \
  "v_mfma_f32_16x16x32_bf16 v[72:75], a[132:135], a[80:83], v[72:75]\n" \
  "ds_read_b64_tr_b16 a[124:125], v99 offset:40960\n" \
  "v_mfma_f32_16x16x32_bf16 v[76:79], a[132:135], a[84:87], v[76:79]\n" \
  "ds_read_b64_tr_b16 a[126:127], v99 offset:45056\n" \
  "ds_write_b128 v122, a[212:215] offset:25600\n" \
  "s_waitcnt lgkmcnt(10)\n" \
  "v_mfma_f32_16x16x32_bf16 v[80:83], a[136:139], a[80:83], v[80:83]\n" \
  "ds_read_b64_tr_b16 a[128:129], v100 offset:40960\n" \
  "v_mfma_f32_16x16x32_bf16 v[84:87], a[136:139], a[84:87], v[84:87]\n" \
  "ds_read_b64_tr_b16 a[130:131], v100 offset:45056\n" \
  "v_mfma_f32_16x16x32_bf16 v[88:91], a[140:143], a[80:83], v[88:91]\n" \
  "ds_read_b64_tr_b16 a[132:133], v101 offset:40960\n" \
  "v_mfma_f32_16x16x32_bf16 v[92:95], a[140:143], a[84:87], v[92:95]\n" \
  "ds_read_b64_tr_b16 a[134:135], v101 offset:45056\n" \
  "s_waitcnt vmcnt(12) lgkmcnt(9)\n" \
  "v_mfma_f32_16x16x32_bf16 v[32:35], a[112:115], a[88:91], v[32:35]\n" \
  "ds_read_b64_tr_b16 a[136:137], v102 offset:40960\n" \
  "v_mfma_f32_16x16x32_bf16 v[36:39], a[112:115], a[92:95], v[36:39]\n" \
  "ds_read_b64_tr_b16 a[138:139], v102 offset:45056\n" \
  "buffer_load_dwordx4 a[28:31], v7, s[60:63], s14 offen offset:512 sc1\n" \
  "v_mfma_f32_16x16x32_bf16 v[40:43], a[116:119], a[88:91], v[40:43]\n" \
  "ds_read_b64_tr_b16 a[140:141], v103 offset:40960\n" \
  "s_add_u32 s14, s14, 2048\n" \
  "v_mfma_f32_16x16x32_bf16 v[44:47], a[116:119], a[92:95], v[44:47]\n" \
  "ds_read_b64_tr_b16 a[142:143], v103 offset:45056\n" \
  "ds_write_b128 v120, a[216:219] offset:0\n" \
  "s_waitcnt lgkmcnt(10)\n" \
  "s_barrier\n" \
  "v_mfma_f32_16x16x32_bf16 v[48:51], a[120:123], a[88:91], v[48:51]\n" \
  "ds_read_b64_tr_b16 a[112:113], v96 offset:49152\n" \
  "v_mfma_f32_16x16x32_bf16 v[52:55], a[120:123], a[92:95], v[52:55]\n" \
  "ds_read_b64_tr_b16 a[114:115], v96 offset:53248\n" \
  "buffer_load_dwordx4 a[32:35], v7, s[60:63], s14 offen offset:0 sc1\n" \
  "v_mfma_f32_16x16x32_bf16 v[56:59], a[124:127], a[88:91], v[56:59]\n" \
  "ds_read_b64_tr_b16 a[116:117], v97 offset:49152\n" \
  "v_mfma_f32_16x16x32_bf16 v[60:63], a[124:127], a[92:95], v[60:63]\n" \
  "ds_read_b64_tr_b16 a[118:119], v97 offset:53248\n" \
  "s_waitcnt lgkmcnt(9)\n" \
  "v_mfma_f32_16x16x32_bf16 v[64:67], a[128:131], a[88:91], v[64:67]\n" \
  "ds_read_b64_tr_b16 a[120:121], v98 offset:49152\n" \
  "v_mfma_f32_16x16x32_bf16 v[68:71], a[128:131], a[92:95], v[68:71]\n" \
  "ds_read_b64_tr_b16 a[122:123], v98 offset:53248\n" \
  "ds_write_b128 v120, a[220:223] offset:1024\n" \
  "v_mfma_f32_16x16x32_bf16 v[72:75], a[132:135], a[88:91], v[72:75]\n" \
  "ds_read_b64_tr_b16 a[124:125], v99 offset:49152\n" \
  "v_mfma_f32_16x16x32_bf16 v[76:79], a[132:135], a[92:95], v[76:79]\n" \
  "ds_read_b64_tr_b16 a[126:127], v99 offset:53248\n" \
  "buffer_load_dwordx4 a[36:39], v7, s[60:63], s14 offen offset:512 sc1\n" \
  "s_waitcnt lgkmcnt(10)\n" \
  "v_mfma_f32_16x16x32_bf16 v[80:83], a[136:139], a[88:91], v[80:83]\n" \
  "ds_read_b64_tr_b16 a[128:129], v100 offset:49152\n" \
  "s_add_u32 s14, s14, 2048\n" \
  "v_mfma_f32_16x16x32_bf16 v[84:87], a[136:139], a[92:95], v[84:87]\n" \
  "ds_read_b64_tr_b16 a[130:131], v100 offset:53248\n" \
  "buffer_load_dwordx4 a[40:43], v7, s[60:63], s14 offen offset:0 sc1\n" \
  "v_mfma_f32_16x16x32_bf16 v[88:91], a[140:143], a[88:91], v[88:91]\n" \
  "ds_read_b64_tr_b16 a[132:133], v101 offset:49152\n" \
  "v_mfma_f32_16x16x32_bf16 v[92:95], a[140:143], a[92:95], v[92:95]\n" \
  "ds_read_b64_tr_b16 a[134:135], v101 offset:53248\n" \
  "s_waitcnt vmcnt(14) lgkmcnt(9)\n" \
  "v_mfma_f32_16x16x32_bf16 v[32:35], a[112:115], a[96:99], v[32:35]\n" \
  "ds_read_b64_tr_b16 a[136:137], v102 offset:49152\n" \
  "v_mfma_f32_16x16x32_bf16 v[36:39], a[112:115], a[100:103], v[36:39]\n" \
  "ds_read_b64_tr_b16 a[138:139], v102 offset:53248\n" \
  "buffer_load_dwordx4 a[44:47], v7, s[60:63], s14 offen offset:512 sc1\n" \
  "v_mfma_f32_16x16x32_bf16 v[40:43], a[116:119], a[96:99], v[40:43]\n" \
  "ds_read_b64_tr_b16 a[140:141], v103 offset:49152\n" \
  "s_add_u32 s14, s14, 2048\n" \
  "v_mfma_f32_16x16x32_bf16 v[44:47], a[116:119], a[100:103], v[44:47]\n" \
  "ds_read_b64_tr_b16 a[142:143], v103 offset:53248\n" \
  "s_waitcnt lgkmcnt(8)\n" \
  "v_mfma_f32_16x16x32_bf16 v[48:51], a[120:123], a[96:99], v[48:51]\n" \
  "ds_read_b64_tr_b16 a[112:113], v96 offset:57344\n" \
  "s_add_u32 m0, s86, 8192\n" \
  "v_mfma_f32_16x16x32_bf16 v[52:55], a[120:123], a[100:103], v[52:55]\n" \
  "ds_read_b64_tr_b16 a[114:115], v96 offset:61440\n" \
  "buffer_load_dwordx4 v0, s[56:59], s13 offen offset:0 lds\n" \
  "v_mfma_f32_16x16x32_bf16 v[56:59], a[124:127], a[96:99], v[56:59]\n" \
  "ds_read_b64_tr_b16 a[116:117], v97 offset:57344\n" \
  "v_mfma_f32_16x16x32_bf16 v[60:63], a[124:127], a[100:103], v[60:63]\n" \
  "ds_read_b64_tr_b16 a[118:119], v97 offset:61440\n" \
  "s_waitcnt lgkmcnt(8)\n" \
  "v_mfma_f32_16x16x32_bf16 v[64:67], a[128:131], a[96:99], v[64:67]\n" \
  "ds_read_b64_tr_b16 a[120:121], v98 offset:57344\n" \
  "v_mfma_f32_16x16x32_bf16 v[68:71], a[128:131], a[100:103], v[68:71]\n" \
  "ds_read_b64_tr_b16 a[122:123], v98 offset:61440\n" \
  "buffer_load_dwordx4 v1, s[56:59], s13 offen offset:1024 lds\n" \
  "v_mfma_f32_16x16x32_bf16 v[72:75], a[132:135], a[96:99], v[72:75]\n" \
  "ds_read_b64_tr_b16 a[124:125], v99 offset:57344\n" \
  "s_add_u32 s13, s13, 65536\n" \
  "v_mfma_f32_16x16x32_bf16 v[76:79], a[132:135], a[100:103], v[76:79]\n" \
  "ds_read_b64_tr_b16 a[126:127], v99 offset:61440\n" \
  "s_waitcnt lgkmcnt(8)\n" \
  "v_mfma_f32_16x16x32_bf16 v[80:83], a[136:139], a[96:99], v[80:83]\n" \
  "ds_read_b64_tr_b16 a[128:129], v100 offset:57344\n" \
  "v_mfma_f32_16x16x32_bf16 v[84:87], a[136:139], a[100:103], v[84:87]\n" \
  "ds_read_b64_tr_b16 a[130:131], v100 offset:61440\n" \
  "v_mfma_f32_16x16x32_bf16 v[88:91], a[140:143], a[96:99], v[88:91]\n" \
  "ds_read_b64_tr_b16 a[132:133], v101 offset:57344\n" \
  "v_mfma_f32_16x16x32_bf16 v[92:95], a[140:143], a[100:103], v[92:95]\n" \
  "ds_read_b64_tr_b16 a[134:135], v101 offset:61440\n" \
  "s_waitcnt vmcnt(15) lgkmcnt(8)\n" \
  "v_mfma_f32_16x16x32_bf16 v[32:35], a[112:115], a[104:107], v[32:35]\n" \
  "ds_read_b64_tr_b16 a[136:137], v102 offset:57344\n" \
  "v_mfma_f32_16x16x32_bf16 v[36:39], a[112:115], a[108:111], v[36:39]\n" \
  "ds_read_b64_tr_b16 a[138:139], v102 offset:61440\n" \
  "buffer_load_dwordx4 a[48:51], v7, s[60:63], s14 offen offset:0 sc1\n" \
  "v_mfma_f32_16x16x32_bf16 v[40:43], a[116:119], a[104:107], v[40:43]\n" \
  "ds_read_b64_tr_b16 a[140:141], v103 offset:57344\n" \
  "v_mfma_f32_16x16x32_bf16 v[44:47], a[116:119], a[108:111], v[44:47]\n" \
  "ds_read_b64_tr_b16 a[142:143], v103 offset:61440\n" \
  "s_waitcnt lgkmcnt(8)\n" \
  "v_mfma_f32_16x16x32_bf16 v[48:51], a[120:123], a[104:107], v[48:51]\n" \
  "ds_read_b64_tr_b16 a[112:113], v104 offset:0\n" \
  "s_add_u32 m0, s86, 16384\n" \
  "v_mfma_f32_16x16x32_bf16 v[52:55], a[120:123], a[108:111], v[52:55]\n" \
  "ds_read_b64_tr_b16 a[114:115], v104 offset:4096\n" \
  "buffer_load_dwordx4 v0, s[56:59], s13 offen offset:0 lds\n" \
  "v_mfma_f32_16x16x32_bf16 v[56:59], a[124:127], a[104:107], v[56:59]\n" \
  "ds_read_b64_tr_b16 a[116:117], v105 offset:0\n" \
  "v_mfma_f32_16x16x32_bf16 v[60:63], a[124:127], a[108:111], v[60:63]\n" \
  "ds_read_b64_tr_b16 a[118:119], v105 offset:4096\n" \
  "s_waitcnt lgkmcnt(8)\n" \
  "v_mfma_f32_16x16x32_bf16 v[64:67], a[128:131], a[104:107], v[64:67]\n" \
  "ds_read_b64_tr_b16 a[120:121], v106 offset:0\n" \
  "v_mfma_f32_16x16x32_bf16 v[68:71], a[128:131], a[108:111], v[68:71]\n" \
  "ds_read_b64_tr_b16 a[122:123], v106 offset:4096\n" \
  "buffer_load_dwordx4 a[52:55], v7, s[60:63], s14 offen offset:512 sc1\n" \
  "v_mfma_f32_16x16x32_bf16 v[72:75], a[132:135], a[104:107], v[72:75]\n" \
  "ds_read_b64_tr_b16 a[124:125], v107 offset:0\n" \
  "s_add_u32 s14, s14, 2048\n" \
  "v_mfma_f32_16x16x32_bf16 v[76:79], a[132:135], a[108:111], v[76:79]\n" \
  "ds_read_b64_tr_b16 a[126:127], v107 offset:4096\n" \
  "s_waitcnt lgkmcnt(8)\n" \
  "v_mfma_f32_16x16x32_bf16 v[80:83], a[136:139], a[104:107], v[80:83]\n" \
  "ds_read_b64_tr_b16 a[128:129], v108 offset:0\n" \
  "v_mfma_f32_16x16x32_bf16 v[84:87], a[136:139], a[108:111], v[84:87]\n" \
  "ds_read_b64_tr_b16 a[130:131], v108 offset:4096\n" \
  "buffer_load_dwordx4 v1, s[56:59], s13 offen offset:1024 lds\n" \
  "v_mfma_f32_16x16x32_bf16 v[88:91], a[140:143], a[104:107], v[88:91]\n" \
  "ds_read_b64_tr_b16 a[132:133], v109 offset:0\n" \
  "s_add_u32 s13, s13, 65536\n" \
  "v_mfma_f32_16x16x32_bf16 v[92:95], a[140:143], a[108:111], v[92:95]\n" \
  "ds_read_b64_tr_b16 a[134:135], v109 offset:4096\n" \
  "s_waitcnt vmcnt(17) lgkmcnt(8)\n" \
  "v_mfma_f32_16x16x32_bf16 v[32:35], a[112:115], a[0:3], v[32:35]\n" \
  "ds_read_b64_tr_b16 a[136:137], v110 offset:0\n" \
  "v_mfma_f32_16x16x32_bf16 v[36:39], a[112:115], a[4:7], v[36:39]\n" \
  "ds_read_b64_tr_b16 a[138:139], v110 offset:4096\n" \
  "buffer_load_dwordx4 a[56:59], v7, s[60:63], s14 offen offset:0 sc1\n" \
  "v_mfma_f32_16x16x32_bf16 v[40:43], a[116:119], a[0:3], v[40:43]\n" \
  "ds_read_b64_tr_b16 a[140:141], v111 offset:0\n" \
  "v_mfma_f32_16x16x32_bf16 v[44:47], a[116:119], a[4:7], v[44:47]\n" \
  "ds_read_b64_tr_b16 a[142:143], v111 offset:4096\n" \
  "s_waitcnt lgkmcnt(8)\n" \
  "v_mfma_f32_16x16x32_bf16 v[48:51], a[120:123], a[0:3], v[48:51]\n" \
  "ds_read_b64_tr_b16 a[112:113], v104 offset:8192\n" \
  "s_add_u32 m0, s86, 24576\n" \
  "v_mfma_f32_16x16x32_bf16 v[52:55], a[120:123], a[4:7], v[52:55]\n" \
  "ds_read_b64_tr_b16 a[114:115], v104 offset:12288\n" \
  "buffer_load_dwordx4 v0, s[56:59], s13 offen offset:0 lds\n" \
  "v_mfma_f32_16x16x32_bf16 v[56:59], a[124:127], a[0:3], v[56:59]\n" \
  "ds_read_b64_tr_b16 a[116:117], v105 offset:8192\n" \
  "v_mfma_f32_16x16x32_bf16 v[60:63], a[124:127], a[4:7], v[60:63]\n" \
  "ds_read_b64_tr_b16 a[118:119], v105 offset:12288\n" \
  "s_waitcnt lgkmcnt(8)\n" \
  "v_mfma_f32_16x16x32_bf16 v[64:67], a[128:131], a[0:3], v[64:67]\n" \
  "ds_read_b64_tr_b16 a[120:121], v106 offset:8192\n" \
  "v_mfma_f32_16x16x32_bf16 v[68:71], a[128:131], a[4:7], v[68:71]\n" \
  "ds_read_b64_tr_b16 a[122:123], v106 offset:12288\n" \
  "buffer_load_dwordx4 v1, s[56:59], s13 offen offset:1024 lds\n" \
  "v_mfma_f32_16x16x32_bf16 v[72:75], a[132:135], a[0:3], v[72:75]\n" \
  "ds_read_b64_tr_b16 a[124:125], v107 offset:8192\n" \
  "s_add_u32 s13, s13, 65536\n" \
  "v_mfma_f32_16x16x32_bf16 v[76:79], a[132:135], a[4:7], v[76:79]\n" \
  "ds_read_b64_tr_b16 a[126:127], v107 offset:12288\n" \
  "s_waitcnt lgkmcnt(8)\n" \
  "v_mfma_f32_16x16x32_bf16 v[80:83], a[136:139], a[0:3], v[80:83]\n" \
  "ds_read_b64_tr_b16 a[128:129], v108 offset:8192\n" \
  "v_mfma_f32_16x16x32_bf16 v[84:87], a[136:139], a[4:7], v[84:87]\n" \
  "ds_read_b64_tr_b16 a[130:131], v108 offset:12288\n" \
  "v_mfma_f32_16x16x32_bf16 v[88:91], a[140:143], a[0:3], v[88:91]\n" \
  "ds_read_b64_tr_b16 a[132:133], v109 offset:8192\n" \
  "v_mfma_f32_16x16x32_bf16 v[92:95], a[140:143], a[4:7], v[92:95]\n" \
  "ds_read_b64_tr_b16 a[134:135], v109 offset:12288\n" \
  "s_waitcnt vmcnt(18) lgkmcnt(8)\n" \
  "v_mfma_f32_16x16x32_bf16 v[32:35], a[112:115], a[8:11], v[32:35]\n" \
  "ds_read_b64_tr_b16 a[136:137], v110 offset:8192\n" \
  "v_mfma_f32_16x16x32_bf16 v[36:39], a[112:115], a[12:15], v[36:39]\n" \
  "ds_read_b64_tr_b16 a[138:139], v110 offset:12288\n" \
  "buffer_load_dwordx4 a[60:63], v7, s[60:63], s14 offen offset:512 sc1\n" \
  "v_mfma_f32_16x16x32_bf16 v[40:43], a[116:119], a[8:11], v[40:43]\n" \
  "ds_read_b64_tr_b16 a[140:141], v111 offset:8192\n" \
  "s_add_u32 s14, s14, 2048\n" \
  "v_mfma_f32_16x16x32_bf16 v[44:47], a[116:119], a[12:15], v[44:47]\n" \
  "ds_read_b64_tr_b16 a[142:143], v111 offset:12288\n" \
  "s_waitcnt lgkmcnt(8)\n" \
  "s_barrier\n" \
  "v_mfma_f32_16x16x32_bf16 v[48:51], a[120:123], a[8:11], v[48:51]\n" \
  "ds_read_b64_tr_b16 a[112:113], v104 offset:16384\n" \
  "s_add_u32 m0, s86, 32768\n" \
  "v_mfma_f32_16x16x32_bf16 v[52:55], a[120:123], a[12:15], v[52:55]\n" \
  "ds_read_b64_tr_b16 a[114:115], v104 offset:20480\n" \
  "buffer_load_dwordx4 v0, s[56:59], s13 offen offset:0 lds\n" \
  "v_mfma_f32_16x16x32_bf16 v[56:59], a[124:127], a[8:11], v[56:59]\n" \
  "ds_read_b64_tr_b16 a[116:117], v105 offset:16384\n" \
  "v_mfma_f32_16x16x32_bf16 v[60:63], a[124:127], a[12:15], v[60:63]\n" \
  "ds_read_b64_tr_b16 a[118:119], v105 offset:20480\n" \
  "s_waitcnt lgkmcnt(8)\n" \
  "v_mfma_f32_16x16x32_bf16 v[64:67], a[128:131], a[8:11], v[64:67]\n" \
  "ds_read_b64_tr_b16 a[120:121], v106 offset:16384\n" \
  "v_mfma_f32_16x16x32_bf16 v[68:71], a[128:131], a[12:15], v[68:71]\n" \
  "ds_read_b64_tr_b16 a[122:123], v106 offset:20480\n" \
  "buffer_load_dwordx4 a[64:67], v7, s[60:63], s14 offen offset:0 sc1\n" \
  "v_mfma_f32_16x16x32_bf16 v[72:75], a[132:135], a[8:11], v[72:75]\n" \
  "ds_read_b64_tr_b16 a[124:125], v107 offset:16384\n" \
  "v_mfma_f32_16x16x32_bf16 v[76:79], a[132:135], a[12:15], v[76:79]\n" \
  "ds_read_b64_tr_b16 a[126:127], v107 offset:20480\n" \
  "s_waitcnt lgkmcnt(8)\n" \
  "v_mfma_f32_16x16x32_bf16 v[80:83], a[136:139], a[8:11], v[80:83]\n" \
  "ds_read_b64_tr_b16 a[128:129], v108 offset:16384\n" \
  "v_mfma_f32_16x16x32_bf16 v[84:87], a[136:139], a[12:15], v[84:87]\n" \
  "ds_read_b64_tr_b16 a[130:131], v108 offset:20480\n" \
  "buffer_load_dwordx4 v1, s[56:59], s13 offen offset:1024 lds\n" \
  "v_mfma_f32_16x16x32_bf16 v[88:91], a[140:143], a[8:11], v[88:91]\n" \
  "ds_read_b64_tr_b16 a[132:133], v109 offset:16384\n" \
  "s_add_u32 s13, s13, 65536\n" \
  "v_mfma_f32_16x16x32_bf16 v[92:95], a[140:143], a[12:15], v[92:95]\n" \
  "ds_read_b64_tr_b16 a[134:135], v109 offset:20480\n" \
  "s_waitcnt vmcnt(19) lgkmcnt(8)\n" \
  "v_cmp_eq_u32 vcc, 0, v30\n" \
  "s_cbranch_vccz 6f\n" \
  "s_mov_b32 s15, 0x400000\n" \
  "5:\n" \
  "buffer_load_dword v30, v5, s[68:71], 0 offen offset:128 sc1\n" \
  "s_waitcnt vmcnt(0)\n" \
  "v_cmp_eq_u32 vcc, 0, v30\n" \
  "s_cbranch_vccz 6f\n" \
  "s_sub_u32 s15, s15, 1\n" \
  "s_cmp_lg_u32 s15, 0\n" \
  "s_cbranch_scc1 5b\n" \
  "6:\n" \
  "v_mfma_f32_16x16x32_bf16 v[32:35], a[112:115], a[16:19], v[32:35]\n" \
  "ds_read_b64_tr_b16 a[136:137], v110 offset:16384\n" \
  "v_mfma_f32_16x16x32_bf16 v[36:39], a[112:115], a[20:23], v[36:39]\n" \
  "ds_read_b64_tr_b16 a[138:139], v110 offset:20480\n" \
  "buffer_load_dwordx4 a[68:71], v7, s[60:63], s14 offen offset:512 sc1\n" \
  "v_mfma_f32_16x16x32_bf16 v[40:43], a[116:119], a[16:19], v[40:43]\n" \
  "ds_read_b64_tr_b16 a[140:141], v111 offset:16384\n" \
  "s_add_u32 s14, s14, 2048\n" \
  "v_mfma_f32_16x16x32_bf16 v[44:47], a[116:119], a[20:23], v[44:47]\n" \
  "ds_read_b64_tr_b16 a[142:143], v111 offset:20480\n" \
  "s_waitcnt lgkmcnt(8)\n" \
  "v_mfma_f32_16x16x32_bf16 v[48:51], a[120:123], a[16:19], v[48:51]\n" \
  "ds_read_b64_tr_b16 a[112:113], v104 offset:24576\n" \
  "s_add_u32 m0, s86, 40960\n" \
  "v_mfma_f32_16x16x32_bf16 v[52:55], a[120:123], a[20:23], v[52:55]\n" \
  "ds_read_b64_tr_b16 a[114:115], v104 offset:28672\n" \
  "buffer_load_dwordx4 v0, s[56:59], s13 offen offset:0 lds\n" \
  "v_mfma_f32_16x16x32_bf16 v[56:59], a[124:127], a[16:19], v[56:59]\n" \
  "ds_read_b64_tr_b16 a[116:117], v105 offset:24576\n" \
  "v_mfma_f32_16x16x32_bf16 v[60:63], a[124:127], a[20:23], v[60:63]\n" \
  "ds_read_b64_tr_b16 a[118:119], v105 offset:28672\n" \
  "s_waitcnt lgkmcnt(8)\n" \
  "v_mfma_f32_16x16x32_bf16 v[64:67], a[128:131], a[16:19], v[64:67]\n" \
  "ds_read_b64_tr_b16 a[120:121], v106 offset:24576\n" \
  "v_mfma_f32_16x16x32_bf16 v[68:71], a[128:131], a[20:23], v[68:71]\n" \
  "ds_read_b64_tr_b16 a[122:123], v106 offset:28672\n" \
  "buffer_load_dwordx4 v1, s[56:59], s13 offen offset:1024 lds\n" \
  "v_mfma_f32_16x16x32_bf16 v[72:75], a[132:135], a[16:19], v[72:75]\n" \
  "ds_read_b64_tr_b16 a[124:125], v107 offset:24576\n" \
  "s_add_u32 s13, s13, 65536\n" \
  "v_mfma_f32_16x16x32_bf16 v[76:79], a[132:135], a[20:23], v[76:79]\n" \
  "ds_read_b64_tr_b16 a[126:127], v107 offset:28672\n" \
  "s_waitcnt lgkmcnt(8)\n" \
  "v_mfma_f32_16x16x32_bf16 v[80:83], a[136:139], a[16:19], v[80:83]\n" \
  "ds_read_b64_tr_b16 a[128:129], v108 offset:24576\n" \
  "v_mfma_f32_16x16x32_bf16 v[84:87], a[136:139], a[20:23], v[84:87]\n" \
  "ds_read_b64_tr_b16 a[130:131], v108 offset:28672\n" \
  "v_mfma_f32_16x16x32_bf16 v[88:91], a[140:143], a[16:19], v[88:91]\n" \
  "ds_read_b64_tr_b16 a[132:133], v109 offset:24576\n" \
  "v_mfma_f32_16x16x32_bf16 v[92:95], a[140:143], a[20:23], v[92:95]\n" \
  "ds_read_b64_tr_b16 a[134:135], v109 offset:28672\n" \
  "s_waitcnt vmcnt(20) lgkmcnt(8)\n" \
  "v_mfma_f32_16x16x32_bf16 v[32:35], a[112:115], a[24:27], v[32:35]\n" \
  "ds_read_b64_tr_b16 a[136:137], v110 offset:24576\n" \
  "v_mfma_f32_16x16x32_bf16 v[36:39], a[112:115], a[28:31], v[36:39]\n" \
  "ds_read_b64_tr_b16 a[138:139], v110 offset:28672\n" \
  "buffer_load_dwordx4 a[72:75], v7, s[60:63], s14 offen offset:0 sc1\n" \
  "v_mfma_f32_16x16x32_bf16 v[40:43], a[116:119], a[24:27], v[40:43]\n" \
  "ds_read_b64_tr_b16 a[140:141], v111 offset:24576\n" \
  "v_mfma_f32_16x16x32_bf16 v[44:47], a[116:119], a[28:31], v[44:47]\n" \
  "ds_read_b64_tr_b16 a[142:143], v111 offset:28672\n" \
  "s_waitcnt lgkmcnt(8)\n" \
  "v_mfma_f32_16x16x32_bf16 v[48:51], a[120:123], a[24:27], v[48:51]\n" \
  "ds_read_b64_tr_b16 a[112:113], v104 offset:32768\n" \
  "s_add_u32 m0, s86, 49152\n" \
  "v_mfma_f32_16x16x32_bf16 v[52:55], a[120:123], a[28:31], v[52:55]\n" \
  "ds_read_b64_tr_b16 a[114:115], v104 offset:36864\n" \
  "buffer_load_dwordx4 v0, s[56:59], s13 offen offset:0 lds\n" \
  "v_mfma_f32_16x16x32_bf16 v[56:59], a[124:127], a[24:27], v[56:59]\n" \
  "ds_read_b64_tr_b16 a[116:117], v105 offset:32768\n" \
  "v_mfma_f32_16x16x32_bf16 v[60:63], a[124:127], a[28:31], v[60:63]\n" \
  "ds_read_b64_tr_b16 a[118:119], v105 offset:36864\n" \
  "s_waitcnt lgkmcnt(8)\n" \
  "v_mfma_f32_16x16x32_bf16 v[64:67], a[128:131], a[24:27], v[64:67]\n" \
  "ds_read_b64_tr_b16 a[120:121], v106 offset:32768\n" \
  "v_mfma_f32_16x16x32_bf16 v[68:71], a[128:131], a[28:31], v[68:71]\n" \
  "ds_read_b64_tr_b16 a[122:123], v106 offset:36864\n" \
  "buffer_load_dwordx4 a[76:79], v7, s[60:63], s14 offen offset:512 sc1\n" \
  "v_mfma_f32_16x16x32_bf16 v[72:75], a[132:135], a[24:27], v[72:75]\n" \
  "ds_read_b64_tr_b16 a[124:125], v107 offset:32768\n" \
  "s_mov_b32 s14, s89\n" \
  "v_mfma_f32_16x16x32_bf16 v[76:79], a[132:135], a[28:31], v[76:79]\n" \
  "ds_read_b64_tr_b16 a[126:127], v107 offset:36864\n" \
  "s_waitcnt lgkmcnt(8)\n" \
  "v_mfma_f32_16x16x32_bf16 v[80:83], a[136:139], a[24:27], v[80:83]\n" \
  "ds_read_b64_tr_b16 a[128:129], v108 offset:32768\n" \
  "v_mfma_f32_16x16x32_bf16 v[84:87], a[136:139], a[28:31], v[84:87]\n" \
  "ds_read_b64_tr_b16 a[130:131], v108 offset:36864\n" \
  "buffer_load_dwordx4 v1, s[56:59], s13 offen offset:1024 lds\n" \
  "v_mfma_f32_16x16x32_bf16 v[88:91], a[140:143], a[24:27], v[88:91]\n" \
  "ds_read_b64_tr_b16 a[132:133], v109 offset:32768\n" \
  "s_add_u32 s13, s13, 65536\n" \
  "v_mfma_f32_16x16x32_bf16 v[92:95], a[140:143], a[28:31], v[92:95]\n" \
  "ds_read_b64_tr_b16 a[134:135], v109 offset:36864\n" \
  "s_waitcnt vmcnt(22) lgkmcnt(8)\n" \
  "v_mfma_f32_16x16x32_bf16 v[32:35], a[112:115], a[32:35], v[32:35]\n" \
  "ds_read_b64_tr_b16 a[136:137], v110 offset:32768\n" \
  "v_mfma_f32_16x16x32_bf16 v[36:39], a[112:115], a[36:39], v[36:39]\n" \
  "ds_read_b64_tr_b16 a[138:139], v110 offset:36864\n" \
  "buffer_load_dwordx4 a[80:83], v7, s[60:63], s14 offen offset:0 sc1\n" \
  "v_mfma_f32_16x16x32_bf16 v[40:43], a[116:119], a[32:35], v[40:43]\n" \
  "ds_read_b64_tr_b16 a[140:141], v111 offset:32768\n" \
  "v_mfma_f32_16x16x32_bf16 v[44:47], a[116:119], a[36:39], v[44:47]\n" \
  "ds_read_b64_tr_b16 a[142:143], v111 offset:36864\n" \
  "s_waitcnt lgkmcnt(8)\n" \
  "v_mfma_f32_16x16x32_bf16 v[48:51], a[120:123], a[32:35], v[48:51]\n" \
  "ds_read_b64_tr_b16 a[112:113], v104 offset:40960\n" \
  "s_add_u32 m0, s86, 57344\n" \
  "v_mfma_f32_16x16x32_bf16 v[52:55], a[120:123], a[36:39], v[52:55]\n" \
  "ds_read_b64_tr_b16 a[114:115], v104 offset:45056\n" \
  "buffer_load_dwordx4 v0, s[56:59], s13 offen offset:0 lds\n" \
  "v_mfma_f32_16x16x32_bf16 v[56:59], a[124:127], a[32:35], v[56:59]\n" \
  "ds_read_b64_tr_b16 a[116:117], v105 offset:40960\n" \
  "v_mfma_f32_16x16x32_bf16 v[60:63], a[124:127], a[36:39], v[60:63]\n" \
  "ds_read_b64_tr_b16 a[118:119], v105 offset:45056\n" \
  "s_waitcnt lgkmcnt(8)\n" \
  "v_mfma_f32_16x16x32_bf16 v[64:67], a[128:131], a[32:35], v[64:67]\n" \
  "ds_read_b64_tr_b16 a[120:121], v106 offset:40960\n" \
  "v_mfma_f32_16x16x32_bf16 v[68:71], a[128:131], a[36:39], v[68:71]\n" \
  "ds_read_b64_tr_b16 a[122:123], v106 offset:45056\n" \
  "buffer_load_dwordx4 v1, s[56:59], s13 offen offset:1024 lds\n" \
  "v_mfma_f32_16x16x32_bf16 v[72:75], a[132:135], a[32:35], v[72:75]\n" \
  "ds_read_b64_tr_b16 a[124:125], v107 offset:40960\n" \
  "s_add_u32 s13, s13, 65536\n" \
  "v_mfma_f32_16x16x32_bf16 v[76:79], a[132:135], a[36:39], v[76:79]\n" \
  "ds_read_b64_tr_b16 a[126:127], v107 offset:45056\n" \
  "s_waitcnt lgkmcnt(8)\n" \
  "v_mfma_f32_16x16x32_bf16 v[80:83], a[136:139], a[32:35], v[80:83]\n" \
  "ds_read_b64_tr_b16 a[128:129], v108 offset:40960\n" \
  "v_mfma_f32_16x16x32_bf16 v[84:87], a[136:139], a[36:39], v[84:87]\n" \
  "ds_read_b64_tr_b16 a[130:131], v108 offset:45056\n" \
  "v_mfma_f32_16x16x32_bf16 v[88:91], a[140:143], a[32:35], v[88:91]\n" \
  "ds_read_b64_tr_b16 a[132:133], v109 offset:40960\n" \
  "v_mfma_f32_16x16x32_bf16 v[92:95], a[140:143], a[36:39], v[92:95]\n" \
  "ds_read_b64_tr_b16 a[134:135], v109 offset:45056\n" \
  "s_waitcnt vmcnt(23) lgkmcnt(8)\n" \
  "v_mfma_f32_16x16x32_bf16 v[32:35], a[112:115], a[40:43], v[32:35]\n" \
  "ds_read_b64_tr_b16 a[136:137], v110 offset:40960\n" \
  "v_mfma_f32_16x16x32_bf16 v[36:39], a[112:115], a[44:47], v[36:39]\n" \
  "ds_read_b64_tr_b16 a[138:139], v110 offset:45056\n" \
  "buffer_load_dwordx4 a[84:87], v7, s[60:63], s14 offen offset:512 sc1\n" \
  "v_mfma_f32_16x16x32_bf16 v[40:43], a[116:119], a[40:43], v[40:43]\n" \
  "ds_read_b64_tr_b16 a[140:141], v111 offset:40960\n" \
  "s_add_u32 s14, s14, 2048\n" \
  "v_mfma_f32_16x16x32_bf16 v[44:47], a[116:119], a[44:47], v[44:47]\n" \
  "ds_read_b64_tr_b16 a[142:143], v111 offset:45056\n" \
  "s_waitcnt lgkmcnt(8)\n" \
  "s_barrier\n" \
  "v_mfma_f32_16x16x32_bf16 v[48:51], a[120:123], a[40:43], v[48:51]\n" \
  "ds_read_b64_tr_b16 a[112:113], v104 offset:49152\n" \
  "s_add_u32 m0, s86, 65536\n" \
  "v_mfma_f32_16x16x32_bf16 v[52:55], a[120:123], a[44:47], v[52:55]\n" \
  "ds_read_b64_tr_b16 a[114:115], v104 offset:53248\n" \
  "buffer_load_dwordx4 v0, s[56:59], s13 offen offset:0 lds\n" \
  "v_mfma_f32_16x16x32_bf16 v[56:59], a[124:127], a[40:43], v[56:59]\n" \
  "ds_read_b64_tr_b16 a[116:117], v105 offset:49152\n" \
  "v_mfma_f32_16x16x32_bf16 v[60:63], a[124:127], a[44:47], v[60:63]\n" \
  "ds_read_b64_tr_b16 a[118:119], v105 offset:53248\n" \
  "s_waitcnt lgkmcnt(8)\n" \
  "v_mfma_f32_16x16x32_bf16 v[64:67], a[128:131], a[40:43], v[64:67]\n" \
  "ds_read_b64_tr_b16 a[120:121], v106 offset:49152\n" \
  "v_mfma_f32_16x16x32_bf16 v[68:71], a[128:131], a[44:47], v[68:71]\n" \
  "ds_read_b64_tr_b16 a[122:123], v106 offset:53248\n" \
  "buffer_load_dwordx4 a[88:91], v7, s[60:63], s14 offen offset:0 sc1\n" \
  "v_mfma_f32_16x16x32_bf16 v[72:75], a[132:135], a[40:43], v[72:75]\n" \
  "ds_read_b64_tr_b16 a[124:125], v107 offset:49152\n" \
  "v_mfma_f32_16x16x32_bf16 v[76:79], a[132:135], a[44:47], v[76:79]\n" \
  "ds_read_b64_tr_b16 a[126:127], v107 offset:53248\n" \
  "s_waitcnt lgkmcnt(8)\n" \
  "v_mfma_f32_16x16x32_bf16 v[80:83], a[136:139], a[40:43], v[80:83]\n" \
  "ds_read_b64_tr_b16 a[128:129], v108 offset:49152\n" \
  "v_mfma_f32_16x16x32_bf16 v[84:87], a[136:139], a[44:47], v[84:87]\n" \
  "ds_read_b64_tr_b16 a[130:131], v108 offset:53248\n" \
  "buffer_load_dwordx4 v1, s[56:59], s13 offen offset:1024 lds\n" \
  "v_mfma_f32_16x16x32_bf16 v[88:91], a[140:143], a[40:43], v[88:91]\n" \
  "ds_read_b64_tr_b16 a[132:133], v109 offset:49152\n" \
  "s_add_u32 s13, s13, 65536\n" \
  "v_mfma_f32_16x16x32_bf16 v[92:95], a[140:143], a[44:47], v[92:95]\n" \
  "ds_read_b64_tr_b16 a[134:135], v109 offset:53248\n" \
  "s_waitcnt vmcnt(22) lgkmcnt(8)\n" \
  "v_mfma_f32_16x16x32_bf16 v[32:35], a[112:115], a[48:51], v[32:35]\n" \
  "ds_read_b64_tr_b16 a[136:137], v110 offset:49152\n" \
  "v_mfma_f32_16x16x32_bf16 v[36:39], a[112:115], a[52:55], v[36:39]\n" \
  "ds_read_b64_tr_b16 a[138:139], v110 offset:53248\n" \
  "buffer_load_dwordx4 a[92:95], v7, s[60:63], s14 offen offset:512 sc1\n" \
  "v_mfma_f32_16x16x32_bf16 v[40:43], a[116:119], a[48:51], v[40:43]\n" \
  "ds_read_b64_tr_b16 a[140:141], v111 offset:49152\n" \
  "s_add_u32 s14, s14, 2048\n" \
  "v_mfma_f32_16x16x32_bf16 v[44:47], a[116:119], a[52:55], v[44:47]\n" \
  "ds_read_b64_tr_b16 a[142:143], v111 offset:53248\n" \
  "s_waitcnt lgkmcnt(8)\n" \
  "v_mfma_f32_16x16x32_bf16 v[48:51], a[120:123], a[48:51], v[48:51]\n" \
  "ds_read_b64_tr_b16 a[112:113], v104 offset:57344\n" \
  "s_add_u32 m0, s86, 73728\n" \
  "v_mfma_f32_16x16x32_bf16 v[52:55], a[120:123], a[52:55], v[52:55]\n" \
  "ds_read_b64_tr_b16 a[114:115], v104 offset:61440\n" \
  "buffer_load_dwordx4 v0, s[56:59], s13 offen offset:0 lds\n" \
  "v_mfma_f32_16x16x32_bf16 v[56:59], a[124:127], a[48:51], v[56:59]\n" \
  "ds_read_b64_tr_b16 a[116:117], v105 offset:57344\n" \
  "v_mfma_f32_16x16x32_bf16 v[60:63], a[124:127], a[52:55], v[60:63]\n" \
  "ds_read_b64_tr_b16 a[118:119], v105 offset:61440\n" \
  "s_waitcnt lgkmcnt(8)\n" \
  "v_mfma_f32_16x16x32_bf16 v[64:67], a[128:131], a[48:51], v[64:67]\n" \
  "ds_read_b64_tr_b16 a[120:121], v106 offset:57344\n" \
  "v_mfma_f32_16x16x32_bf16 v[68:71], a[128:131], a[52:55], v[68:71]\n" \
  "ds_read_b64_tr_b16 a[122:123], v106 offset:61440\n" \
  "buffer_load_dwordx4 v1, s[56:59], s13 offen offset:1024 lds\n" \
  "v_mfma_f32_16x16x32_bf16 v[72:75], a[132:135], a[48:51], v[72:75]\n" \
  "ds_read_b64_tr_b16 a[124:125], v107 offset:57344\n" \
  "s_add_u32 s13, s13, 65536\n" \
  "v_mfma_f32_16x16x32_bf16 v[76:79], a[132:135], a[52:55], v[76:79]\n" \
  "ds_read_b64_tr_b16 a[126:127], v107 offset:61440\n" \
  "s_waitcnt lgkmcnt(8)\n" \
  "v_mfma_f32_16x16x32_bf16 v[80:83], a[136:139], a[48:51], v[80:83]\n" \
  "ds_read_b64_tr_b16 a[128:129], v108 offset:57344\n" \
  "v_mfma_f32_16x16x32_bf16 v[84:87], a[136:139], a[52:55], v[84:87]\n" \
  "ds_read_b64_tr_b16 a[130:131], v108 offset:61440\n" \
  "v_mfma_f32_16x16x32_bf16 v[88:91], a[140:143], a[48:51], v[88:91]\n" \
  "ds_read_b64_tr_b16 a[132:133], v109 offset:57344\n" \
  "v_mfma_f32_16x16x32_bf16 v[92:95], a[140:143], a[52:55], v[92:95]\n" \
  "ds_read_b64_tr_b16 a[134:135], v109 offset:61440\n" \
  "s_waitcnt vmcnt(20) lgkmcnt(8)\n" \
  "v_mfma_f32_16x16x32_bf16 v[32:35], a[112:115], a[56:59], v[32:35]\n" \
  "ds_read_b64_tr_b16 a[136:137], v110 offset:57344\n" \
  "v_mfma_f32_16x16x32_bf16 v[36:39], a[112:115], a[60:63], v[36:39]\n" \
  "ds_read_b64_tr_b16 a[138:139], v110 offset:61440\n" \
  "buffer_load_dwordx4 a[96:99], v7, s[60:63], s14 offen offset:0 sc1\n" \
  "v_mfma_f32_16x16x32_bf16 v[40:43], a[116:119], a[56:59], v[40:43]\n" \
  "ds_read_b64_tr_b16 a[140:141], v111 offset:57344\n" \
  "v_mfma_f32_16x16x32_bf16 v[44:47], a[116:119], a[60:63], v[44:47]\n" \
  "ds_read_b64_tr_b16 a[142:143], v111 offset:61440\n" \
  "s_waitcnt lgkmcnt(8)\n" \
  "v_mfma_f32_16x16x32_bf16 v[48:51], a[120:123], a[56:59], v[48:51]\n" \
  "ds_read_b64_tr_b16 a[112:113], v112 offset:0\n" \
  "s_add_u32 m0, s86, 81920\n" \
  "v_mfma_f32_16x16x32_bf16 v[52:55], a[120:123], a[60:63], v[52:55]\n" \
  "ds_read_b64_tr_b16 a[114:115], v112 offset:4096\n" \
  "buffer_load_dwordx4 v0, s[56:59], s13 offen offset:0 lds\n" \
  "v_mfma_f32_16x16x32_bf16 v[56:59], a[124:127], a[56:59], v[56:59]\n" \
  "ds_read_b64_tr_b16 a[116:117], v113 offset:0\n" \
  "v_mfma_f32_16x16x32_bf16 v[60:63], a[124:127], a[60:63], v[60:63]\n" \
  "ds_read_b64_tr_b16 a[118:119], v113 offset:4096\n" \
  "s_waitcnt lgkmcnt(8)\n" \
  "v_mfma_f32_16x16x32_bf16 v[64:67], a[128:131], a[56:59], v[64:67]\n" \
  "ds_read_b64_tr_b16 a[120:121], v114 offset:0\n" \
  "v_mfma_f32_16x16x32_bf16 v[68:71], a[128:131], a[60:63], v[68:71]\n" \
  "ds_read_b64_tr_b16 a[122:123], v114 offset:4096\n" \
  "buffer_load_dwordx4 a[100:103], v7, s[60:63], s14 offen offset:512 sc1\n" \
  "v_mfma_f32_16x16x32_bf16 v[72:75], a[132:135], a[56:59], v[72:75]\n" \
  "ds_read_b64_tr_b16 a[124:125], v115 offset:0\n" \
  "s_add_u32 s14, s14, 2048\n" \
  "v_mfma_f32_16x16x32_bf16 v[76:79], a[132:135], a[60:63], v[76:79]\n" \
  "ds_read_b64_tr_b16 a[126:127], v115 offset:4096\n" \
  "s_waitcnt lgkmcnt(8)\n" \
  "v_mfma_f32_16x16x32_bf16 v[80:83], a[136:139], a[56:59], v[80:83]\n" \
  "ds_read_b64_tr_b16 a[128:129], v116 offset:0\n" \
  "v_mfma_f32_16x16x32_bf16 v[84:87], a[136:139], a[60:63], v[84:87]\n" \
  "ds_read_b64_tr_b16 a[130:131], v116 offset:4096\n" \
  "buffer_load_dwordx4 v1, s[56:59], s13 offen offset:1024 lds\n" \
  "v_mfma_f32_16x16x32_bf16 v[88:91], a[140:143], a[56:59], v[88:91]\n" \
  "ds_read_b64_tr_b16 a[132:133], v117 offset:0\n" \
  "s_add_u32 s13, s13, 65536\n" \
  "v_mfma_f32_16x16x32_bf16 v[92:95], a[140:143], a[60:63], v[92:95]\n" \
  "ds_read_b64_tr_b16 a[134:135], v117 offset:4096\n" \
  "s_waitcnt vmcnt(20) lgkmcnt(8)\n" \
  "v_mfma_f32_16x16x32_bf16 v[32:35], a[112:115], a[64:67], v[32:35]\n" \
  "ds_read_b64_tr_b16 a[136:137], v118 offset:0\n" \
  "v_mfma_f32_16x16x32_bf16 v[36:39], a[112:115], a[68:71], v[36:39]\n" \
  "ds_read_b64_tr_b16 a[138:139], v118 offset:4096\n" \
  "buffer_load_dwordx4 a[104:107], v7, s[60:63], s14 offen offset:0 sc1\n" \
  "v_mfma_f32_16x16x32_bf16 v[40:43], a[116:119], a[64:67], v[40:43]\n" \
  "ds_read_b64_tr_b16 a[140:141], v119 offset:0\n" \
  "v_mfma_f32_16x16x32_bf16 v[44:47], a[116:119], a[68:71], v[44:47]\n" \
  "ds_read_b64_tr_b16 a[142:143], v119 offset:4096\n" \
  "s_waitcnt lgkmcnt(8)\n" \
  "v_mfma_f32_16x16x32_bf16 v[48:51], a[120:123], a[64:67], v[48:51]\n" \
  "ds_read_b64_tr_b16 a[112:113], v112 offset:8192\n" \
  "s_add_u32 m0, s86, 90112\n" \
  "v_mfma_f32_16x16x32_bf16 v[52:55], a[120:123], a[68:71], v[52:55]\n" \
  "ds_read_b64_tr_b16 a[114:115], v112 offset:12288\n" \
  "buffer_load_dwordx4 v0, s[56:59], s13 offen offset:0 lds\n" \
  "v_mfma_f32_16x16x32_bf16 v[56:59], a[124:127], a[64:67], v[56:59]\n" \
  "ds_read_b64_tr_b16 a[116:117], v113 offset:8192\n" \
  "v_mfma_f32_16x16x32_bf16 v[60:63], a[124:127], a[68:71], v[60:63]\n" \
  "ds_read_b64_tr_b16 a[118:119], v113 offset:12288\n" \
  "s_waitcnt lgkmcnt(8)\n" \
  "v_mfma_f32_16x16x32_bf16 v[64:67], a[128:131], a[64:67], v[64:67]\n" \
  "ds_read_b64_tr_b16 a[120:121], v114 offset:8192\n" \
  "v_mfma_f32_16x16x32_bf16 v[68:71], a[128:131], a[68:71], v[68:71]\n" \
  "ds_read_b64_tr_b16 a[122:123], v114 offset:12288\n" \
  "buffer_load_dwordx4 v1, s[56:59], s13 offen offset:1024 lds\n" \
  "v_mfma_f32_16x16x32_bf16 v[72:75], a[132:135], a[64:67], v[72:75]\n" \
  "ds_read_b64_tr_b16 a[124:125], v115 offset:8192\n" \
  "s_add_u32 s13, s13, 65536\n" \
  "v_mfma_f32_16x16x32_bf16 v[76:79], a[132:135], a[68:71], v[76:79]\n" \
  "ds_read_b64_tr_b16 a[126:127], v115 offset:12288\n" \
  "s_waitcnt lgkmcnt(8)\n" \
  "v_mfma_f32_16x16x32_bf16 v[80:83], a[136:139], a[64:67], v[80:83]\n" \
  "ds_read_b64_tr_b16 a[128:129], v116 offset:8192\n" \
  "v_mfma_f32_16x16x32_bf16 v[84:87], a[136:139], a[68:71], v[84:87]\n" \
  "ds_read_b64_tr_b16 a[130:131], v116 offset:12288\n" \
  "v_mfma_f32_16x16x32_bf16 v[88:91], a[140:143], a[64:67], v[88:91]\n" \
  "ds_read_b64_tr_b16 a[132:133], v117 offset:8192\n" \
  "v_mfma_f32_16x16x32_bf16 v[92:95], a[140:143], a[68:71], v[92:95]\n" \
  "ds_read_b64_tr_b16 a[134:135], v117 offset:12288\n" \
  "s_waitcnt vmcnt(18) lgkmcnt(8)\n" \
  "v_mfma_f32_16x16x32_bf16 v[32:35], a[112:115], a[72:75], v[32:35]\n" \
  "ds_read_b64_tr_b16 a[136:137], v118 offset:8192\n" \
  "v_mfma_f32_16x16x32_bf16 v[36:39], a[112:115], a[76:79], v[36:39]\n" \
  "ds_read_b64_tr_b16 a[138:139], v118 offset:12288\n" \
  "buffer_load_dwordx4 a[108:111], v7, s[60:63], s14 offen offset:512 sc1\n" \
  "v_mfma_f32_16x16x32_bf16 v[40:43], a[116:119], a[72:75], v[40:43]\n" \
  "ds_read_b64_tr_b16 a[140:141], v119 offset:8192\n" \
  "s_add_u32 s14, s14, 2048\n" \
  "v_mfma_f32_16x16x32_bf16 v[44:47], a[116:119], a[76:79], v[44:47]\n" \
  "ds_read_b64_tr_b16 a[142:143], v119 offset:12288\n" \
  "s_waitcnt lgkmcnt(8)\n" \
  "s_barrier\n" \
  "v_mfma_f32_16x16x32_bf16 v[48:51], a[120:123], a[72:75], v[48:51]\n" \
  "ds_read_b64_tr_b16 a[112:113], v112 offset:16384\n" \
  "s_add_u32 m0, s86, 98304\n" \
  "v_mfma_f32_16x16x32_bf16 v[52:55], a[120:123], a[76:79], v[52:55]\n" \
  "ds_read_b64_tr_b16 a[114:115], v112 offset:20480\n" \
  "buffer_load_dwordx4 v0, s[56:59], s13 offen offset:0 lds\n" \
  "v_mfma_f32_16x16x32_bf16 v[56:59], a[124:127], a[72:75], v[56:59]\n" \
  "ds_read_b64_tr_b16 a[116:117], v113 offset:16384\n" \
  "v_mfma_f32_16x16x32_bf16 v[60:63], a[124:127], a[76:79], v[60:63]\n" \
  "ds_read_b64_tr_b16 a[118:119], v113 offset:20480\n" \
  "s_waitcnt lgkmcnt(8)\n" \
  "v_mfma_f32_16x16x32_bf16 v[64:67], a[128:131], a[72:75], v[64:67]\n" \
  "ds_read_b64_tr_b16 a[120:121], v114 offset:16384\n" \
  "v_mfma_f32_16x16x32_bf16 v[68:71], a[128:131], a[76:79], v[68:71]\n" \
  "ds_read_b64_tr_b16 a[122:123], v114 offset:20480\n" \
  "buffer_load_dwordx4 a[0:3], v7, s[60:63], s14 offen offset:0 sc1\n" \
  "v_mfma_f32_16x16x32_bf16 v[72:75], a[132:135], a[72:75], v[72:75]\n" \
  "ds_read_b64_tr_b16 a[124:125], v115 offset:16384\n" \
  "v_mfma_f32_16x16x32_bf16 v[76:79], a[132:135], a[76:79], v[76:79]\n" \
  "ds_read_b64_tr_b16 a[126:127], v115 offset:20480\n" \
  "s_waitcnt lgkmcnt(8)\n" \
  "v_mfma_f32_16x16x32_bf16 v[80:83], a[136:139], a[72:75], v[80:83]\n" \
  "ds_read_b64_tr_b16 a[128:129], v116 offset:16384\n" \
  "v_mfma_f32_16x16x32_bf16 v[84:87], a[136:139], a[76:79], v[84:87]\n" \
  "ds_read_b64_tr_b16 a[130:131], v116 offset:20480\n" \
  "buffer_load_dwordx4 v1, s[56:59], s13 offen offset:1024 lds\n" \
  "v_mfma_f32_16x16x32_bf16 v[88:91], a[140:143], a[72:75], v[88:91]\n" \
  "ds_read_b64_tr_b16 a[132:133], v117 offset:16384\n" \
  "s_add_u32 s13, s13, 65536\n" \
  "v_mfma_f32_16x16x32_bf16 v[92:95], a[140:143], a[76:79], v[92:95]\n" \
  "ds_read_b64_tr_b16 a[134:135], v117 offset:20480\n" \
  "s_waitcnt vmcnt(17) lgkmcnt(8)\n" \
  "v_mfma_f32_16x16x32_bf16 v[32:35], a[112:115], a[80:83], v[32:35]\n" \
  "ds_read_b64_tr_b16 a[136:137], v118 offset:16384\n" \
  "v_mfma_f32_16x16x32_bf16 v[36:39], a[112:115], a[84:87], v[36:39]\n" \
  "ds_read_b64_tr_b16 a[138:139], v118 offset:20480\n" \
  "buffer_load_dwordx4 a[4:7], v7, s[60:63], s14 offen offset:512 sc1\n" \
  "v_mfma_f32_16x16x32_bf16 v[40:43], a[116:119], a[80:83], v[40:43]\n" \
  "ds_read_b64_tr_b16 a[140:141], v119 offset:16384\n" \
  "s_add_u32 s14, s14, 2048\n" \
  "v_mfma_f32_16x16x32_bf16 v[44:47], a[116:119], a[84:87], v[44:47]\n" \
  "ds_read_b64_tr_b16 a[142:143], v119 offset:20480\n" \
  "s_waitcnt lgkmcnt(8)\n" \
  "v_mfma_f32_16x16x32_bf16 v[48:51], a[120:123], a[80:83], v[48:51]\n" \
  "ds_read_b64_tr_b16 a[112:113], v112 offset:24576\n" \
  "s_add_u32 m0, s86, 106496\n" \
  "v_mfma_f32_16x16x32_bf16 v[52:55], a[120:123], a[84:87], v[52:55]\n" \
  "ds_read_b64_tr_b16 a[114:115], v112 offset:28672\n" \
  "buffer_load_dwordx4 v0, s[56:59], s13 offen offset:0 lds\n" \
  "v_mfma_f32_16x16x32_bf16 v[56:59], a[124:127], a[80:83], v[56:59]\n" \
  "ds_read_b64_tr_b16 a[116:117], v113 offset:24576\n" \
  "v_mfma_f32_16x16x32_bf16 v[60:63], a[124:127], a[84:87], v[60:63]\n" \
  "ds_read_b64_tr_b16 a[118:119], v113 offset:28672\n" \
  "s_waitcnt lgkmcnt(8)\n" \
  "v_mfma_f32_16x16x32_bf16 v[64:67], a[128:131], a[80:83], v[64:67]\n" \
  "ds_read_b64_tr_b16 a[120:121], v114 offset:24576\n" \
  "v_mfma_f32_16x16x32_bf16 v[68:71], a[128:131], a[84:87], v[68:71]\n" \
  "ds_read_b64_tr_b16 a[122:123], v114 offset:28672\n" \
  "buffer_load_dwordx4 v1, s[56:59], s13 offen offset:1024 lds\n" \
  "v_mfma_f32_16x16x32_bf16 v[72:75], a[132:135], a[80:83], v[72:75]\n" \
  "ds_read_b64_tr_b16 a[124:125], v115 offset:24576\n" \
  "s_add_u32 s13, s13, 65536\n" \
  "v_mfma_f32_16x16x32_bf16 v[76:79], a[132:135], a[84:87], v[76:79]\n" \
  "ds_read_b64_tr_b16 a[126:127], v115 offset:28672\n" \
  "s_waitcnt lgkmcnt(8)\n" \
  "v_mfma_f32_16x16x32_bf16 v[80:83], a[136:139], a[80:83], v[80:83]\n" \
  "ds_read_b64_tr_b16 a[128:129], v116 offset:24576\n" \
  "v_mfma_f32_16x16x32_bf16 v[84:87], a[136:139], a[84:87], v[84:87]\n" \
  "ds_read_b64_tr_b16 a[130:131], v116 offset:28672\n" \
  "v_mfma_f32_16x16x32_bf16 v[88:91], a[140:143], a[80:83], v[88:91]\n" \
  "ds_read_b64_tr_b16 a[132:133], v117 offset:24576\n" \
  "v_mfma_f32_16x16x32_bf16 v[92:95], a[140:143], a[84:87], v[92:95]\n" \
  "ds_read_b64_tr_b16 a[134:135], v117 offset:28672\n" \
  "s_waitcnt vmcnt(16) lgkmcnt(8)\n" \
  "v_mfma_f32_16x16x32_bf16 v[32:35], a[112:115], a[88:91], v[32:35]\n" \
  "ds_read_b64_tr_b16 a[136:137], v118 offset:24576\n" \
  "v_mfma_f32_16x16x32_bf16 v[36:39], a[112:115], a[92:95], v[36:39]\n" \
  "ds_read_b64_tr_b16 a[138:139], v118 offset:28672\n" \
  "buffer_load_dwordx4 a[8:11], v7, s[60:63], s14 offen offset:0 sc1\n" \
  "v_mfma_f32_16x16x32_bf16 v[40:43], a[116:119], a[88:91], v[40:43]\n" \
  "ds_read_b64_tr_b16 a[140:141], v119 offset:24576\n" \
  "v_mfma_f32_16x16x32_bf16 v[44:47], a[116:119], a[92:95], v[44:47]\n" \
  "ds_read_b64_tr_b16 a[142:143], v119 offset:28672\n" \
  "s_waitcnt lgkmcnt(8)\n" \
  "v_mfma_f32_16x16x32_bf16 v[48:51], a[120:123], a[88:91], v[48:51]\n" \
  "ds_read_b64_tr_b16 a[112:113], v96 offset:0\n" \
  "s_add_u32 m0, s86, 114688\n" \
  "v_mfma_f32_16x16x32_bf16 v[52:55], a[120:123], a[92:95], v[52:55]\n" \
  "ds_read_b64_tr_b16 a[114:115], v96 offset:4096\n" \
  "buffer_load_dwordx4 v0, s[56:59], s13 offen offset:0 lds\n" \
  "v_mfma_f32_16x16x32_bf16 v[56:59], a[124:127], a[88:91], v[56:59]\n" \
  "ds_read_b64_tr_b16 a[116:117], v97 offset:0\n" \
  "v_mfma_f32_16x16x32_bf16 v[60:63], a[124:127], a[92:95], v[60:63]\n" \
  "ds_read_b64_tr_b16 a[118:119], v97 offset:4096\n" \
  "s_waitcnt lgkmcnt(8)\n" \
  "v_mfma_f32_16x16x32_bf16 v[64:67], a[128:131], a[88:91], v[64:67]\n" \
  "ds_read_b64_tr_b16 a[120:121], v98 offset:0\n" \
  "v_mfma_f32_16x16x32_bf16 v[68:71], a[128:131], a[92:95], v[68:71]\n" \
  "ds_read_b64_tr_b16 a[122:123], v98 offset:4096\n" \
  "buffer_load_dwordx4 a[12:15], v7, s[60:63], s14 offen offset:512 sc1\n" \
  "v_mfma_f32_16x16x32_bf16 v[72:75], a[132:135], a[88:91], v[72:75]\n" \
  "ds_read_b64_tr_b16 a[124:125], v99 offset:0\n" \
  "s_add_u32 s14, s14, 2048\n" \
  "v_mfma_f32_16x16x32_bf16 v[76:79], a[132:135], a[92:95], v[76:79]\n" \
  "ds_read_b64_tr_b16 a[126:127], v99 offset:4096\n" \
  "s_waitcnt lgkmcnt(8)\n" \
  "v_mfma_f32_16x16x32_bf16 v[80:83], a[136:139], a[88:91], v[80:83]\n" \
  "ds_read_b64_tr_b16 a[128:129], v100 offset:0\n" \
  "v_mfma_f32_16x16x32_bf16 v[84:87], a[136:139], a[92:95], v[84:87]\n" \
  "ds_read_b64_tr_b16 a[130:131], v100 offset:4096\n" \
  "buffer_load_dwordx4 v1, s[56:59], s13 offen offset:1024 lds\n" \
  "v_mfma_f32_16x16x32_bf16 v[88:91], a[140:143], a[88:91], v[88:91]\n" \
  "ds_read_b64_tr_b16 a[132:133], v101 offset:0\n" \
  "s_add_u32 s13, s13, 65536\n" \
  "v_mfma_f32_16x16x32_bf16 v[92:95], a[140:143], a[92:95], v[92:95]\n" \
  "ds_read_b64_tr_b16 a[134:135], v101 offset:4096\n" \
  "s_waitcnt vmcnt(15) lgkmcnt(8)\n" \
  "v_mfma_f32_16x16x32_bf16 v[32:35], a[112:115], a[96:99], v[32:35]\n" \
  "ds_read_b64_tr_b16 a[136:137], v102 offset:0\n" \
  "v_mfma_f32_16x16x32_bf16 v[36:39], a[112:115], a[100:103], v[36:39]\n" \
  "ds_read_b64_tr_b16 a[138:139], v102 offset:4096\n" \
  "buffer_load_dwordx4 a[16:19], v7, s[60:63], s14 offen offset:0 sc1\n" \
  "v_mfma_f32_16x16x32_bf16 v[40:43], a[116:119], a[96:99], v[40:43]\n" \
  "ds_read_b64_tr_b16 a[140:141], v103 offset:0\n" \
  "v_mfma_f32_16x16x32_bf16 v[44:47], a[116:119], a[100:103], v[44:47]\n" \
  "ds_read_b64_tr_b16 a[142:143], v103 offset:4096\n" \
  "s_waitcnt lgkmcnt(8)\n" \
  "v_mfma_f32_16x16x32_bf16 v[48:51], a[120:123], a[96:99], v[48:51]\n" \
  "ds_read_b64_tr_b16 a[112:113], v96 offset:8192\n" \
  "s_add_u32 m0, s86, 122880\n" \
  "v_mfma_f32_16x16x32_bf16 v[52:55], a[120:123], a[100:103], v[52:55]\n" \
  "ds_read_b64_tr_b16 a[114:115], v96 offset:12288\n" \
  "buffer_load_dwordx4 v0, s[56:59], s13 offen offset:0 lds\n" \
  "v_mfma_f32_16x16x32_bf16 v[56:59], a[124:127], a[96:99], v[56:59]\n" \
  "ds_read_b64_tr_b16 a[116:117], v97 offset:8192\n" \
  "v_mfma_f32_16x16x32_bf16 v[60:63], a[124:127], a[100:103], v[60:63]\n" \
  "ds_read_b64_tr_b16 a[118:119], v97 offset:12288\n" \
  "s_waitcnt lgkmcnt(8)\n" \
  "v_mfma_f32_16x16x32_bf16 v[64:67], a[128:131], a[96:99], v[64:67]\n" \
  "ds_read_b64_tr_b16 a[120:121], v98 offset:8192\n" \
  "v_mfma_f32_16x16x32_bf16 v[68:71], a[128:131], a[100:103], v[68:71]\n" \
  "ds_read_b64_tr_b16 a[122:123], v98 offset:12288\n" \
  "buffer_load_dwordx4 a[20:23], v7, s[60:63], s14 offen offset:512 sc1\n" \
  "v_mfma_f32_16x16x32_bf16 v[72:75], a[132:135], a[96:99], v[72:75]\n" \
  "ds_read_b64_tr_b16 a[124:125], v99 offset:8192\n" \
  "s_add_u32 s14, s14, 2048\n" \
  "v_mfma_f32_16x16x32_bf16 v[76:79], a[132:135], a[100:103], v[76:79]\n" \
  "ds_read_b64_tr_b16 a[126:127], v99 offset:12288\n" \
  "s_waitcnt lgkmcnt(8)\n" \
  "v_mfma_f32_16x16x32_bf16 v[80:83], a[136:139], a[96:99], v[80:83]\n" \
  "ds_read_b64_tr_b16 a[128:129], v100 offset:8192\n" \
  "v_mfma_f32_16x16x32_bf16 v[84:87], a[136:139], a[100:103], v[84:87]\n" \
  "ds_read_b64_tr_b16 a[130:131], v100 offset:12288\n" \
  "buffer_load_dwordx4 v1, s[56:59], s13 offen offset:1024 lds\n" \
  "v_mfma_f32_16x16x32_bf16 v[88:91], a[140:143], a[96:99], v[88:91]\n" \
  "ds_read_b64_tr_b16 a[132:133], v101 offset:8192\n" \
  "s_add_u32 s13, s13, 65536\n" \
  "v_mfma_f32_16x16x32_bf16 v[92:95], a[140:143], a[100:103], v[92:95]\n" \
  "ds_read_b64_tr_b16 a[134:135], v101 offset:12288\n" \
  "s_waitcnt vmcnt(14) lgkmcnt(8)\n" \
  "v_mfma_f32_16x16x32_bf16 v[32:35], a[112:115], a[104:107], v[32:35]\n" \
  "ds_read_b64_tr_b16 a[136:137], v102 offset:8192\n" \
  "v_mfma_f32_16x16x32_bf16 v[36:39], a[112:115], a[108:111], v[36:39]\n" \
  "ds_read_b64_tr_b16 a[138:139], v102 offset:12288\n" \
  "buffer_load_dwordx4 a[24:27], v7, s[60:63], s14 offen offset:0 sc1\n" \
  "v_mfma_f32_16x16x32_bf16 v[40:43], a[116:119], a[104:107], v[40:43]\n" \
  "ds_read_b64_tr_b16 a[140:141], v103 offset:8192\n" \
  "v_mfma_f32_16x16x32_bf16 v[44:47], a[116:119], a[108:111], v[44:47]\n" \
  "ds_read_b64_tr_b16 a[142:143], v103 offset:12288\n" \
  "s_waitcnt lgkmcnt(8)\n" \
  "s_barrier\n" \
  "v_mfma_f32_16x16x32_bf16 v[48:51], a[120:123], a[104:107], v[48:51]\n" \
  "ds_read_b64_tr_b16 a[112:113], v96 offset:16384\n" \
  "s_add_u32 m0, s86, 131072\n" \
  "v_mfma_f32_16x16x32_bf16 v[52:55], a[120:123], a[108:111], v[52:55]\n" \
  "ds_read_b64_tr_b16 a[114:115], v96 offset:20480\n" \
  "buffer_load_dwordx4 v0, s[56:59], s13 offen offset:0 lds\n" \
  "v_mfma_f32_16x16x32_bf16 v[56:59], a[124:127], a[104:107], v[56:59]\n" \
  "ds_read_b64_tr_b16 a[116:117], v97 offset:16384\n" \
  "v_mfma_f32_16x16x32_bf16 v[60:63], a[124:127], a[108:111], v[60:63]\n" \
  "ds_read_b64_tr_b16 a[118:119], v97 offset:20480\n" \
  "s_waitcnt lgkmcnt(8)\n" \
  "v_mfma_f32_16x16x32_bf16 v[64:67], a[128:131], a[104:107], v[64:67]\n" \
  "ds_read_b64_tr_b16 a[120:121], v98 offset:16384\n" \
  "v_mfma_f32_16x16x32_bf16 v[68:71], a[128:131], a[108:111], v[68:71]\n" \
  "ds_read_b64_tr_b16 a[122:123], v98 offset:20480\n" \
  "buffer_load_dwordx4 a[28:31], v7, s[60:63], s14 offen offset:512 sc1\n" \
  "v_mfma_f32_16x16x32_bf16 v[72:75], a[132:135], a[104:107], v[72:75]\n" \
  "ds_read_b64_tr_b16 a[124:125], v99 offset:16384\n" \
  "s_add_u32 s14, s14, 2048\n" \
  "v_mfma_f32_16x16x32_bf16 v[76:79], a[132:135], a[108:111], v[76:79]\n" \
  "ds_read_b64_tr_b16 a[126:127], v99 offset:20480\n" \
  "s_waitcnt lgkmcnt(8)\n" \
  "v_mfma_f32_16x16x32_bf16 v[80:83], a[136:139], a[104:107], v[80:83]\n" \
  "ds_read_b64_tr_b16 a[128:129], v100 offset:16384\n" \
  "v_mfma_f32_16x16x32_bf16 v[84:87], a[136:139], a[108:111], v[84:87]\n" \
  "ds_read_b64_tr_b16 a[130:131], v100 offset:20480\n" \
  "buffer_load_dwordx4 v1, s[56:59], s13 offen offset:1024 lds\n" \
  "v_mfma_f32_16x16x32_bf16 v[88:91], a[140:143], a[104:107], v[88:91]\n" \
  "ds_read_b64_tr_b16 a[132:133], v101 offset:16384\n" \
  "s_add_u32 s13, s13, 65536\n" \
  "v_mfma_f32_16x16x32_bf16 v[92:95], a[140:143], a[108:111], v[92:95]\n" \
  "ds_read_b64_tr_b16 a[134:135], v101 offset:20480\n" \
  "s_waitcnt vmcnt(14) lgkmcnt(8)\n" \
  "v_mfma_f32_16x16x32_bf16 v[32:35], a[112:115], a[0:3], v[32:35]\n" \
  "ds_read_b64_tr_b16 a[136:137], v102 offset:16384\n" \
  "v_mfma_f32_16x16x32_bf16 v[36:39], a[112:115], a[4:7], v[36:39]\n" \
  "ds_read_b64_tr_b16 a[138:139], v102 offset:20480\n" \
  "buffer_load_dwordx4 a[32:35], v7, s[60:63], s14 offen offset:0 sc1\n" \
  "v_mfma_f32_16x16x32_bf16 v[40:43], a[116:119], a[0:3], v[40:43]\n" \
  "ds_read_b64_tr_b16 a[140:141], v103 offset:16384\n" \
  "v_mfma_f32_16x16x32_bf16 v[44:47], a[116:119], a[4:7], v[44:47]\n" \
  "ds_read_b64_tr_b16 a[142:143], v103 offset:20480\n" \
  "s_waitcnt lgkmcnt(8)\n" \
  "v_mfma_f32_16x16x32_bf16 v[48:51], a[120:123], a[0:3], v[48:51]\n" \
  "ds_read_b64_tr_b16 a[112:113], v96 offset:24576\n" \
  "s_add_u32 m0, s86, 139264\n" \
  "v_mfma_f32_16x16x32_bf16 v[52:55], a[120:123], a[4:7], v[52:55]\n" \
  "ds_read_b64_tr_b16 a[114:115], v96 offset:28672\n" \
  "buffer_load_dwordx4 v0, s[56:59], s13 offen offset:0 lds\n" \
  "v_mfma_f32_16x16x32_bf16 v[56:59], a[124:127], a[0:3], v[56:59]\n" \
  "ds_read_b64_tr_b16 a[116:117], v97 offset:24576\n" \
  "v_mfma_f32_16x16x32_bf16 v[60:63], a[124:127], a[4:7], v[60:63]\n" \
  "ds_read_b64_tr_b16 a[118:119], v97 offset:28672\n" \
  "s_waitcnt lgkmcnt(8)\n" \
  "v_mfma_f32_16x16x32_bf16 v[64:67], a[128:131], a[0:3], v[64:67]\n" \
  "ds_read_b64_tr_b16 a[120:121], v98 offset:24576\n" \
  "v_mfma_f32_16x16x32_bf16 v[68:71], a[128:131], a[4:7], v[68:71]\n" \
  "ds_read_b64_tr_b16 a[122:123], v98 offset:28672\n" \
  "buffer_load_dwordx4 a[36:39], v7, s[60:63], s14 offen offset:512 sc1\n" \
  "v_mfma_f32_16x16x32_bf16 v[72:75], a[132:135], a[0:3], v[72:75]\n" \
  "ds_read_b64_tr_b16 a[124:125], v99 offset:24576\n" \
  "s_add_u32 s14, s14, 2048\n" \
  "v_mfma_f32_16x16x32_bf16 v[76:79], a[132:135], a[4:7], v[76:79]\n" \
  "ds_read_b64_tr_b16 a[126:127], v99 offset:28672\n" \
  "s_waitcnt lgkmcnt(8)\n" \
  "v_mfma_f32_16x16x32_bf16 v[80:83], a[136:139], a[0:3], v[80:83]\n" \
  "ds_read_b64_tr_b16 a[128:129], v100 offset:24576\n" \
  "v_mfma_f32_16x16x32_bf16 v[84:87], a[136:139], a[4:7], v[84:87]\n" \
  "ds_read_b64_tr_b16 a[130:131], v100 offset:28672\n" \
  "buffer_load_dwordx4 v1, s[56:59], s13 offen offset:1024 lds\n" \
  "v_mfma_f32_16x16x32_bf16 v[88:91], a[140:143], a[0:3], v[88:91]\n" \
  "ds_read_b64_tr_b16 a[132:133], v101 offset:24576\n" \
  "s_add_u32 s13, s13, 65536\n" \
  "v_mfma_f32_16x16x32_bf16 v[92:95], a[140:143], a[4:7], v[92:95]\n" \
  "ds_read_b64_tr_b16 a[134:135], v101 offset:28672\n" \
  "s_waitcnt vmcnt(13) lgkmcnt(8)\n" \
  "v_mfma_f32_16x16x32_bf16 v[32:35], a[112:115], a[8:11], v[32:35]\n" \
  "ds_read_b64_tr_b16 a[136:137], v102 offset:24576\n" \
  "v_mfma_f32_16x16x32_bf16 v[36:39], a[112:115], a[12:15], v[36:39]\n" \
  "ds_read_b64_tr_b16 a[138:139], v102 offset:28672\n" \
  "buffer_load_dwordx4 a[40:43], v7, s[60:63], s14 offen offset:0 sc1\n" \
  "v_mfma_f32_16x16x32_bf16 v[40:43], a[116:119], a[8:11], v[40:43]\n" \
  "ds_read_b64_tr_b16 a[140:141], v103 offset:24576\n" \
  "v_mfma_f32_16x16x32_bf16 v[44:47], a[116:119], a[12:15], v[44:47]\n" \
  "ds_read_b64_tr_b16 a[142:143], v103 offset:28672\n" \
  "s_waitcnt lgkmcnt(8)\n" \
  "v_mfma_f32_16x16x32_bf16 v[48:51], a[120:123], a[8:11], v[48:51]\n" \
  "ds_read_b64_tr_b16 a[112:113], v96 offset:32768\n" \
  "s_add_u32 m0, s86, 147456\n" \
  "v_mfma_f32_16x16x32_bf16 v[52:55], a[120:123], a[12:15], v[52:55]\n" \
  "ds_read_b64_tr_b16 a[114:115], v96 offset:36864\n" \
  "buffer_load_dwordx4 v0, s[56:59], s13 offen offset:0 lds\n" \
  "v_mfma_f32_16x16x32_bf16 v[56:59], a[124:127], a[8:11], v[56:59]\n" \
  "ds_read_b64_tr_b16 a[116:117], v97 offset:32768\n" \
  "v_mfma_f32_16x16x32_bf16 v[60:63], a[124:127], a[12:15], v[60:63]\n" \
  "ds_read_b64_tr_b16 a[118:119], v97 offset:36864\n" \
  "s_waitcnt lgkmcnt(8)\n" \
  "v_mfma_f32_16x16x32_bf16 v[64:67], a[128:131], a[8:11], v[64:67]\n" \
  "ds_read_b64_tr_b16 a[120:121], v98 offset:32768\n" \
  "v_mfma_f32_16x16x32_bf16 v[68:71], a[128:131], a[12:15], v[68:71]\n" \
  "ds_read_b64_tr_b16 a[122:123], v98 offset:36864\n" \
  "buffer_load_dwordx4 a[44:47], v7, s[60:63], s14 offen offset:512 sc1\n" \
  "v_mfma_f32_16x16x32_bf16 v[72:75], a[132:135], a[8:11], v[72:75]\n" \
  "ds_read_b64_tr_b16 a[124:125], v99 offset:32768\n" \
  "s_add_u32 s14, s14, 2048\n" \
  "v_mfma_f32_16x16x32_bf16 v[76:79], a[132:135], a[12:15], v[76:79]\n" \
  "ds_read_b64_tr_b16 a[126:127], v99 offset:36864\n" \
  "s_waitcnt lgkmcnt(8)\n" \
  "v_mfma_f32_16x16x32_bf16 v[80:83], a[136:139], a[8:11], v[80:83]\n" \
  "ds_read_b64_tr_b16 a[128:129], v100 offset:32768\n" \
  "v_mfma_f32_16x16x32_bf16 v[84:87], a[136:139], a[12:15], v[84:87]\n" \
  "ds_read_b64_tr_b16 a[130:131], v100 offset:36864\n" \
  "buffer_load_dwordx4 v1, s[56:59], s13 offen offset:1024 lds\n" \
  "v_mfma_f32_16x16x32_bf16 v[88:91], a[140:143], a[8:11], v[88:91]\n" \
  "ds_read_b64_tr_b16 a[132:133], v101 offset:32768\n" \
  "s_add_u32 s13, s13, 65536\n" \
  "v_mfma_f32_16x16x32_bf16 v[92:95], a[140:143], a[12:15], v[92:95]\n" \
  "ds_read_b64_tr_b16 a[134:135], v101 offset:36864\n" \
  "s_waitcnt vmcnt(13) lgkmcnt(8)\n" \
  "v_mfma_f32_16x16x32_bf16 v[32:35], a[112:115], a[16:19], v[32:35]\n" \
  "ds_read_b64_tr_b16 a[136:137], v102 offset:32768\n" \
  "v_mfma_f32_16x16x32_bf16 v[36:39], a[112:115], a[20:23], v[36:39]\n" \
  "ds_read_b64_tr_b16 a[138:139], v102 offset:36864\n" \
  "buffer_load_dwordx4 a[48:51], v7, s[60:63], s14 offen offset:0 sc1\n" \
  "v_mfma_f32_16x16x32_bf16 v[40:43], a[116:119], a[16:19], v[40:43]\n" \
  "ds_read_b64_tr_b16 a[140:141], v103 offset:32768\n" \
  "v_mfma_f32_16x16x32_bf16 v[44:47], a[116:119], a[20:23], v[44:47]\n" \
  "ds_read_b64_tr_b16 a[142:143], v103 offset:36864\n" \
  "s_waitcnt lgkmcnt(8)\n" \
  "v_mfma_f32_16x16x32_bf16 v[48:51], a[120:123], a[16:19], v[48:51]\n" \
  "ds_read_b64_tr_b16 a[112:113], v96 offset:40960\n" \
  "s_add_u32 m0, s86, 155648\n" \
  "v_mfma_f32_16x16x32_bf16 v[52:55], a[120:123], a[20:23], v[52:55]\n" \
  "ds_read_b64_tr_b16 a[114:115], v96 offset:45056\n" \
  "buffer_load_dwordx4 v0, s[56:59], s13 offen offset:0 lds\n" \
  "v_mfma_f32_16x16x32_bf16 v[56:59], a[124:127], a[16:19], v[56:59]\n" \
  "ds_read_b64_tr_b16 a[116:117], v97 offset:40960\n" \
  "v_mfma_f32_16x16x32_bf16 v[60:63], a[124:127], a[20:23], v[60:63]\n" \
  "ds_read_b64_tr_b16 a[118:119], v97 offset:45056\n" \
  "s_waitcnt lgkmcnt(8)\n" \
  "v_mfma_f32_16x16x32_bf16 v[64:67], a[128:131], a[16:19], v[64:67]\n" \
  "ds_read_b64_tr_b16 a[120:121], v98 offset:40960\n" \
  "v_mfma_f32_16x16x32_bf16 v[68:71], a[128:131], a[20:23], v[68:71]\n" \
  "ds_read_b64_tr_b16 a[122:123], v98 offset:45056\n" \
  "buffer_load_dwordx4 a[52:55], v7, s[60:63], s14 offen offset:512 sc1\n" \
  "v_mfma_f32_16x16x32_bf16 v[72:75], a[132:135], a[16:19], v[72:75]\n" \
  "ds_read_b64_tr_b16 a[124:125], v99 offset:40960\n" \
  "s_add_u32 s14, s14, 2048\n" \
  "v_mfma_f32_16x16x32_bf16 v[76:79], a[132:135], a[20:23], v[76:79]\n" \
  "ds_read_b64_tr_b16 a[126:127], v99 offset:45056\n" \
  "s_waitcnt lgkmcnt(8)\n" \
  "v_mfma_f32_16x16x32_bf16 v[80:83], a[136:139], a[16:19], v[80:83]\n" \
  "ds_read_b64_tr_b16 a[128:129], v100 offset:40960\n" \
  "v_mfma_f32_16x16x32_bf16 v[84:87], a[136:139], a[20:23], v[84:87]\n" \
  "ds_read_b64_tr_b16 a[130:131], v100 offset:45056\n" \
  "buffer_load_dwordx4 v1, s[56:59], s13 offen offset:1024 lds\n" \
  "v_mfma_f32_16x16x32_bf16 v[88:91], a[140:143], a[16:19], v[88:91]\n" \
  "ds_read_b64_tr_b16 a[132:133], v101 offset:40960\n" \
  "s_add_u32 s13, s13, 65536\n" \
  "v_mfma_f32_16x16x32_bf16 v[92:95], a[140:143], a[20:23], v[92:95]\n" \
  "ds_read_b64_tr_b16 a[134:135], v101 offset:45056\n" \
  "s_waitcnt vmcnt(13) lgkmcnt(8)\n" \
  "v_mfma_f32_16x16x32_bf16 v[32:35], a[112:115], a[24:27], v[32:35]\n" \
  "ds_read_b64_tr_b16 a[136:137], v102 offset:40960\n" \
  "v_mfma_f32_16x16x32_bf16 v[36:39], a[112:115], a[28:31], v[36:39]\n" \
  "ds_read_b64_tr_b16 a[138:139], v102 offset:45056\n" \
  "buffer_load_dwordx4 a[56:59], v7, s[60:63], s14 offen offset:0 sc1\n" \
  "v_mfma_f32_16x16x32_bf16 v[40:43], a[116:119], a[24:27], v[40:43]\n" \
  "ds_read_b64_tr_b16 a[140:141], v103 offset:40960\n" \
  "v_mfma_f32_16x16x32_bf16 v[44:47], a[116:119], a[28:31], v[44:47]\n" \
  "ds_read_b64_tr_b16 a[142:143], v103 offset:45056\n" \
  "s_waitcnt lgkmcnt(8)\n" \
  "s_barrier\n" \
  "v_mfma_f32_16x16x32_bf16 v[48:51], a[120:123], a[24:27], v[48:51]\n" \
  "ds_read_b64_tr_b16 a[112:113], v96 offset:49152\n" \
  "s_add_u32 m0, s86, 0\n" \
  "v_mfma_f32_16x16x32_bf16 v[52:55], a[120:123], a[28:31], v[52:55]\n" \
  "ds_read_b64_tr_b16 a[114:115], v96 offset:53248\n" \
  "buffer_load_dwordx4 v0, s[56:59], s13 offen offset:0 lds\n" \
  "v_mfma_f32_16x16x32_bf16 v[56:59], a[124:127], a[24:27], v[56:59]\n" \
  "ds_read_b64_tr_b16 a[116:117], v97 offset:49152\n" \
  "v_mfma_f32_16x16x32_bf16 v[60:63], a[124:127], a[28:31], v[60:63]\n" \
  "ds_read_b64_tr_b16 a[118:119], v97 offset:53248\n" \
  "s_waitcnt lgkmcnt(8)\n" \
  "v_mfma_f32_16x16x32_bf16 v[64:67], a[128:131], a[24:27], v[64:67]\n" \
  "ds_read_b64_tr_b16 a[120:121], v98 offset:49152\n" \
  "v_mfma_f32_16x16x32_bf16 v[68:71], a[128:131], a[28:31], v[68:71]\n" \
  "ds_read_b64_tr_b16 a[122:123], v98 offset:53248\n" \
  "buffer_load_dwordx4 a[60:63], v7, s[60:63], s14 offen offset:512 sc1\n" \
  "v_mfma_f32_16x16x32_bf16 v[72:75], a[132:135], a[24:27], v[72:75]\n" \
  "ds_read_b64_tr_b16 a[124:125], v99 offset:49152\n" \
  "s_add_u32 s14, s14, 2048\n" \
  "v_mfma_f32_16x16x32_bf16 v[76:79], a[132:135], a[28:31], v[76:79]\n" \
  "ds_read_b64_tr_b16 a[126:127], v99 offset:53248\n" \
  "s_waitcnt lgkmcnt(8)\n" \
  "v_mfma_f32_16x16x32_bf16 v[80:83], a[136:139], a[24:27], v[80:83]\n" \
  "ds_read_b64_tr_b16 a[128:129], v100 offset:49152\n" \
  "v_mfma_f32_16x16x32_bf16 v[84:87], a[136:139], a[28:31], v[84:87]\n" \
  "ds_read_b64_tr_b16 a[130:131], v100 offset:53248\n" \
  "buffer_load_dwordx4 v1, s[56:59], s13 offen offset:1024 lds\n" \
  "v_mfma_f32_16x16x32_bf16 v[88:91], a[140:143], a[24:27], v[88:91]\n" \
  "ds_read_b64_tr_b16 a[132:133], v101 offset:49152\n" \
  "s_add_u32 s13, s13, 65536\n" \
  "v_mfma_f32_16x16x32_bf16 v[92:95], a[140:143], a[28:31], v[92:95]\n" \
  "ds_read_b64_tr_b16 a[134:135], v101 offset:53248\n" \
  "s_waitcnt vmcnt(13) lgkmcnt(8)\n" \
  "v_mfma_f32_16x16x32_bf16 v[32:35], a[112:115], a[32:35], v[32:35]\n" \
  "ds_read_b64_tr_b16 a[136:137], v102 offset:49152\n" \
  "v_mfma_f32_16x16x32_bf16 v[36:39], a[112:115], a[36:39], v[36:39]\n" \
  "ds_read_b64_tr_b16 a[138:139], v102 offset:53248\n" \
  "buffer_load_dwordx4 a[64:67], v7, s[60:63], s14 offen offset:0 sc1\n" \
  "v_mfma_f32_16x16x32_bf16 v[40:43], a[116:119], a[32:35], v[40:43]\n" \
  "ds_read_b64_tr_b16 a[140:141], v103 offset:49152\n" \
  "v_mfma_f32_16x16x32_bf16 v[44:47], a[116:119], a[36:39], v[44:47]\n" \
  "ds_read_b64_tr_b16 a[142:143], v103 offset:53248\n" \
  "s_waitcnt lgkmcnt(8)\n" \
  "v_mfma_f32_16x16x32_bf16 v[48:51], a[120:123], a[32:35], v[48:51]\n" \
  "ds_read_b64_tr_b16 a[112:113], v96 offset:57344\n" \
  "s_add_u32 m0, s86, 8192\n" \
  "v_mfma_f32_16x16x32_bf16 v[52:55], a[120:123], a[36:39], v[52:55]\n" \
  "ds_read_b64_tr_b16 a[114:115], v96 offset:61440\n" \
  "buffer_load_dwordx4 v0, s[56:59], s13 offen offset:0 lds\n" \
  "v_mfma_f32_16x16x32_bf16 v[56:59], a[124:127], a[32:35], v[56:59]\n" \
  "ds_read_b64_tr_b16 a[116:117], v97 offset:57344\n" \
  "v_mfma_f32_16x16x32_bf16 v[60:63], a[124:127], a[36:39], v[60:63]\n" \
  "ds_read_b64_tr_b16 a[118:119], v97 offset:61440\n" \
  "s_waitcnt lgkmcnt(8)\n" \
  "v_mfma_f32_16x16x32_bf16 v[64:67], a[128:131], a[32:35], v[64:67]\n" \
  "ds_read_b64_tr_b16 a[120:121], v98 offset:57344\n" \
  "v_mfma_f32_16x16x32_bf16 v[68:71], a[128:131], a[36:39], v[68:71]\n" \
  "ds_read_b64_tr_b16 a[122:123], v98 offset:61440\n" \
  "buffer_load_dwordx4 a[68:71], v7, s[60:63], s14 offen offset:512 sc1\n" \
  "v_mfma_f32_16x16x32_bf16 v[72:75], a[132:135], a[32:35], v[72:75]\n" \
  "ds_read_b64_tr_b16 a[124:125], v99 offset:57344\n" \
  "s_add_u32 s14, s14, 2048\n" \
  "v_mfma_f32_16x16x32_bf16 v[76:79], a[132:135], a[36:39], v[76:79]\n" \
  "ds_read_b64_tr_b16 a[126:127], v99 offset:61440\n" \
  "s_waitcnt lgkmcnt(8)\n" \
  "v_mfma_f32_16x16x32_bf16 v[80:83], a[136:139], a[32:35], v[80:83]\n" \
  "ds_read_b64_tr_b16 a[128:129], v100 offset:57344\n" \
  "v_mfma_f32_16x16x32_bf16 v[84:87], a[136:139], a[36:39], v[84:87]\n" \
  "ds_read_b64_tr_b16 a[130:131], v100 offset:61440\n" \
  "buffer_load_dwordx4 v1, s[56:59], s13 offen offset:1024 lds\n" \
  "v_mfma_f32_16x16x32_bf16 v[88:91], a[140:143], a[32:35], v[88:91]\n" \
  "ds_read_b64_tr_b16 a[132:133], v101 offset:57344\n" \
  "s_add_u32 s13, s13, 65536\n" \
  "v_mfma_f32_16x16x32_bf16 v[92:95], a[140:143], a[36:39], v[92:95]\n" \
  "ds_read_b64_tr_b16 a[134:135], v101 offset:61440\n" \
  "s_waitcnt vmcnt(13) lgkmcnt(8)\n" \
  "v_mfma_f32_16x16x32_bf16 v[32:35], a[112:115], a[40:43], v[32:35]\n" \
  "ds_read_b64_tr_b16 a[136:137], v102 offset:57344\n" \
  "v_mfma_f32_16x16x32_bf16 v[36:39], a[112:115], a[44:47], v[36:39]\n" \
  "ds_read_b64_tr_b16 a[138:139], v102 offset:61440\n" \
  "buffer_load_dwordx4 a[72:75], v7, s[60:63], s14 offen offset:0 sc1\n" \
  "v_mfma_f32_16x16x32_bf16 v[40:43], a[116:119], a[40:43], v[40:43]\n" \
  "ds_read_b64_tr_b16 a[140:141], v103 offset:57344\n" \
  "v_mfma_f32_16x16x32_bf16 v[44:47], a[116:119], a[44:47], v[44:47]\n" \
  "ds_read_b64_tr_b16 a[142:143], v103 offset:61440\n" \
  "s_waitcnt lgkmcnt(8)\n" \
  "v_mfma_f32_16x16x32_bf16 v[48:51], a[120:123], a[40:43], v[48:51]\n" \
  "ds_read_b64_tr_b16 a[112:113], v104 offset:0\n" \
  "s_add_u32 m0, s86, 16384\n" \
  "v_mfma_f32_16x16x32_bf16 v[52:55], a[120:123], a[44:47], v[52:55]\n" \
  "ds_read_b64_tr_b16 a[114:115], v104 offset:4096\n" \
  "buffer_load_dwordx4 v0, s[56:59], s13 offen offset:0 lds\n" \
  "v_mfma_f32_16x16x32_bf16 v[56:59], a[124:127], a[40:43], v[56:59]\n" \
  "ds_read_b64_tr_b16 a[116:117], v105 offset:0\n" \
  "v_mfma_f32_16x16x32_bf16 v[60:63], a[124:127], a[44:47], v[60:63]\n" \
  "ds_read_b64_tr_b16 a[118:119], v105 offset:4096\n" \
  "s_waitcnt lgkmcnt(8)\n" \
  "v_mfma_f32_16x16x32_bf16 v[64:67], a[128:131], a[40:43], v[64:67]\n" \
  "ds_read_b64_tr_b16 a[120:121], v106 offset:0\n" \
  "v_mfma_f32_16x16x32_bf16 v[68:71], a[128:131], a[44:47], v[68:71]\n" \
  "ds_read_b64_tr_b16 a[122:123], v106 offset:4096\n" \
  "buffer_load_dwordx4 a[76:79], v7, s[60:63], s14 offen offset:512 sc1\n" \
  "v_mfma_f32_16x16x32_bf16 v[72:75], a[132:135], a[40:43], v[72:75]\n" \
  "ds_read_b64_tr_b16 a[124:125], v107 offset:0\n" \
  "s_add_u32 s14, s14, 2048\n" \
  "v_mfma_f32_16x16x32_bf16 v[76:79], a[132:135], a[44:47], v[76:79]\n" \
  "ds_read_b64_tr_b16 a[126:127], v107 offset:4096\n" \
  "s_waitcnt lgkmcnt(8)\n" \
  "v_mfma_f32_16x16x32_bf16 v[80:83], a[136:139], a[40:43], v[80:83]\n" \
  "ds_read_b64_tr_b16 a[128:129], v108 offset:0\n" \
  "v_mfma_f32_16x16x32_bf16 v[84:87], a[136:139], a[44:47], v[84:87]\n" \
  "ds_read_b64_tr_b16 a[130:131], v108 offset:4096\n" \
  "buffer_load_dwordx4 v1, s[56:59], s13 offen offset:1024 lds\n" \
  "v_mfma_f32_16x16x32_bf16 v[88:91], a[140:143], a[40:43], v[88:91]\n" \
  "ds_read_b64_tr_b16 a[132:133], v109 offset:0\n" \
  "s_add_u32 s13, s13, 65536\n" \
  "v_mfma_f32_16x16x32_bf16 v[92:95], a[140:143], a[44:47], v[92:95]\n" \
  "ds_read_b64_tr_b16 a[134:135], v109 offset:4096\n" \
  "s_waitcnt vmcnt(13) lgkmcnt(8)\n" \
  "v_mfma_f32_16x16x32_bf16 v[32:35], a[112:115], a[48:51], v[32:35]\n" \
  "ds_read_b64_tr_b16 a[136:137], v110 offset:0\n" \
  "v_mfma_f32_16x16x32_bf16 v[36:39], a[112:115], a[52:55], v[36:39]\n" \
  "ds_read_b64_tr_b16 a[138:139], v110 offset:4096\n" \
  "buffer_load_dwordx4 a[80:83], v7, s[60:63], s14 offen offset:0 sc1\n" \
  "v_mfma_f32_16x16x32_bf16 v[40:43], a[116:119], a[48:51], v[40:43]\n" \
  "ds_read_b64_tr_b16 a[140:141], v111 offset:0\n" \
  "v_mfma_f32_16x16x32_bf16 v[44:47], a[116:119], a[52:55], v[44:47]\n" \
  "ds_read_b64_tr_b16 a[142:143], v111 offset:4096\n" \
  "s_waitcnt lgkmcnt(8)\n" \
  "v_mfma_f32_16x16x32_bf16 v[48:51], a[120:123], a[48:51], v[48:51]\n" \
  "ds_read_b64_tr_b16 a[112:113], v104 offset:8192\n" \
  "s_add_u32 m0, s86, 24576\n" \
  "v_mfma_f32_16x16x32_bf16 v[52:55], a[120:123], a[52:55], v[52:55]\n" \
  "ds_read_b64_tr_b16 a[114:115], v104 offset:12288\n" \
  "buffer_load_dwordx4 v0, s[56:59], s13 offen offset:0 lds\n" \
  "v_mfma_f32_16x16x32_bf16 v[56:59], a[124:127], a[48:51], v[56:59]\n" \
  "ds_read_b64_tr_b16 a[116:117], v105 offset:8192\n" \
  "v_mfma_f32_16x16x32_bf16 v[60:63], a[124:127], a[52:55], v[60:63]\n" \
  "ds_read_b64_tr_b16 a[118:119], v105 offset:12288\n" \
  "s_waitcnt lgkmcnt(8)\n" \
  "v_mfma_f32_16x16x32_bf16 v[64:67], a[128:131], a[48:51], v[64:67]\n" \
  "ds_read_b64_tr_b16 a[120:121], v106 offset:8192\n" \
  "v_mfma_f32_16x16x32_bf16 v[68:71], a[128:131], a[52:55], v[68:71]\n" \
  "ds_read_b64_tr_b16 a[122:123], v106 offset:12288\n" \
  "buffer_load_dwordx4 a[84:87], v7, s[60:63], s14 offen offset:512 sc1\n" \
  "v_mfma_f32_16x16x32_bf16 v[72:75], a[132:135], a[48:51], v[72:75]\n" \
  "ds_read_b64_tr_b16 a[124:125], v107 offset:8192\n" \
  "s_add_u32 s14, s14, 2048\n" \
  "v_mfma_f32_16x16x32_bf16 v[76:79], a[132:135], a[52:55], v[76:79]\n" \
  "ds_read_b64_tr_b16 a[126:127], v107 offset:12288\n" \
  "s_waitcnt lgkmcnt(8)\n" \
  "v_mfma_f32_16x16x32_bf16 v[80:83], a[136:139], a[48:51], v[80:83]\n" \
  "ds_read_b64_tr_b16 a[128:129], v108 offset:8192\n" \
  "v_mfma_f32_16x16x32_bf16 v[84:87], a[136:139], a[52:55], v[84:87]\n" \
  "ds_read_b64_tr_b16 a[130:131], v108 offset:12288\n" \
  "buffer_load_dwordx4 v1, s[56:59], s13 offen offset:1024 lds\n" \
  "v_mfma_f32_16x16x32_bf16 v[88:91], a[140:143], a[48:51], v[88:91]\n" \
  "ds_read_b64_tr_b16 a[132:133], v109 offset:8192\n" \
  "s_add_u32 s13, s13, 65536\n" \
  "v_mfma_f32_16x16x32_bf16 v[92:95], a[140:143], a[52:55], v[92:95]\n" \
  "ds_read_b64_tr_b16 a[134:135], v109 offset:12288\n" \
  "s_waitcnt vmcnt(13) lgkmcnt(8)\n" \
  "v_mfma_f32_16x16x32_bf16 v[32:35], a[112:115], a[56:59], v[32:35]\n" \
  "ds_read_b64_tr_b16 a[136:137], v110 offset:8192\n" \
  "v_mfma_f32_16x16x32_bf16 v[36:39], a[112:115], a[60:63], v[36:39]\n" \
  "ds_read_b64_tr_b16 a[138:139], v110 offset:12288\n" \
  "buffer_load_dwordx4 a[88:91], v7, s[60:63], s14 offen offset:0 sc1\n" \
  "v_mfma_f32_16x16x32_bf16 v[40:43], a[116:119], a[56:59], v[40:43]\n" \
  "ds_read_b64_tr_b16 a[140:141], v111 offset:8192\n" \
  "v_mfma_f32_16x16x32_bf16 v[44:47], a[116:119], a[60:63], v[44:47]\n" \
  "ds_read_b64_tr_b16 a[142:143], v111 offset:12288\n" \
  "s_waitcnt lgkmcnt(8)\n" \
  "s_barrier\n" \
  "v_mfma_f32_16x16x32_bf16 v[48:51], a[120:123], a[56:59], v[48:51]\n" \
  "ds_read_b64_tr_b16 a[112:113], v104 offset:16384\n" \
  "s_add_u32 m0, s86, 32768\n" \
  "v_mfma_f32_16x16x32_bf16 v[52:55], a[120:123], a[60:63], v[52:55]\n" \
  "ds_read_b64_tr_b16 a[114:115], v104 offset:20480\n" \
  "buffer_load_dwordx4 v0, s[56:59], s13 offen offset:0 lds\n" \
  "v_mfma_f32_16x16x32_bf16 v[56:59], a[124:127], a[56:59], v[56:59]\n" \
  "ds_read_b64_tr_b16 a[116:117], v105 offset:16384\n" \
  "v_mfma_f32_16x16x32_bf16 v[60:63], a[124:127], a[60:63], v[60:63]\n" \
  "ds_read_b64_tr_b16 a[118:119], v105 offset:20480\n" \
  "s_waitcnt lgkmcnt(8)\n" \
  "v_mfma_f32_16x16x32_bf16 v[64:67], a[128:131], a[56:59], v[64:67]\n" \
  "ds_read_b64_tr_b16 a[120:121], v106 offset:16384\n" \
  "v_mfma_f32_16x16x32_bf16 v[68:71], a[128:131], a[60:63], v[68:71]\n" \
  "ds_read_b64_tr_b16 a[122:123], v106 offset:20480\n" \
  "buffer_load_dwordx4 a[92:95], v7, s[60:63], s14 offen offset:512 sc1\n" \
  "v_mfma_f32_16x16x32_bf16 v[72:75], a[132:135], a[56:59], v[72:75]\n" \
  "ds_read_b64_tr_b16 a[124:125], v107 offset:16384\n" \
  "s_add_u32 s14, s14, 2048\n" \
  "v_mfma_f32_16x16x32_bf16 v[76:79], a[132:135], a[60:63], v[76:79]\n" \
  "ds_read_b64_tr_b16 a[126:127], v107 offset:20480\n" \
  "s_waitcnt lgkmcnt(8)\n" \
  "v_mfma_f32_16x16x32_bf16 v[80:83], a[136:139], a[56:59], v[80:83]\n" \
  "ds_read_b64_tr_b16 a[128:129], v108 offset:16384\n" \
  "v_mfma_f32_16x16x32_bf16 v[84:87], a[136:139], a[60:63], v[84:87]\n" \
  "ds_read_b64_tr_b16 a[130:131], v108 offset:20480\n" \
  "buffer_load_dwordx4 v1, s[56:59], s13 offen offset:1024 lds\n" \
  "v_mfma_f32_16x16x32_bf16 v[88:91], a[140:143], a[56:59], v[88:91]\n" \
  "ds_read_b64_tr_b16 a[132:133], v109 offset:16384\n" \
  "s_add_u32 s13, s13, 65536\n" \
  "v_mfma_f32_16x16x32_bf16 v[92:95], a[140:143], a[60:63], v[92:95]\n" \
  "ds_read_b64_tr_b16 a[134:135], v109 offset:20480\n" \
  "s_waitcnt vmcnt(13) lgkmcnt(8)\n" \
  "v_mfma_f32_16x16x32_bf16 v[32:35], a[112:115], a[64:67], v[32:35]\n" \
  "ds_read_b64_tr_b16 a[136:137], v110 offset:16384\n" \
  "v_mfma_f32_16x16x32_bf16 v[36:39], a[112:115], a[68:71], v[36:39]\n" \
  "ds_read_b64_tr_b16 a[138:139], v110 offset:20480\n" \
  "buffer_load_dwordx4 a[96:99], v7, s[60:63], s14 offen offset:0 sc1\n" \
  "v_mfma_f32_16x16x32_bf16 v[40:43], a[116:119], a[64:67], v[40:43]\n" \
  "ds_read_b64_tr_b16 a[140:141], v111 offset:16384\n" \
  "v_mfma_f32_16x16x32_bf16 v[44:47], a[116:119], a[68:71], v[44:47]\n" \
  "ds_read_b64_tr_b16 a[142:143], v111 offset:20480\n" \
  "s_waitcnt lgkmcnt(8)\n" \
  "v_mfma_f32_16x16x32_bf16 v[48:51], a[120:123], a[64:67], v[48:51]\n" \
  "ds_read_b64_tr_b16 a[112:113], v104 offset:24576\n" \
  "s_add_u32 m0, s86, 40960\n" \
  "v_mfma_f32_16x16x32_bf16 v[52:55], a[120:123], a[68:71], v[52:55]\n" \
  "ds_read_b64_tr_b16 a[114:115], v104 offset:28672\n" \
  "buffer_load_dwordx4 v0, s[56:59], s13 offen offset:0 lds\n" \
  "v_mfma_f32_16x16x32_bf16 v[56:59], a[124:127], a[64:67], v[56:59]\n" \
  "ds_read_b64_tr_b16 a[116:117], v105 offset:24576\n" \
  "v_mfma_f32_16x16x32_bf16 v[60:63], a[124:127], a[68:71], v[60:63]\n" \
  "ds_read_b64_tr_b16 a[118:119], v105 offset:28672\n" \
  "s_waitcnt lgkmcnt(8)\n" \
  "v_mfma_f32_16x16x32_bf16 v[64:67], a[128:131], a[64:67], v[64:67]\n" \
  "ds_read_b64_tr_b16 a[120:121], v106 offset:24576\n" \
  "v_mfma_f32_16x16x32_bf16 v[68:71], a[128:131], a[68:71], v[68:71]\n" \
  "ds_read_b64_tr_b16 a[122:123], v106 offset:28672\n" \
  "buffer_load_dwordx4 a[100:103], v7, s[60:63], s14 offen offset:512 sc1\n" \
  "v_mfma_f32_16x16x32_bf16 v[72:75], a[132:135], a[64:67], v[72:75]\n" \
  "ds_read_b64_tr_b16 a[124:125], v107 offset:24576\n" \
  "s_add_u32 s14, s14, 2048\n" \
  "v_mfma_f32_16x16x32_bf16 v[76:79], a[132:135], a[68:71], v[76:79]\n" \
  "ds_read_b64_tr_b16 a[126:127], v107 offset:28672\n" \
  "s_waitcnt lgkmcnt(8)\n" \
  "v_mfma_f32_16x16x32_bf16 v[80:83], a[136:139], a[64:67], v[80:83]\n" \
  "ds_read_b64_tr_b16 a[128:129], v108 offset:24576\n" \
  "v_mfma_f32_16x16x32_bf16 v[84:87], a[136:139], a[68:71], v[84:87]\n" \
  "ds_read_b64_tr_b16 a[130:131], v108 offset:28672\n" \
  "buffer_load_dwordx4 v1, s[56:59], s13 offen offset:1024 lds\n" \
  "v_mfma_f32_16x16x32_bf16 v[88:91], a[140:143], a[64:67], v[88:91]\n" \
  "ds_read_b64_tr_b16 a[132:133], v109 offset:24576\n" \
  "s_add_u32 s13, s13, 65536\n" \
  "v_mfma_f32_16x16x32_bf16 v[92:95], a[140:143], a[68:71], v[92:95]\n" \
  "ds_read_b64_tr_b16 a[134:135], v109 offset:28672\n" \
  "s_waitcnt vmcnt(13) lgkmcnt(8)\n" \
  "v_mfma_f32_16x16x32_bf16 v[32:35], a[112:115], a[72:75], v[32:35]\n" \
  "ds_read_b64_tr_b16 a[136:137], v110 offset:24576\n" \
  "v_mfma_f32_16x16x32_bf16 v[36:39], a[112:115], a[76:79], v[36:39]\n" \
  "ds_read_b64_tr_b16 a[138:139], v110 offset:28672\n" \
  "buffer_load_dwordx4 a[104:107], v7, s[60:63], s14 offen offset:0 sc1\n" \
  "v_mfma_f32_16x16x32_bf16 v[40:43], a[116:119], a[72:75], v[40:43]\n" \
  "ds_read_b64_tr_b16 a[140:141], v111 offset:24576\n" \
  "v_mfma_f32_16x16x32_bf16 v[44:47], a[116:119], a[76:79], v[44:47]\n" \
  "ds_read_b64_tr_b16 a[142:143], v111 offset:28672\n" \
  "s_waitcnt lgkmcnt(8)\n" \
  "v_mfma_f32_16x16x32_bf16 v[48:51], a[120:123], a[72:75], v[48:51]\n" \
  "ds_read_b64_tr_b16 a[112:113], v104 offset:32768\n" \
  "s_add_u32 m0, s86, 49152\n" \
  "v_mfma_f32_16x16x32_bf16 v[52:55], a[120:123], a[76:79], v[52:55]\n" \
  "ds_read_b64_tr_b16 a[114:115], v104 offset:36864\n" \
  "buffer_load_dwordx4 v0, s[56:59], s13 offen offset:0 lds\n" \
  "v_mfma_f32_16x16x32_bf16 v[56:59], a[124:127], a[72:75], v[56:59]\n" \
  "ds_read_b64_tr_b16 a[116:117], v105 offset:32768\n" \
  "v_mfma_f32_16x16x32_bf16 v[60:63], a[124:127], a[76:79], v[60:63]\n" \
  "ds_read_b64_tr_b16 a[118:119], v105 offset:36864\n" \
  "s_waitcnt lgkmcnt(8)\n" \
  "v_mfma_f32_16x16x32_bf16 v[64:67], a[128:131], a[72:75], v[64:67]\n" \
  "ds_read_b64_tr_b16 a[120:121], v106 offset:32768\n" \
  "v_mfma_f32_16x16x32_bf16 v[68:71], a[128:131], a[76:79], v[68:71]\n" \
  "ds_read_b64_tr_b16 a[122:123], v106 offset:36864\n" \
  "buffer_load_dwordx4 a[108:111], v7, s[60:63], s14 offen offset:512 sc1\n" \
  "v_mfma_f32_16x16x32_bf16 v[72:75], a[132:135], a[72:75], v[72:75]\n" \
  "ds_read_b64_tr_b16 a[124:125], v107 offset:32768\n" \
  "s_add_u32 s14, s14, 2048\n" \
  "v_mfma_f32_16x16x32_bf16 v[76:79], a[132:135], a[76:79], v[76:79]\n" \
  "ds_read_b64_tr_b16 a[126:127], v107 offset:36864\n" \
  "s_waitcnt lgkmcnt(8)\n" \
  "v_mfma_f32_16x16x32_bf16 v[80:83], a[136:139], a[72:75], v[80:83]\n" \
  "ds_read_b64_tr_b16 a[128:129], v108 offset:32768\n" \
  "v_mfma_f32_16x16x32_bf16 v[84:87], a[136:139], a[76:79], v[84:87]\n" \
  "ds_read_b64_tr_b16 a[130:131], v108 offset:36864\n" \
  "buffer_load_dwordx4 v1, s[56:59], s13 offen offset:1024 lds\n" \
  "v_mfma_f32_16x16x32_bf16 v[88:91], a[140:143], a[72:75], v[88:91]\n" \
  "ds_read_b64_tr_b16 a[132:133], v109 offset:32768\n" \
  "s_add_u32 s13, s13, 65536\n" \
  "v_mfma_f32_16x16x32_bf16 v[92:95], a[140:143], a[76:79], v[92:95]\n" \
  "ds_read_b64_tr_b16 a[134:135], v109 offset:36864\n" \
  "s_waitcnt vmcnt(13) lgkmcnt(8)\n" \
  "v_mfma_f32_16x16x32_bf16 v[32:35], a[112:115], a[80:83], v[32:35]\n" \
  "ds_read_b64_tr_b16 a[136:137], v110 offset:32768\n" \
  "v_mfma_f32_16x16x32_bf16 v[36:39], a[112:115], a[84:87], v[36:39]\n" \
  "ds_read_b64_tr_b16 a[138:139], v110 offset:36864\n" \
  "buffer_load_dwordx4 a[0:3], v7, s[60:63], s14 offen offset:0 sc1\n" \
  "v_mfma_f32_16x16x32_bf16 v[40:43], a[116:119], a[80:83], v[40:43]\n" \
  "ds_read_b64_tr_b16 a[140:141], v111 offset:32768\n" \
  "v_mfma_f32_16x16x32_bf16 v[44:47], a[116:119], a[84:87], v[44:47]\n" \
  "ds_read_b64_tr_b16 a[142:143], v111 offset:36864\n" \
  "s_waitcnt lgkmcnt(8)\n" \
  "v_mfma_f32_16x16x32_bf16 v[48:51], a[120:123], a[80:83], v[48:51]\n" \
  "ds_read_b64_tr_b16 a[112:113], v104 offset:40960\n" \
  "s_add_u32 m0, s86, 57344\n" \
  "v_mfma_f32_16x16x32_bf16 v[52:55], a[120:123], a[84:87], v[52:55]\n" \
  "ds_read_b64_tr_b16 a[114:115], v104 offset:45056\n" \
  "buffer_load_dwordx4 v0, s[56:59], s13 offen offset:0 lds\n" \
  "v_mfma_f32_16x16x32_bf16 v[56:59], a[124:127], a[80:83], v[56:59]\n" \
  "ds_read_b64_tr_b16 a[116:117], v105 offset:40960\n" \
  "v_mfma_f32_16x16x32_bf16 v[60:63], a[124:127], a[84:87], v[60:63]\n" \
  "ds_read_b64_tr_b16 a[118:119], v105 offset:45056\n" \
  "s_waitcnt lgkmcnt(8)\n" \
  "v_mfma_f32_16x16x32_bf16 v[64:67], a[128:131], a[80:83], v[64:67]\n" \
  "ds_read_b64_tr_b16 a[120:121], v106 offset:40960\n" \
  "v_mfma_f32_16x16x32_bf16 v[68:71], a[128:131], a[84:87], v[68:71]\n" \
  "ds_read_b64_tr_b16 a[122:123], v106 offset:45056\n" \
  "buffer_load_dwordx4 a[4:7], v7, s[60:63], s14 offen offset:512 sc1\n" \
  "v_mfma_f32_16x16x32_bf16 v[72:75], a[132:135], a[80:83], v[72:75]\n" \
  "ds_read_b64_tr_b16 a[124:125], v107 offset:40960\n" \
  "s_add_u32 s14, s14, 2048\n" \
  "v_mfma_f32_16x16x32_bf16 v[76:79], a[132:135], a[84:87], v[76:79]\n" \
  "ds_read_b64_tr_b16 a[126:127], v107 offset:45056\n" \
  "s_waitcnt lgkmcnt(8)\n" \
  "v_mfma_f32_16x16x32_bf16 v[80:83], a[136:139], a[80:83], v[80:83]\n" \
  "ds_read_b64_tr_b16 a[128:129], v108 offset:40960\n" \
  "v_mfma_f32_16x16x32_bf16 v[84:87], a[136:139], a[84:87], v[84:87]\n" \
  "ds_read_b64_tr_b16 a[130:131], v108 offset:45056\n" \
  "buffer_load_dwordx4 v1, s[56:59], s13 offen offset:1024 lds\n" \
  "v_mfma_f32_16x16x32_bf16 v[88:91], a[140:143], a[80:83], v[88:91]\n" \
  "ds_read_b64_tr_b16 a[132:133], v109 offset:40960\n" \
  "s_add_u32 s13, s13, 65536\n" \
  "v_mfma_f32_16x16x32_bf16 v[92:95], a[140:143], a[84:87], v[92:95]\n" \
  "ds_read_b64_tr_b16 a[134:135], v109 offset:45056\n" \
  "s_waitcnt vmcnt(13) lgkmcnt(8)\n" \
  "v_mfma_f32_16x16x32_bf16 v[32:35], a[112:115], a[88:91], v[32:35]\n" \
  "ds_read_b64_tr_b16 a[136:137], v110 offset:40960\n" \
  "v_mfma_f32_16x16x32_bf16 v[36:39], a[112:115], a[92:95], v[36:39]\n" \
  "ds_read_b64_tr_b16 a[138:139], v110 offset:45056\n" \
  "buffer_load_dwordx4 a[8:11], v7, s[60:63], s14 offen offset:0 sc1\n" \
  "v_mfma_f32_16x16x32_bf16 v[40:43], a[116:119], a[88:91], v[40:43]\n" \
  "ds_read_b64_tr_b16 a[140:141], v111 offset:40960\n" \
  "v_mfma_f32_16x16x32_bf16 v[44:47], a[116:119], a[92:95], v[44:47]\n" \
  "ds_read_b64_tr_b16 a[142:143], v111 offset:45056\n" \
  "s_waitcnt lgkmcnt(8)\n" \
  "s_barrier\n" \
  "v_mfma_f32_16x16x32_bf16 v[48:51], a[120:123], a[88:91], v[48:51]\n" \
  "ds_read_b64_tr_b16 a[112:113], v104 offset:49152\n" \
  "s_add_u32 m0, s86, 65536\n" \
  "v_mfma_f32_16x16x32_bf16 v[52:55], a[120:123], a[92:95], v[52:55]\n" \
  "ds_read_b64_tr_b16 a[114:115], v104 offset:53248\n" \
  "buffer_load_dwordx4 v0, s[56:59], s13 offen offset:0 lds\n" \
  "v_mfma_f32_16x16x32_bf16 v[56:59], a[124:127], a[88:91], v[56:59]\n" \
  "ds_read_b64_tr_b16 a[116:117], v105 offset:49152\n" \
  "v_mfma_f32_16x16x32_bf16 v[60:63], a[124:127], a[92:95], v[60:63]\n" \
  "ds_read_b64_tr_b16 a[118:119], v105 offset:53248\n" \
  "s_waitcnt lgkmcnt(8)\n" \
  "v_mfma_f32_16x16x32_bf16 v[64:67], a[128:131], a[88:91], v[64:67]\n" \
  "ds_read_b64_tr_b16 a[120:121], v106 offset:49152\n" \
  "v_mfma_f32_16x16x32_bf16 v[68:71], a[128:131], a[92:95], v[68:71]\n" \
  "ds_read_b64_tr_b16 a[122:123], v106 offset:53248\n" \
  "buffer_load_dwordx4 a[12:15], v7, s[60:63], s14 offen offset:512 sc1\n" \
  "v_mfma_f32_16x16x32_bf16 v[72:75], a[132:135], a[88:91], v[72:75]\n" \
  "ds_read_b64_tr_b16 a[124:125], v107 offset:49152\n" \
  "s_add_u32 s14, s14, 2048\n" \
  "v_mfma_f32_16x16x32_bf16 v[76:79], a[132:135], a[92:95], v[76:79]\n" \
  "ds_read_b64_tr_b16 a[126:127], v107 offset:53248\n" \
  "s_waitcnt lgkmcnt(8)\n" \
  "v_mfma_f32_16x16x32_bf16 v[80:83], a[136:139], a[88:91], v[80:83]\n" \
  "ds_read_b64_tr_b16 a[128:129], v108 offset:49152\n" \
  "v_mfma_f32_16x16x32_bf16 v[84:87], a[136:139], a[92:95], v[84:87]\n" \
  "ds_read_b64_tr_b16 a[130:131], v108 offset:53248\n" \
  "buffer_load_dwordx4 v1, s[56:59], s13 offen offset:1024 lds\n" \
  "v_mfma_f32_16x16x32_bf16 v[88:91], a[140:143], a[88:91], v[88:91]\n" \
  "ds_read_b64_tr_b16 a[132:133], v109 offset:49152\n" \
  "s_add_u32 s13, s13, 65536\n" \
  "v_mfma_f32_16x16x32_bf16 v[92:95], a[140:143], a[92:95], v[92:95]\n" \
  "ds_read_b64_tr_b16 a[134:135], v109 offset:53248\n" \
  "s_waitcnt vmcnt(13) lgkmcnt(8)\n" \
  "v_mfma_f32_16x16x32_bf16 v[32:35], a[112:115], a[96:99], v[32:35]\n" \
  "ds_read_b64_tr_b16 a[136:137], v110 offset:49152\n" \
  "v_mfma_f32_16x16x32_bf16 v[36:39], a[112:115], a[100:103], v[36:39]\n" \
  "ds_read_b64_tr_b16 a[138:139], v110 offset:53248\n" \
  "buffer_load_dwordx4 a[16:19], v7, s[60:63], s14 offen offset:0 sc1\n" \
  "v_mfma_f32_16x16x32_bf16 v[40:43], a[116:119], a[96:99], v[40:43]\n" \
  "ds_read_b64_tr_b16 a[140:141], v111 offset:49152\n" \
  "v_mfma_f32_16x16x32_bf16 v[44:47], a[116:119], a[100:103], v[44:47]\n" \
  "ds_read_b64_tr_b16 a[142:143], v111 offset:53248\n" \
  "s_waitcnt lgkmcnt(8)\n" \
  "v_mfma_f32_16x16x32_bf16 v[48:51], a[120:123], a[96:99], v[48:51]\n" \
  "ds_read_b64_tr_b16 a[112:113], v104 offset:57344\n" \
  "s_add_u32 m0, s86, 73728\n" \
  "v_mfma_f32_16x16x32_bf16 v[52:55], a[120:123], a[100:103], v[52:55]\n" \
  "ds_read_b64_tr_b16 a[114:115], v104 offset:61440\n" \
  "buffer_load_dwordx4 v0, s[56:59], s13 offen offset:0 lds\n" \
  "v_mfma_f32_16x16x32_bf16 v[56:59], a[124:127], a[96:99], v[56:59]\n" \
  "ds_read_b64_tr_b16 a[116:117], v105 offset:57344\n" \
  "v_mfma_f32_16x16x32_bf16 v[60:63], a[124:127], a[100:103], v[60:63]\n" \
  "ds_read_b64_tr_b16 a[118:119], v105 offset:61440\n" \
  "s_waitcnt lgkmcnt(8)\n" \
  "v_mfma_f32_16x16x32_bf16 v[64:67], a[128:131], a[96:99], v[64:67]\n" \
  "ds_read_b64_tr_b16 a[120:121], v106 offset:57344\n" \
  "v_mfma_f32_16x16x32_bf16 v[68:71], a[128:131], a[100:103], v[68:71]\n" \
  "ds_read_b64_tr_b16 a[122:123], v106 offset:61440\n" \
  "buffer_load_dwordx4 a[20:23], v7, s[60:63], s14 offen offset:512 sc1\n" \
  "v_mfma_f32_16x16x32_bf16 v[72:75], a[132:135], a[96:99], v[72:75]\n" \
  "ds_read_b64_tr_b16 a[124:125], v107 offset:57344\n" \
  "s_add_u32 s14, s14, 2048\n" \
  "v_mfma_f32_16x16x32_bf16 v[76:79], a[132:135], a[100:103], v[76:79]\n" \
  "ds_read_b64_tr_b16 a[126:127], v107 offset:61440\n" \
  "s_waitcnt lgkmcnt(8)\n" \
  "v_mfma_f32_16x16x32_bf16 v[80:83], a[136:139], a[96:99], v[80:83]\n" \
  "ds_read_b64_tr_b16 a[128:129], v108 offset:57344\n" \
  "v_mfma_f32_16x16x32_bf16 v[84:87], a[136:139], a[100:103], v[84:87]\n" \
  "ds_read_b64_tr_b16 a[130:131], v108 offset:61440\n" \
  "buffer_load_dwordx4 v1, s[56:59], s13 offen offset:1024 lds\n" \
  "v_mfma_f32_16x16x32_bf16 v[88:91], a[140:143], a[96:99], v[88:91]\n" \
  "ds_read_b64_tr_b16 a[132:133], v109 offset:57344\n" \
  "s_add_u32 s13, s13, 65536\n" \
  "v_mfma_f32_16x16x32_bf16 v[92:95], a[140:143], a[100:103], v[92:95]\n" \
  "ds_read_b64_tr_b16 a[134:135], v109 offset:61440\n" \
  "s_waitcnt vmcnt(13) lgkmcnt(8)\n" \
  "v_mfma_f32_16x16x32_bf16 v[32:35], a[112:115], a[104:107], v[32:35]\n" \
  "ds_read_b64_tr_b16 a[136:137], v110 offset:57344\n" \
  "v_mfma_f32_16x16x32_bf16 v[36:39], a[112:115], a[108:111], v[36:39]\n" \
  "ds_read_b64_tr_b16 a[138:139], v110 offset:61440\n" \
  "buffer_load_dwordx4 a[24:27], v7, s[60:63], s14 offen offset:0 sc1\n" \
  "v_mfma_f32_16x16x32_bf16 v[40:43], a[116:119], a[104:107], v[40:43]\n" \
  "ds_read_b64_tr_b16 a[140:141], v111 offset:57344\n" \
  "v_mfma_f32_16x16x32_bf16 v[44:47], a[116:119], a[108:111], v[44:47]\n" \
  "ds_read_b64_tr_b16 a[142:143], v111 offset:61440\n" \
  "s_waitcnt lgkmcnt(8)\n" \
  "v_mfma_f32_16x16x32_bf16 v[48:51], a[120:123], a[104:107], v[48:51]\n" \
  "ds_read_b64_tr_b16 a[112:113], v112 offset:0\n" \
  "v_mfma_f32_16x16x32_bf16 v[52:55], a[120:123], a[108:111], v[52:55]\n" \
  "ds_read_b64_tr_b16 a[114:115], v112 offset:4096\n" \
  "buffer_load_dwordx4 a[28:31], v7, s[60:63], s14 offen offset:512 sc1\n" \
  "v_mfma_f32_16x16x32_bf16 v[56:59], a[124:127], a[104:107], v[56:59]\n" \
  "ds_read_b64_tr_b16 a[116:117], v113 offset:0\n" \
  "s_add_u32 s14, s14, 2048\n" \
  "v_mfma_f32_16x16x32_bf16 v[60:63], a[124:127], a[108:111], v[60:63]\n" \
  "ds_read_b64_tr_b16 a[118:119], v113 offset:4096\n" \
  "s_waitcnt lgkmcnt(8)\n" \
  "v_mfma_f32_16x16x32_bf16 v[64:67], a[128:131], a[104:107], v[64:67]\n" \
  "ds_read_b64_tr_b16 a[120:121], v114 offset:0\n" \
  "v_mfma_f32_16x16x32_bf16 v[68:71], a[128:131], a[108:111], v[68:71]\n" \
  "ds_read_b64_tr_b16 a[122:123], v114 offset:4096\n" \
  "buffer_load_dwordx4 a[32:35], v7, s[60:63], s14 offen offset:0 sc1\n" \
  "v_mfma_f32_16x16x32_bf16 v[72:75], a[132:135], a[104:107], v[72:75]\n" \
  "ds_read_b64_tr_b16 a[124:125], v115 offset:0\n" \
  "v_mfma_f32_16x16x32_bf16 v[76:79], a[132:135], a[108:111], v[76:79]\n" \
  "ds_read_b64_tr_b16 a[126:127], v115 offset:4096\n" \
  "s_waitcnt lgkmcnt(8)\n" \
  "v_mfma_f32_16x16x32_bf16 v[80:83], a[136:139], a[104:107], v[80:83]\n" \
  "ds_read_b64_tr_b16 a[128:129], v116 offset:0\n" \
  "v_mfma_f32_16x16x32_bf16 v[84:87], a[136:139], a[108:111], v[84:87]\n" \
  "ds_read_b64_tr_b16 a[130:131], v116 offset:4096\n" \
  "buffer_load_dwordx4 a[36:39], v7, s[60:63], s14 offen offset:512 sc1\n" \
  "v_mfma_f32_16x16x32_bf16 v[88:91], a[140:143], a[104:107], v[88:91]\n" \
  "ds_read_b64_tr_b16 a[132:133], v117 offset:0\n" \
  "s_add_u32 s14, s14, 2048\n" \
  "v_mfma_f32_16x16x32_bf16 v[92:95], a[140:143], a[108:111], v[92:95]\n" \
  "ds_read_b64_tr_b16 a[134:135], v117 offset:4096\n" \
  "s_waitcnt vmcnt(13) lgkmcnt(8)\n" \
  "v_mfma_f32_16x16x32_bf16 v[32:35], a[112:115], a[0:3], v[32:35]\n" \
  "ds_read_b64_tr_b16 a[136:137], v118 offset:0\n" \
  "v_mfma_f32_16x16x32_bf16 v[36:39], a[112:115], a[4:7], v[36:39]\n" \
  "ds_read_b64_tr_b16 a[138:139], v118 offset:4096\n" \
  "buffer_load_dwordx4 a[40:43], v7, s[60:63], s14 offen offset:0 sc1\n" \
  "v_mfma_f32_16x16x32_bf16 v[40:43], a[116:119], a[0:3], v[40:43]\n" \
  "ds_read_b64_tr_b16 a[140:141], v119 offset:0\n" \
  "v_mfma_f32_16x16x32_bf16 v[44:47], a[116:119], a[4:7], v[44:47]\n" \
  "ds_read_b64_tr_b16 a[142:143], v119 offset:4096\n" \
  "s_waitcnt lgkmcnt(8)\n" \
  "v_mfma_f32_16x16x32_bf16 v[48:51], a[120:123], a[0:3], v[48:51]\n" \
  "ds_read_b64_tr_b16 a[112:113], v112 offset:8192\n" \
  "v_mfma_f32_16x16x32_bf16 v[52:55], a[120:123], a[4:7], v[52:55]\n" \
  "ds_read_b64_tr_b16 a[114:115], v112 offset:12288\n" \
  "buffer_load_dwordx4 a[44:47], v7, s[60:63], s14 offen offset:512 sc1\n" \
  "v_mfma_f32_16x16x32_bf16 v[56:59], a[124:127], a[0:3], v[56:59]\n" \
  "ds_read_b64_tr_b16 a[116:117], v113 offset:8192\n" \
  "s_add_u32 s14, s14, 2048\n" \
  "v_mfma_f32_16x16x32_bf16 v[60:63], a[124:127], a[4:7], v[60:63]\n" \
  "ds_read_b64_tr_b16 a[118:119], v113 offset:12288\n" \
  "s_waitcnt lgkmcnt(8)\n" \
  "v_mfma_f32_16x16x32_bf16 v[64:67], a[128:131], a[0:3], v[64:67]\n" \
  "ds_read_b64_tr_b16 a[120:121], v114 offset:8192\n" \
  "v_mfma_f32_16x16x32_bf16 v[68:71], a[128:131], a[4:7], v[68:71]\n" \
  "ds_read_b64_tr_b16 a[122:123], v114 offset:12288\n" \
  "buffer_load_dwordx4 a[48:51], v7, s[60:63], s14 offen offset:0 sc1\n" \
  "v_mfma_f32_16x16x32_bf16 v[72:75], a[132:135], a[0:3], v[72:75]\n" \
  "ds_read_b64_tr_b16 a[124:125], v115 offset:8192\n" \
  "v_mfma_f32_16x16x32_bf16 v[76:79], a[132:135], a[4:7], v[76:79]\n" \
  "ds_read_b64_tr_b16 a[126:127], v115 offset:12288\n" \
  "s_waitcnt lgkmcnt(8)\n" \
  "v_mfma_f32_16x16x32_bf16 v[80:83], a[136:139], a[0:3], v[80:83]\n" \
  "ds_read_b64_tr_b16 a[128:129], v116 offset:8192\n" \
  "v_mfma_f32_16x16x32_bf16 v[84:87], a[136:139], a[4:7], v[84:87]\n" \
  "ds_read_b64_tr_b16 a[130:131], v116 offset:12288\n" \
  "v_mfma_f32_16x16x32_bf16 v[88:91], a[140:143], a[0:3], v[88:91]\n" \
  "ds_read_b64_tr_b16 a[132:133], v117 offset:8192\n" \
  "v_mfma_f32_16x16x32_bf16 v[92:95], a[140:143], a[4:7], v[92:95]\n" \
  "ds_read_b64_tr_b16 a[134:135], v117 offset:12288\n" \
  "s_waitcnt vmcnt(12) lgkmcnt(8)\n" \
  "v_mfma_f32_16x16x32_bf16 v[32:35], a[112:115], a[8:11], v[32:35]\n" \
  "ds_read_b64_tr_b16 a[136:137], v118 offset:8192\n" \
  "v_mfma_f32_16x16x32_bf16 v[36:39], a[112:115], a[12:15], v[36:39]\n" \
  "ds_read_b64_tr_b16 a[138:139], v118 offset:12288\n" \
  "buffer_load_dwordx4 a[52:55], v7, s[60:63], s14 offen offset:512 sc1\n" \
  "v_mfma_f32_16x16x32_bf16 v[40:43], a[116:119], a[8:11], v[40:43]\n" \
  "ds_read_b64_tr_b16 a[140:141], v119 offset:8192\n" \
  "s_add_u32 s14, s14, 2048\n" \
  "v_mfma_f32_16x16x32_bf16 v[44:47], a[116:119], a[12:15], v[44:47]\n" \
  "ds_read_b64_tr_b16 a[142:143], v119 offset:12288\n" \
  "s_waitcnt lgkmcnt(8)\n" \
  "s_barrier\n" \
  "v_mfma_f32_16x16x32_bf16 v[48:51], a[120:123], a[8:11], v[48:51]\n" \
  "ds_read_b64_tr_b16 a[112:113], v112 offset:16384\n" \
  "v_mfma_f32_16x16x32_bf16 v[52:55], a[120:123], a[12:15], v[52:55]\n" \
  "ds_read_b64_tr_b16 a[114:115], v112 offset:20480\n" \
  "buffer_load_dwordx4 a[56:59], v7, s[60:63], s14 offen offset:0 sc1\n" \
  "v_mfma_f32_16x16x32_bf16 v[56:59], a[124:127], a[8:11], v[56:59]\n" \
  "ds_read_b64_tr_b16 a[116:117], v113 offset:16384\n" \
  "v_mfma_f32_16x16x32_bf16 v[60:63], a[124:127], a[12:15], v[60:63]\n" \
  "ds_read_b64_tr_b16 a[118:119], v113 offset:20480\n" \
  "s_waitcnt lgkmcnt(8)\n" \
  "v_mfma_f32_16x16x32_bf16 v[64:67], a[128:131], a[8:11], v[64:67]\n" \
  "ds_read_b64_tr_b16 a[120:121], v114 offset:16384\n" \
  "v_mfma_f32_16x16x32_bf16 v[68:71], a[128:131], a[12:15], v[68:71]\n" \
  "ds_read_b64_tr_b16 a[122:123], v114 offset:20480\n" \
  "buffer_load_dwordx4 a[60:63], v7, s[60:63], s14 offen offset:512 sc1\n" \
  "v_mfma_f32_16x16x32_bf16 v[72:75], a[132:135], a[8:11], v[72:75]\n" \
  "ds_read_b64_tr_b16 a[124:125], v115 offset:16384\n" \
  "s_add_u32 s14, s14, 2048\n" \
  "v_mfma_f32_16x16x32_bf16 v[76:79], a[132:135], a[12:15], v[76:79]\n" \
  "ds_read_b64_tr_b16 a[126:127], v115 offset:20480\n" \
  "s_waitcnt lgkmcnt(8)\n" \
  "v_mfma_f32_16x16x32_bf16 v[80:83], a[136:139], a[8:11], v[80:83]\n" \
  "ds_read_b64_tr_b16 a[128:129], v116 offset:16384\n" \
  "v_mfma_f32_16x16x32_bf16 v[84:87], a[136:139], a[12:15], v[84:87]\n" \
  "ds_read_b64_tr_b16 a[130:131], v116 offset:20480\n" \
  "buffer_load_dwordx4 a[64:67], v7, s[60:63], s14 offen offset:0 sc1\n" \
  "v_mfma_f32_16x16x32_bf16 v[88:91], a[140:143], a[8:11], v[88:91]\n" \
  "ds_read_b64_tr_b16 a[132:133], v117 offset:16384\n" \
  "v_mfma_f32_16x16x32_bf16 v[92:95], a[140:143], a[12:15], v[92:95]\n" \
  "ds_read_b64_tr_b16 a[134:135], v117 offset:20480\n" \
  "s_waitcnt vmcnt(12) lgkmcnt(8)\n" \
  "v_mfma_f32_16x16x32_bf16 v[32:35], a[112:115], a[16:19], v[32:35]\n" \
  "ds_read_b64_tr_b16 a[136:137], v118 offset:16384\n" \
  "v_mfma_f32_16x16x32_bf16 v[36:39], a[112:115], a[20:23], v[36:39]\n" \
  "ds_read_b64_tr_b16 a[138:139], v118 offset:20480\n" \
  "buffer_load_dwordx4 a[68:71], v7, s[60:63], s14 offen offset:512 sc1\n" \
  "v_mfma_f32_16x16x32_bf16 v[40:43], a[116:119], a[16:19], v[40:43]\n" \
  "ds_read_b64_tr_b16 a[140:141], v119 offset:16384\n" \
  "s_add_u32 s14, s14, 2048\n" \
  "v_mfma_f32_16x16x32_bf16 v[44:47], a[116:119], a[20:23], v[44:47]\n" \
  "ds_read_b64_tr_b16 a[142:143], v119 offset:20480\n" \
  "buffer_load_dwordx4 a[72:75], v7, s[60:63], s14 offen offset:0 sc1\n" \
  "s_waitcnt lgkmcnt(8)\n" \
  "v_mfma_f32_16x16x32_bf16 v[48:51], a[120:123], a[16:19], v[48:51]\n" \
  "ds_read_b64_tr_b16 a[112:113], v112 offset:24576\n" \
  "v_mfma_f32_16x16x32_bf16 v[52:55], a[120:123], a[20:23], v[52:55]\n" \
  "ds_read_b64_tr_b16 a[114:115], v112 offset:28672\n" \
  "buffer_load_dwordx4 a[76:79], v7, s[60:63], s14 offen offset:512 sc1\n" \
  "v_mfma_f32_16x16x32_bf16 v[56:59], a[124:127], a[16:19], v[56:59]\n" \
  "ds_read_b64_tr_b16 a[116:117], v113 offset:24576\n" \
  "s_add_u32 s14, s14, 2048\n" \
  "v_mfma_f32_16x16x32_bf16 v[60:63], a[124:127], a[20:23], v[60:63]\n" \
  "ds_read_b64_tr_b16 a[118:119], v113 offset:28672\n" \
  "s_waitcnt lgkmcnt(8)\n" \
  "v_mfma_f32_16x16x32_bf16 v[64:67], a[128:131], a[16:19], v[64:67]\n" \
  "ds_read_b64_tr_b16 a[120:121], v114 offset:24576\n" \
  "v_mfma_f32_16x16x32_bf16 v[68:71], a[128:131], a[20:23], v[68:71]\n" \
  "ds_read_b64_tr_b16 a[122:123], v114 offset:28672\n" \
  "buffer_load_dwordx4 a[80:83], v7, s[60:63], s14 offen offset:0 sc1\n" \
  "v_mfma_f32_16x16x32_bf16 v[72:75], a[132:135], a[16:19], v[72:75]\n" \
  "ds_read_b64_tr_b16 a[124:125], v115 offset:24576\n" \
  "v_mfma_f32_16x16x32_bf16 v[76:79], a[132:135], a[20:23], v[76:79]\n" \
  "ds_read_b64_tr_b16 a[126:127], v115 offset:28672\n" \
  "buffer_load_dwordx4 a[84:87], v7, s[60:63], s14 offen offset:512 sc1\n" \
  "s_waitcnt lgkmcnt(8)\n" \
  "v_mfma_f32_16x16x32_bf16 v[80:83], a[136:139], a[16:19], v[80:83]\n" \
  "ds_read_b64_tr_b16 a[128:129], v116 offset:24576\n" \
  "s_add_u32 s14, s14, 2048\n" \
  "v_mfma_f32_16x16x32_bf16 v[84:87], a[136:139], a[20:23], v[84:87]\n" \
  "ds_read_b64_tr_b16 a[130:131], v116 offset:28672\n" \
  "buffer_load_dwordx4 a[88:91], v7, s[60:63], s14 offen offset:0 sc1\n" \
  "v_mfma_f32_16x16x32_bf16 v[88:91], a[140:143], a[16:19], v[88:91]\n" \
  "ds_read_b64_tr_b16 a[132:133], v117 offset:24576\n" \
  "v_mfma_f32_16x16x32_bf16 v[92:95], a[140:143], a[20:23], v[92:95]\n" \
  "ds_read_b64_tr_b16 a[134:135], v117 offset:28672\n" \
  "s_waitcnt vmcnt(15) lgkmcnt(8)\n" \
  "v_mfma_f32_16x16x32_bf16 v[32:35], a[112:115], a[24:27], v[32:35]\n" \
  "ds_read_b64_tr_b16 a[136:137], v118 offset:24576\n" \
  "v_mfma_f32_16x16x32_bf16 v[36:39], a[112:115], a[28:31], v[36:39]\n" \
  "ds_read_b64_tr_b16 a[138:139], v118 offset:28672\n" \
  "buffer_load_dwordx4 a[92:95], v7, s[60:63], s14 offen offset:512 sc1\n" \
  "v_mfma_f32_16x16x32_bf16 v[40:43], a[116:119], a[24:27], v[40:43]\n" \
  "ds_read_b64_tr_b16 a[140:141], v119 offset:24576\n" \
  "s_add_u32 s14, s14, 2048\n" \
  "v_mfma_f32_16x16x32_bf16 v[44:47], a[116:119], a[28:31], v[44:47]\n" \
  "ds_read_b64_tr_b16 a[142:143], v119 offset:28672\n" \
  "buffer_load_dwordx4 a[96:99], v7, s[60:63], s14 offen offset:0 sc1\n" \
  "s_waitcnt lgkmcnt(8)\n" \
  "v_mfma_f32_16x16x32_bf16 v[48:51], a[120:123], a[24:27], v[48:51]\n" \
  "ds_read_b64_tr_b16 a[112:113], v96 offset:0\n" \
  "v_mfma_f32_16x16x32_bf16 v[52:55], a[120:123], a[28:31], v[52:55]\n" \
  "ds_read_b64_tr_b16 a[114:115], v96 offset:4096\n" \
  "buffer_load_dwordx4 a[100:103], v7, s[60:63], s14 offen offset:512 sc1\n" \
  "v_mfma_f32_16x16x32_bf16 v[56:59], a[124:127], a[24:27], v[56:59]\n" \
  "ds_read_b64_tr_b16 a[116:117], v97 offset:0\n" \
  "s_add_u32 s14, s14, 2048\n" \
  "v_mfma_f32_16x16x32_bf16 v[60:63], a[124:127], a[28:31], v[60:63]\n" \
  "ds_read_b64_tr_b16 a[118:119], v97 offset:4096\n" \
  "s_waitcnt lgkmcnt(8)\n" \
  "v_mfma_f32_16x16x32_bf16 v[64:67], a[128:131], a[24:27], v[64:67]\n" \
  "ds_read_b64_tr_b16 a[120:121], v98 offset:0\n" \
  "v_mfma_f32_16x16x32_bf16 v[68:71], a[128:131], a[28:31], v[68:71]\n" \
  "ds_read_b64_tr_b16 a[122:123], v98 offset:4096\n" \
  "buffer_load_dwordx4 a[104:107], v7, s[60:63], s14 offen offset:0 sc1\n" \
  "v_mfma_f32_16x16x32_bf16 v[72:75], a[132:135], a[24:27], v[72:75]\n" \
  "ds_read_b64_tr_b16 a[124:125], v99 offset:0\n" \
  "v_mfma_f32_16x16x32_bf16 v[76:79], a[132:135], a[28:31], v[76:79]\n" \
  "ds_read_b64_tr_b16 a[126:127], v99 offset:4096\n" \
  "buffer_load_dwordx4 a[108:111], v7, s[60:63], s14 offen offset:512 sc1\n" \
  "s_waitcnt lgkmcnt(8)\n" \
  "v_mfma_f32_16x16x32_bf16 v[80:83], a[136:139], a[24:27], v[80:83]\n" \
  "ds_read_b64_tr_b16 a[128:129], v100 offset:0\n" \
  "s_add_u32 s14, s14, 2048\n" \
  "v_mfma_f32_16x16x32_bf16 v[84:87], a[136:139], a[28:31], v[84:87]\n" \
  "ds_read_b64_tr_b16 a[130:131], v100 offset:4096\n" \
  "v_mfma_f32_16x16x32_bf16 v[88:91], a[140:143], a[24:27], v[88:91]\n" \
  "ds_read_b64_tr_b16 a[132:133], v101 offset:0\n" \
  "v_mfma_f32_16x16x32_bf16 v[92:95], a[140:143], a[28:31], v[92:95]\n" \
  "ds_read_b64_tr_b16 a[134:135], v101 offset:4096\n" \
  "s_waitcnt vmcnt(18) lgkmcnt(8)\n" \
  "v_mfma_f32_16x16x32_bf16 v[32:35], a[112:115], a[32:35], v[32:35]\n" \
  "ds_read_b64_tr_b16 a[136:137], v102 offset:0\n" \
  "v_mfma_f32_16x16x32_bf16 v[36:39], a[112:115], a[36:39], v[36:39]\n" \
  "ds_read_b64_tr_b16 a[138:139], v102 offset:4096\n" \
  "v_mfma_f32_16x16x32_bf16 v[40:43], a[116:119], a[32:35], v[40:43]\n" \
  "ds_read_b64_tr_b16 a[140:141], v103 offset:0\n" \
  "v_mfma_f32_16x16x32_bf16 v[44:47], a[116:119], a[36:39], v[44:47]\n" \
  "ds_read_b64_tr_b16 a[142:143], v103 offset:4096\n" \
  "s_waitcnt lgkmcnt(8)\n" \
  "v_mfma_f32_16x16x32_bf16 v[48:51], a[120:123], a[32:35], v[48:51]\n" \
  "ds_read_b64_tr_b16 a[112:113], v96 offset:8192\n" \
  "v_mfma_f32_16x16x32_bf16 v[52:55], a[120:123], a[36:39], v[52:55]\n" \
  "ds_read_b64_tr_b16 a[114:115], v96 offset:12288\n" \
  "v_mfma_f32_16x16x32_bf16 v[56:59], a[124:127], a[32:35], v[56:59]\n" \
  "ds_read_b64_tr_b16 a[116:117], v97 offset:8192\n" \
  "v_mfma_f32_16x16x32_bf16 v[60:63], a[124:127], a[36:39], v[60:63]\n" \
  "ds_read_b64_tr_b16 a[118:119], v97 offset:12288\n" \
  "s_waitcnt lgkmcnt(8)\n" \
  "v_mfma_f32_16x16x32_bf16 v[64:67], a[128:131], a[32:35], v[64:67]\n" \
  "ds_read_b64_tr_b16 a[120:121], v98 offset:8192\n" \
  "v_mfma_f32_16x16x32_bf16 v[68:71], a[128:131], a[36:39], v[68:71]\n" \
  "ds_read_b64_tr_b16 a[122:123], v98 offset:12288\n" \
  "v_mfma_f32_16x16x32_bf16 v[72:75], a[132:135], a[32:35], v[72:75]\n" \
  "ds_read_b64_tr_b16 a[124:125], v99 offset:8192\n" \
  "v_mfma_f32_16x16x32_bf16 v[76:79], a[132:135], a[36:39], v[76:79]\n" \
  "ds_read_b64_tr_b16 a[126:127], v99 offset:12288\n" \
  "s_waitcnt lgkmcnt(8)\n" \
  "v_mfma_f32_16x16x32_bf16 v[80:83], a[136:139], a[32:35], v[80:83]\n" \
  "ds_read_b64_tr_b16 a[128:129], v100 offset:8192\n" \
  "v_mfma_f32_16x16x32_bf16 v[84:87], a[136:139], a[36:39], v[84:87]\n" \
  "ds_read_b64_tr_b16 a[130:131], v100 offset:12288\n" \
  "v_mfma_f32_16x16x32_bf16 v[88:91], a[140:143], a[32:35], v[88:91]\n" \
  "ds_read_b64_tr_b16 a[132:133], v101 offset:8192\n" \
  "v_mfma_f32_16x16x32_bf16 v[92:95], a[140:143], a[36:39], v[92:95]\n" \
  "ds_read_b64_tr_b16 a[134:135], v101 offset:12288\n" \
  "s_waitcnt vmcnt(16) lgkmcnt(8)\n" \
  "v_mfma_f32_16x16x32_bf16 v[32:35], a[112:115], a[40:43], v[32:35]\n" \
  "ds_read_b64_tr_b16 a[136:137], v102 offset:8192\n" \
  "v_mfma_f32_16x16x32_bf16 v[36:39], a[112:115], a[44:47], v[36:39]\n" \
  "ds_read_b64_tr_b16 a[138:139], v102 offset:12288\n" \
  "v_mfma_f32_16x16x32_bf16 v[40:43], a[116:119], a[40:43], v[40:43]\n" \
  "ds_read_b64_tr_b16 a[140:141], v103 offset:8192\n" \
  "v_mfma_f32_16x16x32_bf16 v[44:47], a[116:119], a[44:47], v[44:47]\n" \
  "ds_read_b64_tr_b16 a[142:143], v103 offset:12288\n" \
  "s_waitcnt lgkmcnt(8)\n" \
  "s_barrier\n" \
  "v_mfma_f32_16x16x32_bf16 v[48:51], a[120:123], a[40:43], v[48:51]\n" \
  "ds_read_b64_tr_b16 a[112:113], v96 offset:16384\n" \
  "v_mfma_f32_16x16x32_bf16 v[52:55], a[120:123], a[44:47], v[52:55]\n" \
  "ds_read_b64_tr_b16 a[114:115], v96 offset:20480\n" \
  "v_mfma_f32_16x16x32_bf16 v[56:59], a[124:127], a[40:43], v[56:59]\n" \
  "ds_read_b64_tr_b16 a[116:117], v96 offset:24576\n" \
  "v_mfma_f32_16x16x32_bf16 v[60:63], a[124:127], a[44:47], v[60:63]\n" \
  "ds_read_b64_tr_b16 a[118:119], v96 offset:28672\n" \
  "s_waitcnt lgkmcnt(8)\n" \
  "v_mfma_f32_16x16x32_bf16 v[64:67], a[128:131], a[40:43], v[64:67]\n" \
  "ds_read_b64_tr_b16 a[120:121], v96 offset:32768\n" \
  "v_mfma_f32_16x16x32_bf16 v[68:71], a[128:131], a[44:47], v[68:71]\n" \
  "ds_read_b64_tr_b16 a[122:123], v96 offset:36864\n" \
  "v_mfma_f32_16x16x32_bf16 v[72:75], a[132:135], a[40:43], v[72:75]\n" \
  "ds_read_b64_tr_b16 a[124:125], v96 offset:40960\n" \
  "v_mfma_f32_16x16x32_bf16 v[76:79], a[132:135], a[44:47], v[76:79]\n" \
  "ds_read_b64_tr_b16 a[126:127], v96 offset:45056\n" \
  "s_waitcnt lgkmcnt(8)\n" \
  "v_mfma_f32_16x16x32_bf16 v[80:83], a[136:139], a[40:43], v[80:83]\n" \
  "ds_read_b64_tr_b16 a[128:129], v96 offset:49152\n" \
  "v_mfma_f32_16x16x32_bf16 v[84:87], a[136:139], a[44:47], v[84:87]\n" \
  "ds_read_b64_tr_b16 a[130:131], v96 offset:53248\n" \
  "v_mfma_f32_16x16x32_bf16 v[88:91], a[140:143], a[40:43], v[88:91]\n" \
  "ds_read_b64_tr_b16 a[132:133], v96 offset:57344\n" \
  "v_mfma_f32_16x16x32_bf16 v[92:95], a[140:143], a[44:47], v[92:95]\n" \
  "ds_read_b64_tr_b16 a[134:135], v96 offset:61440\n" \
  "s_waitcnt vmcnt(14) lgkmcnt(8)\n" \
  "v_mfma_f32_16x16x32_bf16 v[32:35], a[112:115], a[48:51], v[32:35]\n" \
  "ds_read_b64_tr_b16 a[136:137], v104 offset:0\n" \
  "v_mfma_f32_16x16x32_bf16 v[36:39], a[112:115], a[52:55], v[36:39]\n" \
  "ds_read_b64_tr_b16 a[138:139], v104 offset:4096\n" \
  "s_waitcnt vmcnt(12)\n" \
  "v_mfma_f32_16x16x32_bf16 v[32:35], a[116:119], a[56:59], v[32:35]\n" \
  "ds_read_b64_tr_b16 a[140:141], v104 offset:8192\n" \
  "v_mfma_f32_16x16x32_bf16 v[36:39], a[116:119], a[60:63], v[36:39]\n" \
  "ds_read_b64_tr_b16 a[142:143], v104 offset:12288\n" \
  "s_waitcnt vmcnt(10) lgkmcnt(8)\n" \
  "v_mfma_f32_16x16x32_bf16 v[32:35], a[120:123], a[64:67], v[32:35]\n" \
  "ds_read_b64_tr_b16 a[112:113], v97 offset:16384\n" \
  "v_mfma_f32_16x16x32_bf16 v[36:39], a[120:123], a[68:71], v[36:39]\n" \
  "ds_read_b64_tr_b16 a[114:115], v97 offset:20480\n" \
  "s_waitcnt vmcnt(8)\n" \
  "v_mfma_f32_16x16x32_bf16 v[32:35], a[124:127], a[72:75], v[32:35]\n" \
  "ds_read_b64_tr_b16 a[116:117], v97 offset:24576\n" \
  "v_mfma_f32_16x16x32_bf16 v[36:39], a[124:127], a[76:79], v[36:39]\n" \
  "ds_read_b64_tr_b16 a[118:119], v97 offset:28672\n" \
  "s_waitcnt vmcnt(6) lgkmcnt(8)\n" \
  "v_mfma_f32_16x16x32_bf16 v[32:35], a[128:131], a[80:83], v[32:35]\n" \
  "ds_read_b64_tr_b16 a[120:121], v97 offset:32768\n" \
  "v_mfma_f32_16x16x32_bf16 v[36:39], a[128:131], a[84:87], v[36:39]\n" \
  "ds_read_b64_tr_b16 a[122:123], v97 offset:36864\n" \
  "s_waitcnt vmcnt(4)\n" \
  "v_mfma_f32_16x16x32_bf16 v[32:35], a[132:135], a[88:91], v[32:35]\n" \
  "ds_read_b64_tr_b16 a[124:125], v97 offset:40960\n" \
  "v_mfma_f32_16x16x32_bf16 v[36:39], a[132:135], a[92:95], v[36:39]\n" \
  "ds_read_b64_tr_b16 a[126:127], v97 offset:45056\n" \
  "s_waitcnt vmcnt(2) lgkmcnt(8)\n" \
  "v_mfma_f32_16x16x32_bf16 v[32:35], a[136:139], a[96:99], v[32:35]\n" \
  "ds_read_b64_tr_b16 a[128:129], v97 offset:49152\n" \
  "v_mfma_f32_16x16x32_bf16 v[36:39], a[136:139], a[100:103], v[36:39]\n" \
  "ds_read_b64_tr_b16 a[130:131], v97 offset:53248\n" \
  "s_waitcnt vmcnt(0)\n" \
  "v_mfma_f32_16x16x32_bf16 v[32:35], a[140:143], a[104:107], v[32:35]\n" \
  "ds_read_b64_tr_b16 a[132:133], v97 offset:57344\n" \
  "v_mfma_f32_16x16x32_bf16 v[36:39], a[140:143], a[108:111], v[36:39]\n" \
  "ds_read_b64_tr_b16 a[134:135], v97 offset:61440\n" \
  "s_waitcnt lgkmcnt(8)\n" \
  "v_mfma_f32_16x16x32_bf16 v[40:43], a[112:115], a[48:51], v[40:43]\n" \
  "ds_read_b64_tr_b16 a[136:137], v105 offset:0\n" \
  "v_mfma_f32_16x16x32_bf16 v[44:47], a[112:115], a[52:55], v[44:47]\n" \
  "ds_read_b64_tr_b16 a[138:139], v105 offset:4096\n" \
  "v_mfma_f32_16x16x32_bf16 v[40:43], a[116:119], a[56:59], v[40:43]\n" \
  "ds_read_b64_tr_b16 a[140:141], v105 offset:8192\n" \
  "v_mfma_f32_16x16x32_bf16 v[44:47], a[116:119], a[60:63], v[44:47]\n" \
  "ds_read_b64_tr_b16 a[142:143], v105 offset:12288\n" \
  "v_cvt_pk_bf16_f32 v32, v32, v33\n" \
  "s_waitcnt lgkmcnt(8)\n" \
  "v_mfma_f32_16x16x32_bf16 v[40:43], a[120:123], a[64:67], v[40:43]\n" \
  "ds_read_b64_tr_b16 a[112:113], v98 offset:16384\n" \
  "v_cvt_pk_bf16_f32 v33, v34, v35\n" \
  "v_mfma_f32_16x16x32_bf16 v[44:47], a[120:123], a[68:71], v[44:47]\n" \
  "ds_read_b64_tr_b16 a[114:115], v98 offset:20480\n" \
  "v_cvt_pk_bf16_f32 v34, v36, v37\n" \
  "v_mfma_f32_16x16x32_bf16 v[40:43], a[124:127], a[72:75], v[40:43]\n" \
  "ds_read_b64_tr_b16 a[116:117], v98 offset:24576\n" \
  "v_cvt_pk_bf16_f32 v35, v38, v39\n" \
  "v_mfma_f32_16x16x32_bf16 v[44:47], a[124:127], a[76:79], v[44:47]\n" \
  "ds_read_b64_tr_b16 a[118:119], v98 offset:28672\n" \
  "v_permlane16_swap_b32 v32, v34\n" \
  "s_waitcnt lgkmcnt(8)\n" \
  "v_mfma_f32_16x16x32_bf16 v[40:43], a[128:131], a[80:83], v[40:43]\n" \
  "ds_read_b64_tr_b16 a[120:121], v98 offset:32768\n" \
  "v_permlane16_swap_b32 v33, v35\n" \
  "v_mfma_f32_16x16x32_bf16 v[44:47], a[128:131], a[84:87], v[44:47]\n" \
  "ds_read_b64_tr_b16 a[122:123], v98 offset:36864\n" \
  "v_min_u32 v32, -2, v32\n" \
  "v_mfma_f32_16x16x32_bf16 v[40:43], a[132:135], a[88:91], v[40:43]\n" \
  "ds_read_b64_tr_b16 a[124:125], v98 offset:40960\n" \
  "v_min_u32 v33, -2, v33\n" \
  "v_mfma_f32_16x16x32_bf16 v[44:47], a[132:135], a[92:95], v[44:47]\n" \
  "ds_read_b64_tr_b16 a[126:127], v98 offset:45056\n" \
  "s_waitcnt lgkmcnt(8)\n" \
  "v_mfma_f32_16x16x32_bf16 v[40:43], a[136:139], a[96:99], v[40:43]\n" \
  "ds_read_b64_tr_b16 a[128:129], v98 offset:49152\n" \
  "v_min_u32 v34, -2, v34\n" \
  "v_mfma_f32_16x16x32_bf16 v[44:47], a[136:139], a[100:103], v[44:47]\n" \
  "ds_read_b64_tr_b16 a[130:131], v98 offset:53248\n" \
  "v_min_u32 v35, -2, v35\n" \
  "v_mfma_f32_16x16x32_bf16 v[40:43], a[140:143], a[104:107], v[40:43]\n" \
  "ds_read_b64_tr_b16 a[132:133], v98 offset:57344\n" \
  "buffer_store_dwordx4 v[32:35], v10, s[64:67], 0 offen offset:0 sc1\n" \
  "v_mfma_f32_16x16x32_bf16 v[44:47], a[140:143], a[108:111], v[44:47]\n" \
  "ds_read_b64_tr_b16 a[134:135], v98 offset:61440\n" \
  "s_waitcnt lgkmcnt(8)\n" \
  "v_mfma_f32_16x16x32_bf16 v[48:51], a[112:115], a[48:51], v[48:51]\n" \
  "ds_read_b64_tr_b16 a[136:137], v106 offset:0\n" \
  "v_mfma_f32_16x16x32_bf16 v[52:55], a[112:115], a[52:55], v[52:55]\n" \
  "ds_read_b64_tr_b16 a[138:139], v106 offset:4096\n" \
  "v_mfma_f32_16x16x32_bf16 v[48:51], a[116:119], a[56:59], v[48:51]\n" \
  "ds_read_b64_tr_b16 a[140:141], v106 offset:8192\n" \
  "v_mfma_f32_16x16x32_bf16 v[52:55], a[116:119], a[60:63], v[52:55]\n" \
  "ds_read_b64_tr_b16 a[142:143], v106 offset:12288\n" \
  "v_cvt_pk_bf16_f32 v40, v40, v41\n" \
  "s_waitcnt lgkmcnt(8)\n" \
  "v_mfma_f32_16x16x32_bf16 v[48:51], a[120:123], a[64:67], v[48:51]\n" \
  "ds_read_b64_tr_b16 a[112:113], v99 offset:16384\n" \
  "v_cvt_pk_bf16_f32 v41, v42, v43\n" \
  "v_mfma_f32_16x16x32_bf16 v[52:55], a[120:123], a[68:71], v[52:55]\n" \
  "ds_read_b64_tr_b16 a[114:115], v99 offset:20480\n" \
  "v_cvt_pk_bf16_f32 v42, v44, v45\n" \
  "v_mfma_f32_16x16x32_bf16 v[48:51], a[124:127], a[72:75], v[48:51]\n" \
  "ds_read_b64_tr_b16 a[116:117], v99 offset:24576\n" \
  "v_cvt_pk_bf16_f32 v43, v46, v47\n" \
  "v_mfma_f32_16x16x32_bf16 v[52:55], a[124:127], a[76:79], v[52:55]\n" \
  "ds_read_b64_tr_b16 a[118:119], v99 offset:28672\n" \
  "v_permlane16_swap_b32 v40, v42\n" \
  "s_waitcnt lgkmcnt(8)\n" \
  "v_mfma_f32_16x16x32_bf16 v[48:51], a[128:131], a[80:83], v[48:51]\n" \
  "ds_read_b64_tr_b16 a[120:121], v99 offset:32768\n" \
  "v_permlane16_swap_b32 v41, v43\n" \
  "v_mfma_f32_16x16x32_bf16 v[52:55], a[128:131], a[84:87], v[52:55]\n" \
  "ds_read_b64_tr_b16 a[122:123], v99 offset:36864\n" \
  "v_min_u32 v40, -2, v40\n" \
  "v_mfma_f32_16x16x32_bf16 v[48:51], a[132:135], a[88:91], v[48:51]\n" \
  "ds_read_b64_tr_b16 a[124:125], v99 offset:40960\n" \
  "v_min_u32 v41, -2, v41\n" \
  "v_mfma_f32_16x16x32_bf16 v[52:55], a[132:135], a[92:95], v[52:55]\n" \
  "ds_read_b64_tr_b16 a[126:127], v99 offset:45056\n" \
  "s_waitcnt lgkmcnt(8)\n" \
  "v_mfma_f32_16x16x32_bf16 v[48:51], a[136:139], a[96:99], v[48:51]\n" \
  "ds_read_b64_tr_b16 a[128:129], v99 offset:49152\n" \
  "v_min_u32 v42, -2, v42\n" \
  "v_mfma_f32_16x16x32_bf16 v[52:55], a[136:139], a[100:103], v[52:55]\n" \
  "ds_read_b64_tr_b16 a[130:131], v99 offset:53248\n" \
  "v_min_u32 v43, -2, v43\n" \
  "v_mfma_f32_16x16x32_bf16 v[48:51], a[140:143], a[104:107], v[48:51]\n" \
  "ds_read_b64_tr_b16 a[132:133], v99 offset:57344\n" \
  "buffer_store_dwordx4 v[40:43], v10, s[64:67], 0 offen offset:1024 sc1\n" \
  "v_mfma_f32_16x16x32_bf16 v[52:55], a[140:143], a[108:111], v[52:55]\n" \
  "ds_read_b64_tr_b16 a[134:135], v99 offset:61440\n" \
  "s_waitcnt lgkmcnt(8)\n" \
  "v_mfma_f32_16x16x32_bf16 v[56:59], a[112:115], a[48:51], v[56:59]\n" \
  "ds_read_b64_tr_b16 a[136:137], v107 offset:0\n" \
  "v_mfma_f32_16x16x32_bf16 v[60:63], a[112:115], a[52:55], v[60:63]\n" \
  "ds_read_b64_tr_b16 a[138:139], v107 offset:4096\n" \
  "v_mfma_f32_16x16x32_bf16 v[56:59], a[116:119], a[56:59], v[56:59]\n" \
  "ds_read_b64_tr_b16 a[140:141], v107 offset:8192\n" \
  "v_mfma_f32_16x16x32_bf16 v[60:63], a[116:119], a[60:63], v[60:63]\n" \
  "ds_read_b64_tr_b16 a[142:143], v107 offset:12288\n" \
  "v_cvt_pk_bf16_f32 v48, v48, v49\n" \
  "s_waitcnt lgkmcnt(8)\n" \
  "v_mfma_f32_16x16x32_bf16 v[56:59], a[120:123], a[64:67], v[56:59]\n" \
  "ds_read_b64_tr_b16 a[112:113], v100 offset:16384\n" \
  "v_cvt_pk_bf16_f32 v49, v50, v51\n" \
  "v_mfma_f32_16x16x32_bf16 v[60:63], a[120:123], a[68:71], v[60:63]\n" \
  "ds_read_b64_tr_b16 a[114:115], v100 offset:20480\n" \
  "v_cvt_pk_bf16_f32 v50, v52, v53\n" \
  "v_mfma_f32_16x16x32_bf16 v[56:59], a[124:127], a[72:75], v[56:59]\n" \
  "ds_read_b64_tr_b16 a[116:117], v100 offset:24576\n" \
  "v_cvt_pk_bf16_f32 v51, v54, v55\n" \
  "v_mfma_f32_16x16x32_bf16 v[60:63], a[124:127], a[76:79], v[60:63]\n" \
  "ds_read_b64_tr_b16 a[118:119], v100 offset:28672\n" \
  "v_permlane16_swap_b32 v48, v50\n" \
  "s_waitcnt lgkmcnt(8)\n" \
  "v_mfma_f32_16x16x32_bf16 v[56:59], a[128:131], a[80:83], v[56:59]\n" \
  "ds_read_b64_tr_b16 a[120:121], v100 offset:32768\n" \
  "v_permlane16_swap_b32 v49, v51\n" \
  "v_mfma_f32_16x16x32_bf16 v[60:63], a[128:131], a[84:87], v[60:63]\n" \
  "ds_read_b64_tr_b16 a[122:123], v100 offset:36864\n" \
  "v_min_u32 v48, -2, v48\n" \
  "v_mfma_f32_16x16x32_bf16 v[56:59], a[132:135], a[88:91], v[56:59]\n" \
  "ds_read_b64_tr_b16 a[124:125], v100 offset:40960\n" \
  "v_min_u32 v49, -2, v49\n" \
  "v_mfma_f32_16x16x32_bf16 v[60:63], a[132:135], a[92:95], v[60:63]\n" \
  "ds_read_b64_tr_b16 a[126:127], v100 offset:45056\n" \
  "s_waitcnt lgkmcnt(8)\n" \
  "v_mfma_f32_16x16x32_bf16 v[56:59], a[136:139], a[96:99], v[56:59]\n" \
  "ds_read_b64_tr_b16 a[128:129], v100 offset:49152\n" \
  "v_min_u32 v50, -2, v50\n" \
  "v_mfma_f32_16x16x32_bf16 v[60:63], a[136:139], a[100:103], v[60:63]\n" \
  "ds_read_b64_tr_b16 a[130:131], v100 offset:53248\n" \
  "v_min_u32 v51, -2, v51\n" \
  "v_mfma_f32_16x16x32_bf16 v[56:59], a[140:143], a[104:107], v[56:59]\n" \
  "ds_read_b64_tr_b16 a[132:133], v100 offset:57344\n" \
  "buffer_store_dwordx4 v[48:51], v10, s[64:67], 0 offen offset:2048 sc1\n" \
  "v_mfma_f32_16x16x32_bf16 v[60:63], a[140:143], a[108:111], v[60:63]\n" \
  "ds_read_b64_tr_b16 a[134:135], v100 offset:61440\n" \
  "s_waitcnt lgkmcnt(8)\n" \
  "v_mfma_f32_16x16x32_bf16 v[64:67], a[112:115], a[48:51], v[64:67]\n" \
  "ds_read_b64_tr_b16 a[136:137], v108 offset:0\n" \
  "v_mfma_f32_16x16x32_bf16 v[68:71], a[112:115], a[52:55], v[68:71]\n" \
  "ds_read_b64_tr_b16 a[138:139], v108 offset:4096\n" \
  "v_mfma_f32_16x16x32_bf16 v[64:67], a[116:119], a[56:59], v[64:67]\n" \
  "ds_read_b64_tr_b16 a[140:141], v108 offset:8192\n" \
  "v_mfma_f32_16x16x32_bf16 v[68:71], a[116:119], a[60:63], v[68:71]\n" \
  "ds_read_b64_tr_b16 a[142:143], v108 offset:12288\n" \
  "v_cvt_pk_bf16_f32 v56, v56, v57\n" \
  "s_waitcnt lgkmcnt(8)\n" \
  "v_mfma_f32_16x16x32_bf16 v[64:67], a[120:123], a[64:67], v[64:67]\n" \
  "ds_read_b64_tr_b16 a[112:113], v101 offset:16384\n" \
  "v_cvt_pk_bf16_f32 v57, v58, v59\n" \
  "v_mfma_f32_16x16x32_bf16 v[68:71], a[120:123], a[68:71], v[68:71]\n" \
  "ds_read_b64_tr_b16 a[114:115], v101 offset:20480\n" \
  "v_cvt_pk_bf16_f32 v58, v60, v61\n" \
  "v_mfma_f32_16x16x32_bf16 v[64:67], a[124:127], a[72:75], v[64:67]\n" \
  "ds_read_b64_tr_b16 a[116:117], v101 offset:24576\n" \
  "v_cvt_pk_bf16_f32 v59, v62, v63\n" \
  "v_mfma_f32_16x16x32_bf16 v[68:71], a[124:127], a[76:79], v[68:71]\n" \
  "ds_read_b64_tr_b16 a[118:119], v101 offset:28672\n" \
  "v_permlane16_swap_b32 v56, v58\n" \
  "s_waitcnt lgkmcnt(8)\n" \
  "v_mfma_f32_16x16x32_bf16 v[64:67], a[128:131], a[80:83], v[64:67]\n" \
  "ds_read_b64_tr_b16 a[120:121], v101 offset:32768\n" \
  "v_permlane16_swap_b32 v57, v59\n" \
  "v_mfma_f32_16x16x32_bf16 v[68:71], a[128:131], a[84:87], v[68:71]\n" \
  "ds_read_b64_tr_b16 a[122:123], v101 offset:36864\n" \
  "v_min_u32 v56, -2, v56\n" \
  "v_mfma_f32_16x16x32_bf16 v[64:67], a[132:135], a[88:91], v[64:67]\n" \
  "ds_read_b64_tr_b16 a[124:125], v101 offset:40960\n" \
  "v_min_u32 v57, -2, v57\n" \
  "v_mfma_f32_16x16x32_bf16 v[68:71], a[132:135], a[92:95], v[68:71]\n" \
  "ds_read_b64_tr_b16 a[126:127], v101 offset:45056\n" \
  "s_waitcnt lgkmcnt(8)\n" \
  "v_mfma_f32_16x16x32_bf16 v[64:67], a[136:139], a[96:99], v[64:67]\n" \
  "ds_read_b64_tr_b16 a[128:129], v101 offset:49152\n" \
  "v_min_u32 v58, -2, v58\n" \
  "v_mfma_f32_16x16x32_bf16 v[68:71], a[136:139], a[100:103], v[68:71]\n" \
  "ds_read_b64_tr_b16 a[130:131], v101 offset:53248\n" \
  "v_min_u32 v59, -2, v59\n" \
  "v_mfma_f32_16x16x32_bf16 v[64:67], a[140:143], a[104:107], v[64:67]\n" \
  "ds_read_b64_tr_b16 a[132:133], v101 offset:57344\n" \
  "buffer_store_dwordx4 v[56:59], v10, s[64:67], 0 offen offset:3072 sc1\n" \
  "v_mfma_f32_16x16x32_bf16 v[68:71], a[140:143], a[108:111], v[68:71]\n" \
  "ds_read_b64_tr_b16 a[134:135], v101 offset:61440\n" \
  "s_waitcnt lgkmcnt(8)\n" \
  "v_mfma_f32_16x16x32_bf16 v[72:75], a[112:115], a[48:51], v[72:75]\n" \
  "ds_read_b64_tr_b16 a[136:137], v109 offset:0\n" \
  "v_mfma_f32_16x16x32_bf16 v[76:79], a[112:115], a[52:55], v[76:79]\n" \
  "ds_read_b64_tr_b16 a[138:139], v109 offset:4096\n" \
  "v_mfma_f32_16x16x32_bf16 v[72:75], a[116:119], a[56:59], v[72:75]\n" \
  "ds_read_b64_tr_b16 a[140:141], v109 offset:8192\n" \
  "v_mfma_f32_16x16x32_bf16 v[76:79], a[116:119], a[60:63], v[76:79]\n" \
  "ds_read_b64_tr_b16 a[142:143], v109 offset:12288\n" \
  "v_cvt_pk_bf16_f32 v64, v64, v65\n" \
  "s_waitcnt lgkmcnt(8)\n" \
  "v_mfma_f32_16x16x32_bf16 v[72:75], a[120:123], a[64:67], v[72:75]\n" \
  "ds_read_b64_tr_b16 a[112:113], v102 offset:16384\n" \
  "v_cvt_pk_bf16_f32 v65, v66, v67\n" \
  "v_mfma_f32_16x16x32_bf16 v[76:79], a[120:123], a[68:71], v[76:79]\n" \
  "ds_read_b64_tr_b16 a[114:115], v102 offset:20480\n" \
  "v_cvt_pk_bf16_f32 v66, v68, v69\n" \
  "v_mfma_f32_16x16x32_bf16 v[72:75], a[124:127], a[72:75], v[72:75]\n" \
  "ds_read_b64_tr_b16 a[116:117], v102 offset:24576\n" \
  "v_cvt_pk_bf16_f32 v67, v70, v71\n" \
  "v_mfma_f32_16x16x32_bf16 v[76:79], a[124:127], a[76:79], v[76:79]\n" \
  "ds_read_b64_tr_b16 a[118:119], v102 offset:28672\n" \
  "v_permlane16_swap_b32 v64, v66\n" \
  "s_waitcnt lgkmcnt(8)\n" \
  "v_mfma_f32_16x16x32_bf16 v[72:75], a[128:131], a[80:83], v[72:75]\n" \
  "ds_read_b64_tr_b16 a[120:121], v102 offset:32768\n" \
  "v_permlane16_swap_b32 v65, v67\n" \
  "v_mfma_f32_16x16x32_bf16 v[76:79], a[128:131], a[84:87], v[76:79]\n" \
  "ds_read_b64_tr_b16 a[122:123], v102 offset:36864\n" \
  "v_min_u32 v64, -2, v64\n" \
  "v_mfma_f32_16x16x32_bf16 v[72:75], a[132:135], a[88:91], v[72:75]\n" \
  "ds_read_b64_tr_b16 a[124:125], v102 offset:40960\n" \
  "v_min_u32 v65, -2, v65\n" \
  "v_mfma_f32_16x16x32_bf16 v[76:79], a[132:135], a[92:95], v[76:79]\n" \
  "ds_read_b64_tr_b16 a[126:127], v102 offset:45056\n" \
  "s_waitcnt lgkmcnt(8)\n" \
  "v_mfma_f32_16x16x32_bf16 v[72:75], a[136:139], a[96:99], v[72:75]\n" \
  "ds_read_b64_tr_b16 a[128:129], v102 offset:49152\n" \
  "v_min_u32 v66, -2, v66\n" \
  "v_mfma_f32_16x16x32_bf16 v[76:79], a[136:139], a[100:103], v[76:79]\n" \
  "ds_read_b64_tr_b16 a[130:131], v102 offset:53248\n" \
  "v_min_u32 v67, -2, v67\n" \
  "v_mfma_f32_16x16x32_bf16 v[72:75], a[140:143], a[104:107], v[72:75]\n" \
  "ds_read_b64_tr_b16 a[132:133], v102 offset:57344\n" \
  "buffer_store_dwordx4 v[64:67], v10, s[64:67], s16 offen offset:0 sc1\n" \
  "v_mfma_f32_16x16x32_bf16 v[76:79], a[140:143], a[108:111], v[76:79]\n" \
  "ds_read_b64_tr_b16 a[134:135], v102 offset:61440\n" \
  "s_waitcnt lgkmcnt(8)\n" \
  "v_mfma_f32_16x16x32_bf16 v[80:83], a[112:115], a[48:51], v[80:83]\n" \
  "ds_read_b64_tr_b16 a[136:137], v110 offset:0\n" \
  "v_mfma_f32_16x16x32_bf16 v[84:87], a[112:115], a[52:55], v[84:87]\n" \
  "ds_read_b64_tr_b16 a[138:139], v110 offset:4096\n" \
  "v_mfma_f32_16x16x32_bf16 v[80:83], a[116:119], a[56:59], v[80:83]\n" \
  "ds_read_b64_tr_b16 a[140:141], v110 offset:8192\n" \
  "v_mfma_f32_16x16x32_bf16 v[84:87], a[116:119], a[60:63], v[84:87]\n" \
  "ds_read_b64_tr_b16 a[142:143], v110 offset:12288\n" \
  "v_cvt_pk_bf16_f32 v72, v72, v73\n" \
  "s_waitcnt lgkmcnt(8)\n" \
  "v_mfma_f32_16x16x32_bf16 v[80:83], a[120:123], a[64:67], v[80:83]\n" \
  "ds_read_b64_tr_b16 a[112:113], v103 offset:16384\n" \
  "v_cvt_pk_bf16_f32 v73, v74, v75\n" \
  "v_mfma_f32_16x16x32_bf16 v[84:87], a[120:123], a[68:71], v[84:87]\n" \
  "ds_read_b64_tr_b16 a[114:115], v103 offset:20480\n" \
  "v_cvt_pk_bf16_f32 v74, v76, v77\n" \
  "v_mfma_f32_16x16x32_bf16 v[80:83], a[124:127], a[72:75], v[80:83]\n" \
  "ds_read_b64_tr_b16 a[116:117], v103 offset:24576\n" \
  "v_cvt_pk_bf16_f32 v75, v78, v79\n" \
  "v_mfma_f32_16x16x32_bf16 v[84:87], a[124:127], a[76:79], v[84:87]\n" \
  "ds_read_b64_tr_b16 a[118:119], v103 offset:28672\n" \
  "v_permlane16_swap_b32 v72, v74\n" \
  "s_waitcnt lgkmcnt(8)\n" \
  "v_mfma_f32_16x16x32_bf16 v[80:83], a[128:131], a[80:83], v[80:83]\n" \
  "ds_read_b64_tr_b16 a[120:121], v103 offset:32768\n" \
  "v_permlane16_swap_b32 v73, v75\n" \
  "v_mfma_f32_16x16x32_bf16 v[84:87], a[128:131], a[84:87], v[84:87]\n" \
  "ds_read_b64_tr_b16 a[122:123], v103 offset:36864\n" \
  "v_min_u32 v72, -2, v72\n" \
  "v_mfma_f32_16x16x32_bf16 v[80:83], a[132:135], a[88:91], v[80:83]\n" \
  "ds_read_b64_tr_b16 a[124:125], v103 offset:40960\n" \
  "v_min_u32 v73, -2, v73\n" \
  "v_mfma_f32_16x16x32_bf16 v[84:87], a[132:135], a[92:95], v[84:87]\n" \
  "ds_read_b64_tr_b16 a[126:127], v103 offset:45056\n" \
  "s_waitcnt lgkmcnt(8)\n" \
  "v_mfma_f32_16x16x32_bf16 v[80:83], a[136:139], a[96:99], v[80:83]\n" \
  "ds_read_b64_tr_b16 a[128:129], v103 offset:49152\n" \
  "v_min_u32 v74, -2, v74\n" \
  "v_mfma_f32_16x16x32_bf16 v[84:87], a[136:139], a[100:103], v[84:87]\n" \
  "ds_read_b64_tr_b16 a[130:131], v103 offset:53248\n" \
  "v_min_u32 v75, -2, v75\n" \
  "v_mfma_f32_16x16x32_bf16 v[80:83], a[140:143], a[104:107], v[80:83]\n" \
  "ds_read_b64_tr_b16 a[132:133], v103 offset:57344\n" \
  "buffer_store_dwordx4 v[72:75], v10, s[64:67], s16 offen offset:1024 sc1\n" \
  "v_mfma_f32_16x16x32_bf16 v[84:87], a[140:143], a[108:111], v[84:87]\n" \
  "ds_read_b64_tr_b16 a[134:135], v103 offset:61440\n" \
  "s_waitcnt lgkmcnt(8)\n" \
  "v_mfma_f32_16x16x32_bf16 v[88:91], a[112:115], a[48:51], v[88:91]\n" \
  "ds_read_b64_tr_b16 a[136:137], v111 offset:0\n" \
  "v_mfma_f32_16x16x32_bf16 v[92:95], a[112:115], a[52:55], v[92:95]\n" \
  "ds_read_b64_tr_b16 a[138:139], v111 offset:4096\n" \
  "v_mfma_f32_16x16x32_bf16 v[88:91], a[116:119], a[56:59], v[88:91]\n" \
  "ds_read_b64_tr_b16 a[140:141], v111 offset:8192\n" \
  "v_mfma_f32_16x16x32_bf16 v[92:95], a[116:119], a[60:63], v[92:95]\n" \
  "ds_read_b64_tr_b16 a[142:143], v111 offset:12288\n" \
  "v_cvt_pk_bf16_f32 v80, v80, v81\n" \
  "s_waitcnt lgkmcnt(8)\n" \
  "v_mfma_f32_16x16x32_bf16 v[88:91], a[120:123], a[64:67], v[88:91]\n" \
  "v_cvt_pk_bf16_f32 v81, v82, v83\n" \
  "v_mfma_f32_16x16x32_bf16 v[92:95], a[120:123], a[68:71], v[92:95]\n" \
  "v_cvt_pk_bf16_f32 v82, v84, v85\n" \
  "v_mfma_f32_16x16x32_bf16 v[88:91], a[124:127], a[72:75], v[88:91]\n" \
  "v_cvt_pk_bf16_f32 v83, v86, v87\n" \
  "v_mfma_f32_16x16x32_bf16 v[92:95], a[124:127], a[76:79], v[92:95]\n" \
  "v_permlane16_swap_b32 v80, v82\n" \
  "s_waitcnt lgkmcnt(4)\n" \
  "v_mfma_f32_16x16x32_bf16 v[88:91], a[128:131], a[80:83], v[88:91]\n" \
  "v_permlane16_swap_b32 v81, v83\n" \
  "v_mfma_f32_16x16x32_bf16 v[92:95], a[128:131], a[84:87], v[92:95]\n" \
  "v_min_u32 v80, -2, v80\n" \
  "v_mfma_f32_16x16x32_bf16 v[88:91], a[132:135], a[88:91], v[88:91]\n" \
  "v_min_u32 v81, -2, v81\n" \
  "v_mfma_f32_16x16x32_bf16 v[92:95], a[132:135], a[92:95], v[92:95]\n" \
  "s_waitcnt lgkmcnt(0)\n" \
  "v_mfma_f32_16x16x32_bf16 v[88:91], a[136:139], a[96:99], v[88:91]\n" \
  "v_min_u32 v82, -2, v82\n" \
  "v_mfma_f32_16x16x32_bf16 v[92:95], a[136:139], a[100:103], v[92:95]\n" \
  "v_min_u32 v83, -2, v83\n" \
  "v_mfma_f32_16x16x32_bf16 v[88:91], a[140:143], a[104:107], v[88:91]\n" \
  "buffer_store_dwordx4 v[80:83], v10, s[64:67], s16 offen offset:2048 sc1\n" \
  "v_mfma_f32_16x16x32_bf16 v[92:95], a[140:143], a[108:111], v[92:95]\n" \
  "s_nop 7\n" \
  "v_cvt_pk_bf16_f32 v88, v88, v89\n" \
  "v_cvt_pk_bf16_f32 v89, v90, v91\n" \
  "v_cvt_pk_bf16_f32 v90, v92, v93\n" \
  "v_cvt_pk_bf16_f32 v91, v94, v95\n" \
  "s_nop 0\n" \
  "v_permlane16_swap_b32 v88, v90\n" \
  "v_permlane16_swap_b32 v89, v91\n" \
  "v_min_u32 v88, -2, v88\n" \
  "v_min_u32 v89, -2, v89\n" \
  "v_min_u32 v90, -2, v90\n" \
  "v_min_u32 v91, -2, v91\n" \
  "buffer_store_dwordx4 v[88:91], v10, s[64:67], s16 offen offset:3072 sc1\n" \
  "buffer_load_dwordx4 v[96:99], v12, s[48:51], 0 offen offset:0 sc1\n" \
  "buffer_load_dwordx4 v[112:115], v12, s[52:55], 0 offen offset:0 sc1\n" \
  "buffer_load_dwordx4 v[100:103], v12, s[48:51], 0 offen offset:2048 sc1\n" \
  "buffer_load_dwordx4 v[116:119], v12, s[52:55], 0 offen offset:2048 sc1\n" \
  "v_accvgpr_read_b32 v128, a224\n" \
  "v_accvgpr_read_b32 v129, a225\n" \
  "v_accvgpr_read_b32 v130, a226\n" \
  "v_accvgpr_read_b32 v131, a227\n" \
  "v_accvgpr_read_b32 v132, a228\n" \
  "v_accvgpr_read_b32 v133, a229\n" \
  "v_accvgpr_read_b32 v134, a230\n" \
  "v_accvgpr_read_b32 v135, a231\n" \
  "v_accvgpr_read_b32 v136, a232\n" \
  "v_accvgpr_read_b32 v137, a233\n" \
  "v_accvgpr_read_b32 v138, a234\n" \
  "v_accvgpr_read_b32 v139, a235\n" \
  "v_accvgpr_read_b32 v140, a236\n" \
  "v_accvgpr_read_b32 v141, a237\n" \
  "v_accvgpr_read_b32 v142, a238\n" \
  "v_accvgpr_read_b32 v143, a239\n" \
  "v_mbcnt_lo_u32_b32 v178, -1, 0\n" \
  "v_mbcnt_hi_u32_b32 v178, -1, v178\n" \
  "v_and_b32 v177, 7, v178\n" \
  "s_lshl_b32 s19, s83, 3\n" \
  "v_lshlrev_b32 v177, 5, v177\n" \
  "v_add_u32 v177, 114688, v177\n" \
  "v_add_u32 v176, s19, v177\n" \
  "v_lshlrev_b32 v32, 16, v128\n" \
  "v_and_b32 v33, 0xffff0000, v128\n" \
  "v_lshlrev_b32 v34, 16, v129\n" \
  "v_and_b32 v35, 0xffff0000, v129\n" \
  "v_lshlrev_b32 v36, 16, v130\n" \
  "v_and_b32 v37, 0xffff0000, v130\n" \
  "v_lshlrev_b32 v38, 16, v131\n" \
  "v_and_b32 v39, 0xffff0000, v131\n" \
  "v_lshlrev_b32 v40, 16, v132\n" \
  "v_and_b32 v41, 0xffff0000, v132\n" \
  "v_lshlrev_b32 v42, 16, v133\n" \
  "v_and_b32 v43, 0xffff0000, v133\n" \
  "v_lshlrev_b32 v44, 16, v134\n" \
  "v_and_b32 v45, 0xffff0000, v134\n" \
  "v_lshlrev_b32 v46, 16, v135\n" \
  "v_and_b32 v47, 0xffff0000, v135\n" \
  "v_lshlrev_b32 v48, 16, v136\n" \
  "v_and_b32 v49, 0xffff0000, v136\n" \
  "v_lshlrev_b32 v50, 16, v137\n" \
  "v_and_b32 v51, 0xffff0000, v137\n" \
  "v_lshlrev_b32 v52, 16, v138\n" \
  "v_and_b32 v53, 0xffff0000, v138\n" \
  "v_lshlrev_b32 v54, 16, v139\n" \
  "v_and_b32 v55, 0xffff0000, v139\n" \
  "v_lshlrev_b32 v56, 16, v140\n" \
  "v_and_b32 v57, 0xffff0000, v140\n" \
  "v_lshlrev_b32 v58, 16, v141\n" \
  "v_and_b32 v59, 0xffff0000, v141\n" \
  "v_lshlrev_b32 v60, 16, v142\n" \
  "v_and_b32 v61, 0xffff0000, v142\n" \
  "v_lshlrev_b32 v62, 16, v143\n" \
  "v_and_b32 v63, 0xffff0000, v143\n" \
  "buffer_load_dwordx4 v[104:107], v12, s[48:51], s16 offen offset:0 sc1\n" \
  "buffer_load_dwordx4 v[120:123], v12, s[52:55], s16 offen offset:0 sc1\n" \
  "buffer_load_dwordx4 v[108:111], v12, s[48:51], s16 offen offset:2048 sc1\n" \
  "buffer_load_dwordx4 v[124:127], v12, s[52:55], s16 offen offset:2048 sc1\n" \
  "s_cmp_lg_u32 s18, 0\n" \
  "s_cbranch_scc1 20f\n" \
  "v_accvgpr_read_b32 v0, a152\n" \
  "v_accvgpr_read_b32 v1, a153\n" \
  "v_accvgpr_read_b32 v2, a154\n" \
  "v_accvgpr_read_b32 v3, a155\n" \
  "v_accvgpr_read_b32 v4, a156\n" \
  "v_accvgpr_read_b32 v5, a157\n" \
  "v_accvgpr_read_b32 v6, a158\n" \
  "v_accvgpr_read_b32 v144, a240\n" \
  "v_accvgpr_read_b32 v145, a241\n" \
  "v_accvgpr_read_b32 v146, a242\n" \
  "v_accvgpr_read_b32 v147, a243\n" \
  "v_accvgpr_read_b32 v148, a244\n" \
  "v_accvgpr_read_b32 v149, a245\n" \
  "v_accvgpr_read_b32 v150, a246\n" \
  "v_accvgpr_read_b32 v151, a247\n" \
  "v_accvgpr_read_b32 v152, a248\n" \
  "v_accvgpr_read_b32 v153, a249\n" \
  "v_accvgpr_read_b32 v154, a250\n" \
  "v_accvgpr_read_b32 v155, a251\n" \
  "v_accvgpr_read_b32 v156, a252\n" \
  "v_accvgpr_read_b32 v157, a253\n" \
  "v_accvgpr_read_b32 v158, a254\n" \
  "v_accvgpr_read_b32 v159, a255\n" \
  "v_accvgpr_read_b32 v16, a159\n" \
  "v_lshlrev_b32 v64, 16, v144\n" \
  "v_and_b32 v65, 0xffff0000, v144\n" \
  "v_lshlrev_b32 v66, 16, v145\n" \
  "v_and_b32 v67, 0xffff0000, v145\n" \
  "v_lshlrev_b32 v68, 16, v146\n" \
  "v_and_b32 v69, 0xffff0000, v146\n" \
  "v_lshlrev_b32 v70, 16, v147\n" \
  "v_and_b32 v71, 0xffff0000, v147\n" \
  "v_lshlrev_b32 v72, 16, v148\n" \
  "v_and_b32 v73, 0xffff0000, v148\n" \
  "v_lshlrev_b32 v74, 16, v149\n" \
  "v_and_b32 v75, 0xffff0000, v149\n" \
  "v_lshlrev_b32 v76, 16, v150\n" \
  "v_and_b32 v77, 0xffff0000, v150\n" \
  "v_lshlrev_b32 v78, 16, v151\n" \
  "v_and_b32 v79, 0xffff0000, v151\n" \
  "v_lshlrev_b32 v80, 16, v152\n" \
  "v_and_b32 v81, 0xffff0000, v152\n" \
  "v_lshlrev_b32 v82, 16, v153\n" \
  "v_and_b32 v83, 0xffff0000, v153\n" \
  "v_lshlrev_b32 v84, 16, v154\n" \
  "v_and_b32 v85, 0xffff0000, v154\n" \
  "v_lshlrev_b32 v86, 16, v155\n" \
  "v_and_b32 v87, 0xffff0000, v155\n" \
  "v_lshlrev_b32 v88, 16, v156\n" \
  "v_and_b32 v89, 0xffff0000, v156\n" \
  "v_lshlrev_b32 v90, 16, v157\n" \
  "v_and_b32 v91, 0xffff0000, v157\n" \
  "v_lshlrev_b32 v92, 16, v158\n" \
  "v_and_b32 v93, 0xffff0000, v158\n" \
  "v_lshlrev_b32 v94, 16, v159\n" \
  "v_and_b32 v95, 0xffff0000, v159\n" \
  "s_waitcnt vmcnt(6)\n" \
  "v_max3_u32 v188, v96, v97, v98\n" \
  "v_max3_u32 v189, v99, v112, v113\n" \
  "v_max3_u32 v188, v188, v114, v115\n" \
  "v_max_u32 v188, v188, v189\n" \
  "v_cmp_eq_u32 vcc, -1, v188\n" \
  "s_cbranch_vccz 11f\n" \
  "s_mov_b32 s15, 0x100000\n" \
  "10:\n" \
  "buffer_load_dwordx4 v[96:99], v12, s[48:51], 0 offen offset:0 sc1\n" \
  "buffer_load_dwordx4 v[112:115], v12, s[52:55], 0 offen offset:0 sc1\n" \
  "buffer_load_dwordx4 v[100:103], v12, s[48:51], 0 offen offset:2048 sc1\n" \
  "buffer_load_dwordx4 v[116:119], v12, s[52:55], 0 offen offset:2048 sc1\n" \
  "buffer_load_dwordx4 v[104:107], v12, s[48:51], s16 offen offset:0 sc1\n" \
  "buffer_load_dwordx4 v[120:123], v12, s[52:55], s16 offen offset:0 sc1\n" \
  "buffer_load_dwordx4 v[108:111], v12, s[48:51], s16 offen offset:2048 sc1\n" \
  "buffer_load_dwordx4 v[124:127], v12, s[52:55], s16 offen offset:2048 sc1\n" \
  "s_waitcnt vmcnt(0)\n" \
  "v_max3_u32 v188, v96, v97, v98\n" \
  "v_max3_u32 v189, v99, v112, v113\n" \
  "v_max3_u32 v188, v188, v114, v115\n" \
  "v_max_u32 v188, v188, v189\n" \
  "v_cmp_eq_u32 vcc, -1, v188\n" \
  "s_cbranch_vccz 11f\n" \
  "s_sub_u32 s15, s15, 1\n" \
  "s_cmp_lg_u32 s15, 0\n" \
  "s_cbranch_scc1 10b\n" \
  "11:\n" \
  "v_lshlrev_b32 v190, 16, v96\n" \
  "v_and_b32 v191, 0xffff0000, v96\n" \
  "v_lshlrev_b32 v192, 16, v112\n" \
  "v_and_b32 v193, 0xffff0000, v112\n" \
  "v_pk_add_f32 v[190:191], v[190:191], v[192:193]\n" \
  "v_pk_add_f32 v[32:33], v[32:33], v[190:191]\n" \
  "v_lshlrev_b32 v190, 16, v97\n" \
  "v_and_b32 v191, 0xffff0000, v97\n" \
  "v_lshlrev_b32 v192, 16, v113\n" \
  "v_and_b32 v193, 0xffff0000, v113\n" \
  "v_pk_add_f32 v[190:191], v[190:191], v[192:193]\n" \
  "v_pk_add_f32 v[34:35], v[34:35], v[190:191]\n" \
  "v_lshlrev_b32 v190, 16, v98\n" \
  "v_and_b32 v191, 0xffff0000, v98\n" \
  "v_lshlrev_b32 v192, 16, v114\n" \
  "v_and_b32 v193, 0xffff0000, v114\n" \
  "v_pk_add_f32 v[190:191], v[190:191], v[192:193]\n" \
  "v_pk_add_f32 v[36:37], v[36:37], v[190:191]\n" \
  "v_lshlrev_b32 v190, 16, v99\n" \
  "v_and_b32 v191, 0xffff0000, v99\n" \
  "v_lshlrev_b32 v192, 16, v115\n" \
  "v_and_b32 v193, 0xffff0000, v115\n" \
  "v_pk_add_f32 v[190:191], v[190:191], v[192:193]\n" \
  "v_pk_add_f32 v[38:39], v[38:39], v[190:191]\n" \
  "v_cvt_pk_bf16_f32 v128, v32, v33\n" \
  "v_cvt_pk_bf16_f32 v129, v34, v35\n" \
  "v_cvt_pk_bf16_f32 v130, v36, v37\n" \
  "v_cvt_pk_bf16_f32 v131, v38, v39\n" \
  "v_lshlrev_b32 v32, 16, v128\n" \
  "v_and_b32 v33, 0xffff0000, v128\n" \
  "v_lshlrev_b32 v34, 16, v129\n" \
  "v_and_b32 v35, 0xffff0000, v129\n" \
  "v_lshlrev_b32 v36, 16, v130\n" \
  "v_and_b32 v37, 0xffff0000, v130\n" \
  "v_lshlrev_b32 v38, 16, v131\n" \
  "v_and_b32 v39, 0xffff0000, v131\n" \
  "v_pk_add_f32 v[32:33], v[32:33], v[16:17] op_sel_hi:[1,0] neg_lo:[0,1] neg_hi:[0,1]\n" \
  "v_pk_add_f32 v[34:35], v[34:35], v[16:17] op_sel_hi:[1,0] neg_lo:[0,1] neg_hi:[0,1]\n" \
  "v_pk_add_f32 v[36:37], v[36:37], v[16:17] op_sel_hi:[1,0] neg_lo:[0,1] neg_hi:[0,1]\n" \
  "v_pk_add_f32 v[38:39], v[38:39], v[16:17] op_sel_hi:[1,0] neg_lo:[0,1] neg_hi:[0,1]\n" \
  "v_pk_mul_f32 v[20:21], v[32:33], v[32:33]\n" \
  "v_pk_add_f32 v[18:19], v[32:33], 0 op_sel_hi:[1,0]\n" \
  "v_pk_fma_f32 v[20:21], v[34:35], v[34:35], v[20:21]\n" \
  "v_pk_add_f32 v[18:19], v[18:19], v[34:35]\n" \
  "v_pk_fma_f32 v[20:21], v[36:37], v[36:37], v[20:21]\n" \
  "v_pk_add_f32 v[18:19], v[18:19], v[36:37]\n" \
  "v_pk_fma_f32 v[20:21], v[38:39], v[38:39], v[20:21]\n" \
  "v_pk_add_f32 v[18:19], v[18:19], v[38:39]\n" \
  "s_waitcnt vmcnt(4)\n" \
  "v_max3_u32 v188, v100, v101, v102\n" \
  "v_max3_u32 v189, v103, v116, v117\n" \
  "v_max3_u32 v188, v188, v118, v119\n" \
  "v_max_u32 v188, v188, v189\n" \
  "v_cmp_eq_u32 vcc, -1, v188\n" \
  "s_cbranch_vccz 13f\n" \
  "s_mov_b32 s15, 0x100000\n" \
  "12:\n" \
  "buffer_load_dwordx4 v[100:103], v12, s[48:51], 0 offen offset:2048 sc1\n" \
  "buffer_load_dwordx4 v[116:119], v12, s[52:55], 0 offen offset:2048 sc1\n" \
  "buffer_load_dwordx4 v[104:107], v12, s[48:51], s16 offen offset:0 sc1\n" \
  "buffer_load_dwordx4 v[120:123], v12, s[52:55], s16 offen offset:0 sc1\n" \
  "buffer_load_dwordx4 v[108:111], v12, s[48:51], s16 offen offset:2048 sc1\n" \
  "buffer_load_dwordx4 v[124:127], v12, s[52:55], s16 offen offset:2048 sc1\n" \
  "s_waitcnt vmcnt(0)\n" \
  "v_max3_u32 v188, v100, v101, v102\n" \
  "v_max3_u32 v189, v103, v116, v117\n" \
  "v_max3_u32 v188, v188, v118, v119\n" \
  "v_max_u32 v188, v188, v189\n" \
  "v_cmp_eq_u32 vcc, -1, v188\n" \
  "s_cbranch_vccz 13f\n" \
  "s_sub_u32 s15, s15, 1\n" \
  "s_cmp_lg_u32 s15, 0\n" \
  "s_cbranch_scc1 12b\n" \
  "13:\n" \
  "v_lshlrev_b32 v190, 16, v100\n" \
  "v_and_b32 v191, 0xffff0000, v100\n" \
  "v_lshlrev_b32 v192, 16, v116\n" \
  "v_and_b32 v193, 0xffff0000, v116\n" \
  "v_pk_add_f32 v[190:191], v[190:191], v[192:193]\n" \
  "v_pk_add_f32 v[40:41], v[40:41], v[190:191]\n" \
  "v_lshlrev_b32 v190, 16, v101\n" \
  "v_and_b32 v191, 0xffff0000, v101\n" \
  "v_lshlrev_b32 v192, 16, v117\n" \
  "v_and_b32 v193, 0xffff0000, v117\n" \
  "v_pk_add_f32 v[190:191], v[190:191], v[192:193]\n" \
  "v_pk_add_f32 v[42:43], v[42:43], v[190:191]\n" \
  "v_lshlrev_b32 v190, 16, v102\n" \
  "v_and_b32 v191, 0xffff0000, v102\n" \
  "v_lshlrev_b32 v192, 16, v118\n" \
  "v_and_b32 v193, 0xffff0000, v118\n" \
  "v_pk_add_f32 v[190:191], v[190:191], v[192:193]\n" \
  "v_pk_add_f32 v[44:45], v[44:45], v[190:191]\n" \
  "v_lshlrev_b32 v190, 16, v103\n" \
  "v_and_b32 v191, 0xffff0000, v103\n" \
  "v_lshlrev_b32 v192, 16, v119\n" \
  "v_and_b32 v193, 0xffff0000, v119\n" \
  "v_pk_add_f32 v[190:191], v[190:191], v[192:193]\n" \
  "v_pk_add_f32 v[46:47], v[46:47], v[190:191]\n" \
  "v_cvt_pk_bf16_f32 v132, v40, v41\n" \
  "v_cvt_pk_bf16_f32 v133, v42, v43\n" \
  "v_cvt_pk_bf16_f32 v134, v44, v45\n" \
  "v_cvt_pk_bf16_f32 v135, v46, v47\n" \
  "v_lshlrev_b32 v40, 16, v132\n" \
  "v_and_b32 v41, 0xffff0000, v132\n" \
  "v_lshlrev_b32 v42, 16, v133\n" \
  "v_and_b32 v43, 0xffff0000, v133\n" \
  "v_lshlrev_b32 v44, 16, v134\n" \
  "v_and_b32 v45, 0xffff0000, v134\n" \
  "v_lshlrev_b32 v46, 16, v135\n" \
  "v_and_b32 v47, 0xffff0000, v135\n" \
  "v_pk_add_f32 v[40:41], v[40:41], v[16:17] op_sel_hi:[1,0] neg_lo:[0,1] neg_hi:[0,1]\n" \
  "v_pk_add_f32 v[42:43], v[42:43], v[16:17] op_sel_hi:[1,0] neg_lo:[0,1] neg_hi:[0,1]\n" \
  "v_pk_add_f32 v[44:45], v[44:45], v[16:17] op_sel_hi:[1,0] neg_lo:[0,1] neg_hi:[0,1]\n" \
  "v_pk_add_f32 v[46:47], v[46:47], v[16:17] op_sel_hi:[1,0] neg_lo:[0,1] neg_hi:[0,1]\n" \
  "v_pk_fma_f32 v[20:21], v[40:41], v[40:41], v[20:21]\n" \
  "v_pk_add_f32 v[18:19], v[18:19], v[40:41]\n" \
  "v_pk_fma_f32 v[20:21], v[42:43], v[42:43], v[20:21]\n" \
  "v_pk_add_f32 v[18:19], v[18:19], v[42:43]\n" \
  "v_pk_fma_f32 v[20:21], v[44:45], v[44:45], v[20:21]\n" \
  "v_pk_add_f32 v[18:19], v[18:19], v[44:45]\n" \
  "v_pk_fma_f32 v[20:21], v[46:47], v[46:47], v[20:21]\n" \
  "v_pk_add_f32 v[18:19], v[18:19], v[46:47]\n" \
  "s_waitcnt vmcnt(2)\n" \
  "v_max3_u32 v188, v104, v105, v106\n" \
  "v_max3_u32 v189, v107, v120, v121\n" \
  "v_max3_u32 v188, v188, v122, v123\n" \
  "v_max_u32 v188, v188, v189\n" \
  "v_cmp_eq_u32 vcc, -1, v188\n" \
  "s_cbranch_vccz 15f\n" \
  "s_mov_b32 s15, 0x100000\n" \
  "14:\n" \
  "buffer_load_dwordx4 v[104:107], v12, s[48:51], s16 offen offset:0 sc1\n" \
  "buffer_load_dwordx4 v[120:123], v12, s[52:55], s16 offen offset:0 sc1\n" \
  "buffer_load_dwordx4 v[108:111], v12, s[48:51], s16 offen offset:2048 sc1\n" \
  "buffer_load_dwordx4 v[124:127], v12, s[52:55], s16 offen offset:2048 sc1\n" \
  "s_waitcnt vmcnt(0)\n" \
  "v_max3_u32 v188, v104, v105, v106\n" \
  "v_max3_u32 v189, v107, v120, v121\n" \
  "v_max3_u32 v188, v188, v122, v123\n" \
  "v_max_u32 v188, v188, v189\n" \
  "v_cmp_eq_u32 vcc, -1, v188\n" \
  "s_cbranch_vccz 15f\n" \
  "s_sub_u32 s15, s15, 1\n" \
  "s_cmp_lg_u32 s15, 0\n" \
  "s_cbranch_scc1 14b\n" \
  "15:\n" \
  "v_lshlrev_b32 v190, 16, v104\n" \
  "v_and_b32 v191, 0xffff0000, v104\n" \
  "v_lshlrev_b32 v192, 16, v120\n" \
  "v_and_b32 v193, 0xffff0000, v120\n" \
  "v_pk_add_f32 v[190:191], v[190:191], v[192:193]\n" \
  "v_pk_add_f32 v[48:49], v[48:49], v[190:191]\n" \
  "v_lshlrev_b32 v190, 16, v105\n" \
  "v_and_b32 v191, 0xffff0000, v105\n" \
  "v_lshlrev_b32 v192, 16, v121\n" \
  "v_and_b32 v193, 0xffff0000, v121\n" \
  "v_pk_add_f32 v[190:191], v[190:191], v[192:193]\n" \
  "v_pk_add_f32 v[50:51], v[50:51], v[190:191]\n" \
  "v_lshlrev_b32 v190, 16, v106\n" \
  "v_and_b32 v191, 0xffff0000, v106\n" \
  "v_lshlrev_b32 v192, 16, v122\n" \
  "v_and_b32 v193, 0xffff0000, v122\n" \
  "v_pk_add_f32 v[190:191], v[190:191], v[192:193]\n" \
  "v_pk_add_f32 v[52:53], v[52:53], v[190:191]\n" \
  "v_lshlrev_b32 v190, 16, v107\n" \
  "v_and_b32 v191, 0xffff0000, v107\n" \
  "v_lshlrev_b32 v192, 16, v123\n" \
  "v_and_b32 v193, 0xffff0000, v123\n" \
  "v_pk_add_f32 v[190:191], v[190:191], v[192:193]\n" \
  "v_pk_add_f32 v[54:55], v[54:55], v[190:191]\n" \
  "v_cvt_pk_bf16_f32 v136, v48, v49\n" \
  "v_cvt_pk_bf16_f32 v137, v50, v51\n" \
  "v_cvt_pk_bf16_f32 v138, v52, v53\n" \
  "v_cvt_pk_bf16_f32 v139, v54, v55\n" \
  "v_lshlrev_b32 v48, 16, v136\n" \
  "v_and_b32 v49, 0xffff0000, v136\n" \
  "v_lshlrev_b32 v50, 16, v137\n" \
  "v_and_b32 v51, 0xffff0000, v137\n" \
  "v_lshlrev_b32 v52, 16, v138\n" \
  "v_and_b32 v53, 0xffff0000, v138\n" \
  "v_lshlrev_b32 v54, 16, v139\n" \
  "v_and_b32 v55, 0xffff0000, v139\n" \
  "v_pk_add_f32 v[48:49], v[48:49], v[16:17] op_sel_hi:[1,0] neg_lo:[0,1] neg_hi:[0,1]\n" \
  "v_pk_add_f32 v[50:51], v[50:51], v[16:17] op_sel_hi:[1,0] neg_lo:[0,1] neg_hi:[0,1]\n" \
  "v_pk_add_f32 v[52:53], v[52:53], v[16:17] op_sel_hi:[1,0] neg_lo:[0,1] neg_hi:[0,1]\n" \
  "v_pk_add_f32 v[54:55], v[54:55], v[16:17] op_sel_hi:[1,0] neg_lo:[0,1] neg_hi:[0,1]\n" \
  "v_pk_fma_f32 v[20:21], v[48:49], v[48:49], v[20:21]\n" \
  "v_pk_add_f32 v[18:19], v[18:19], v[48:49]\n" \
  "v_pk_fma_f32 v[20:21], v[50:51], v[50:51], v[20:21]\n" \
  "v_pk_add_f32 v[18:19], v[18:19], v[50:51]\n" \
  "v_pk_fma_f32 v[20:21], v[52:53], v[52:53], v[20:21]\n" \
  "v_pk_add_f32 v[18:19], v[18:19], v[52:53]\n" \
  "v_pk_fma_f32 v[20:21], v[54:55], v[54:55], v[20:21]\n" \
  "v_pk_add_f32 v[18:19], v[18:19], v[54:55]\n" \
  "s_waitcnt vmcnt(0)\n" \
  "v_max3_u32 v188, v108, v109, v110\n" \
  "v_max3_u32 v189, v111, v124, v125\n" \
  "v_max3_u32 v188, v188, v126, v127\n" \
  "v_max_u32 v188, v188, v189\n" \
  "v_cmp_eq_u32 vcc, -1, v188\n" \
  "s_cbranch_vccz 17f\n" \
  "s_mov_b32 s15, 0x100000\n" \
  "16:\n" \
  "buffer_load_dwordx4 v[108:111], v12, s[48:51], s16 offen offset:2048 sc1\n" \
  "buffer_load_dwordx4 v[124:127], v12, s[52:55], s16 offen offset:2048 sc1\n" \
  "s_waitcnt vmcnt(0)\n" \
  "v_max3_u32 v188, v108, v109, v110\n" \
  "v_max3_u32 v189, v111, v124, v125\n" \
  "v_max3_u32 v188, v188, v126, v127\n" \
  "v_max_u32 v188, v188, v189\n" \
  "v_cmp_eq_u32 vcc, -1, v188\n" \
  "s_cbranch_vccz 17f\n" \
  "s_sub_u32 s15, s15, 1\n" \
  "s_cmp_lg_u32 s15, 0\n" \
  "s_cbranch_scc1 16b\n" \
  "17:\n" \
  "v_lshlrev_b32 v190, 16, v108\n" \
  "v_and_b32 v191, 0xffff0000, v108\n" \
  "v_lshlrev_b32 v192, 16, v124\n" \
  "v_and_b32 v193, 0xffff0000, v124\n" \
  "v_pk_add_f32 v[190:191], v[190:191], v[192:193]\n" \
  "v_pk_add_f32 v[56:57], v[56:57], v[190:191]\n" \
  "v_lshlrev_b32 v190, 16, v109\n" \
  "v_and_b32 v191, 0xffff0000, v109\n" \
  "v_lshlrev_b32 v192, 16, v125\n" \
  "v_and_b32 v193, 0xffff0000, v125\n" \
  "v_pk_add_f32 v[190:191], v[190:191], v[192:193]\n" \
  "v_pk_add_f32 v[58:59], v[58:59], v[190:191]\n" \
  "v_lshlrev_b32 v190, 16, v110\n" \
  "v_and_b32 v191, 0xffff0000, v110\n" \
  "v_lshlrev_b32 v192, 16, v126\n" \
  "v_and_b32 v193, 0xffff0000, v126\n" \
  "v_pk_add_f32 v[190:191], v[190:191], v[192:193]\n" \
  "v_pk_add_f32 v[60:61], v[60:61], v[190:191]\n" \
  "v_lshlrev_b32 v190, 16, v111\n" \
  "v_and_b32 v191, 0xffff0000, v111\n" \
  "v_lshlrev_b32 v192, 16, v127\n" \
  "v_and_b32 v193, 0xffff0000, v127\n" \
  "v_pk_add_f32 v[190:191], v[190:191], v[192:193]\n" \
  "v_pk_add_f32 v[62:63], v[62:63], v[190:191]\n" \
  "v_cvt_pk_bf16_f32 v140, v56, v57\n" \
  "v_cvt_pk_bf16_f32 v141, v58, v59\n" \
  "v_cvt_pk_bf16_f32 v142, v60, v61\n" \
  "v_cvt_pk_bf16_f32 v143, v62, v63\n" \
  "v_lshlrev_b32 v56, 16, v140\n" \
  "v_and_b32 v57, 0xffff0000, v140\n" \
  "v_lshlrev_b32 v58, 16, v141\n" \
  "v_and_b32 v59, 0xffff0000, v141\n" \
  "v_lshlrev_b32 v60, 16, v142\n" \
  "v_and_b32 v61, 0xffff0000, v142\n" \
  "v_lshlrev_b32 v62, 16, v143\n" \
  "v_and_b32 v63, 0xffff0000, v143\n" \
  "v_pk_add_f32 v[56:57], v[56:57], v[16:17] op_sel_hi:[1,0] neg_lo:[0,1] neg_hi:[0,1]\n" \
  "v_pk_add_f32 v[58:59], v[58:59], v[16:17] op_sel_hi:[1,0] neg_lo:[0,1] neg_hi:[0,1]\n" \
  "v_pk_add_f32 v[60:61], v[60:61], v[16:17] op_sel_hi:[1,0] neg_lo:[0,1] neg_hi:[0,1]\n" \
  "v_pk_add_f32 v[62:63], v[62:63], v[16:17] op_sel_hi:[1,0] neg_lo:[0,1] neg_hi:[0,1]\n" \
  "v_pk_fma_f32 v[20:21], v[56:57], v[56:57], v[20:21]\n" \
  "v_pk_add_f32 v[18:19], v[18:19], v[56:57]\n" \
  "v_pk_fma_f32 v[20:21], v[58:59], v[58:59], v[20:21]\n" \
  "v_pk_add_f32 v[18:19], v[18:19], v[58:59]\n" \
  "v_pk_fma_f32 v[20:21], v[60:61], v[60:61], v[20:21]\n" \
  "v_pk_add_f32 v[18:19], v[18:19], v[60:61]\n" \
  "v_pk_fma_f32 v[20:21], v[62:63], v[62:63], v[20:21]\n" \
  "v_pk_add_f32 v[18:19], v[18:19], v[62:63]\n" \
  "v_add_f32 v18, v18, v19\n" \
  "v_add_f32 v19, v20, v21\n" \
  "s_nop 0\n" \
  "v_add_f32_dpp v18, v18, v18 row_ror:8 row_mask:0xf bank_mask:0xf\n" \
  "v_add_f32_dpp v19, v19, v19 row_ror:8 row_mask:0xf bank_mask:0xf\n" \
  "v_mov_b32 v22, v18\n" \
  "v_mov_b32 v23, v19\n" \
  "s_nop 0\n" \
  "v_permlane16_swap_b32 v18, v22\n" \
  "v_permlane16_swap_b32 v19, v23\n" \
  "v_pk_add_f32 v[18:19], v[18:19], v[22:23]\n" \
  "v_mov_b32 v22, v18\n" \
  "v_mov_b32 v23, v19\n" \
  "s_nop 0\n" \
  "v_permlane32_swap_b32 v18, v22\n" \
  "v_permlane32_swap_b32 v19, v23\n" \
  "v_pk_add_f32 v[18:19], v[18:19], v[22:23]\n" \
  "ds_write_b64 v176, v[18:19] offset:0\n" \
  "s_waitcnt lgkmcnt(0)\n" \
  "s_barrier\n" \
  "ds_read_b128 v[180:183], v177 offset:0\n" \
  "ds_read_b128 v[184:187], v177 offset:16\n" \
  "s_waitcnt lgkmcnt(1)\n" \
  "v_pk_add_f32 v[180:181], v[180:181], v[182:183]\n" \
  "s_waitcnt lgkmcnt(0)\n" \
  "v_pk_add_f32 v[184:185], v[184:185], v[186:187]\n" \
  "v_pk_add_f32 v[180:181], v[180:181], v[184:185]\n" \
  "v_mul_f32 v180, 0x3a800000, v180\n" \
  "v_mul_f32 v181, 0x3a800000, v181\n" \
  "v_fma_f32 v181, -v180, v180, v181\n" \
  "v_add_f32 v180, v180, v16\n" \
  "v_max_f32 v181, 0, v181\n" \
  "v_cvt_pk_bf16_f32 v180, v180, v181\n" \
  "v_and_b32 v181, 0xffff0000, v180\n" \
  "v_lshlrev_b32 v180, 16, v180\n" \
  "v_add_f32 v181, 0x3727c5ac, v181\n" \
  "v_sub_f32 v22, v180, v16\n" \
  "v_cvt_pk_bf16_f32 v181, v181, v181\n" \
  "v_lshlrev_b32 v181, 16, v181\n" \
  "v_sqrt_f32 v181, v181\n" \
  "s_nop 0\n" \
  "v_cvt_pk_bf16_f32 v181, v181, v181\n" \
  "v_lshlrev_b32 v181, 16, v181\n" \
  "v_rcp_f32 v17, v181\n" \
  "s_nop 0\n" \
  "v_pk_add_f32 v[32:33], v[32:33], v[22:23] op_sel_hi:[1,0] neg_lo:[0,1] neg_hi:[0,1]\n" \
  "v_pk_add_f32 v[34:35], v[34:35], v[22:23] op_sel_hi:[1,0] neg_lo:[0,1] neg_hi:[0,1]\n" \
  "v_pk_add_f32 v[36:37], v[36:37], v[22:23] op_sel_hi:[1,0] neg_lo:[0,1] neg_hi:[0,1]\n" \
  "v_pk_add_f32 v[38:39], v[38:39], v[22:23] op_sel_hi:[1,0] neg_lo:[0,1] neg_hi:[0,1]\n" \
  "v_pk_add_f32 v[40:41], v[40:41], v[22:23] op_sel_hi:[1,0] neg_lo:[0,1] neg_hi:[0,1]\n" \
  "v_pk_add_f32 v[42:43], v[42:43], v[22:23] op_sel_hi:[1,0] neg_lo:[0,1] neg_hi:[0,1]\n" \
  "v_pk_add_f32 v[44:45], v[44:45], v[22:23] op_sel_hi:[1,0] neg_lo:[0,1] neg_hi:[0,1]\n" \
  "v_pk_add_f32 v[46:47], v[46:47], v[22:23] op_sel_hi:[1,0] neg_lo:[0,1] neg_hi:[0,1]\n" \
  "v_pk_add_f32 v[48:49], v[48:49], v[22:23] op_sel_hi:[1,0] neg_lo:[0,1] neg_hi:[0,1]\n" \
  "v_pk_add_f32 v[50:51], v[50:51], v[22:23] op_sel_hi:[1,0] neg_lo:[0,1] neg_hi:[0,1]\n" \
  "v_pk_add_f32 v[52:53], v[52:53], v[22:23] op_sel_hi:[1,0] neg_lo:[0,1] neg_hi:[0,1]\n" \
  "v_pk_add_f32 v[54:55], v[54:55], v[22:23] op_sel_hi:[1,0] neg_lo:[0,1] neg_hi:[0,1]\n" \
  "v_pk_add_f32 v[56:57], v[56:57], v[22:23] op_sel_hi:[1,0] neg_lo:[0,1] neg_hi:[0,1]\n" \
  "v_pk_add_f32 v[58:59], v[58:59], v[22:23] op_sel_hi:[1,0] neg_lo:[0,1] neg_hi:[0,1]\n" \
  "v_pk_add_f32 v[60:61], v[60:61], v[22:23] op_sel_hi:[1,0] neg_lo:[0,1] neg_hi:[0,1]\n" \
  "v_pk_add_f32 v[62:63], v[62:63], v[22:23] op_sel_hi:[1,0] neg_lo:[0,1] neg_hi:[0,1]\n" \
  "v_add_f32 v16, v16, v22\n" \
  "s_add_u32 s40, s40, 4194304\n" \
  "s_addc_u32 s41, s41, 0\n" \
  "s_add_u32 s44, s44, 16777216\n" \
  "s_addc_u32 s45, s45, 0\n" \
  "s_add_u32 s48, s48, 8388608\n" \
  "s_addc_u32 s49, s49, 0\n" \
  "s_add_u32 s52, s52, 8388608\n" \
  "s_addc_u32 s53, s53, 0\n" \
  "s_add_u32 s60, s60, 16777216\n" \
  "s_addc_u32 s61, s61, 0\n" \
  "s_add_u32 s64, s64, 8388608\n" \
  "s_addc_u32 s65, s65, 0\n" \
  "s_add_u32 s68, s68, 98304\n" \
  "s_addc_u32 s69, s69, 0\n" \
  "s_add_u32 s72, s72, 4194304\n" \
  "s_addc_u32 s73, s73, 0\n" \
  "s_mov_b32 s18, 1\n" \
  "s_branch 9b\n" \
  "20:\n" \
  "v_mbcnt_lo_u32_b32 v16, -1, 0\n" \
  "v_mbcnt_hi_u32_b32 v16, -1, v16\n" \
  "s_lshl_b32 s19, s83, 9\n" \
  "v_and_b32 v17, 7, v16\n" \
  "v_lshlrev_b32 v17, 11, v17\n" \
  "v_bfe_u32 v18, v16, 3, 2\n" \
  "v_lshl_add_u32 v17, v18, 4, v17\n" \
  "v_lshrrev_b32 v16, 5, v16\n" \
  "v_lshl_add_u32 v17, v16, 8, v17\n" \
  "v_add_u32 v17, s19, v17\n" \
  "s_waitcnt vmcnt(6)\n" \
  "v_max3_u32 v188, v96, v97, v98\n" \
  "v_max3_u32 v189, v99, v112, v113\n" \
  "v_max3_u32 v188, v188, v114, v115\n" \
  "v_max_u32 v188, v188, v189\n" \
  "v_cmp_eq_u32 vcc, -1, v188\n" \
  "s_cbranch_vccz 23f\n" \
  "s_mov_b32 s15, 0x100000\n" \
  "22:\n" \
  "buffer_load_dwordx4 v[96:99], v12, s[48:51], 0 offen offset:0 sc1\n" \
  "buffer_load_dwordx4 v[112:115], v12, s[52:55], 0 offen offset:0 sc1\n" \
  "buffer_load_dwordx4 v[100:103], v12, s[48:51], 0 offen offset:2048 sc1\n" \
  "buffer_load_dwordx4 v[116:119], v12, s[52:55], 0 offen offset:2048 sc1\n" \
  "buffer_load_dwordx4 v[104:107], v12, s[48:51], s16 offen offset:0 sc1\n" \
  "buffer_load_dwordx4 v[120:123], v12, s[52:55], s16 offen offset:0 sc1\n" \
  "buffer_load_dwordx4 v[108:111], v12, s[48:51], s16 offen offset:2048 sc1\n" \
  "buffer_load_dwordx4 v[124:127], v12, s[52:55], s16 offen offset:2048 sc1\n" \
  "s_waitcnt vmcnt(0)\n" \
  "v_max3_u32 v188, v96, v97, v98\n" \
  "v_max3_u32 v189, v99, v112, v113\n" \
  "v_max3_u32 v188, v188, v114, v115\n" \
  "v_max_u32 v188, v188, v189\n" \
  "v_cmp_eq_u32 vcc, -1, v188\n" \
  "s_cbranch_vccz 23f\n" \
  "s_sub_u32 s15, s15, 1\n" \
  "s_cmp_lg_u32 s15, 0\n" \
  "s_cbranch_scc1 22b\n" \
  "23:\n" \
  "v_lshlrev_b32 v190, 16, v96\n" \
  "v_and_b32 v191, 0xffff0000, v96\n" \
  "v_lshlrev_b32 v192, 16, v112\n" \
  "v_and_b32 v193, 0xffff0000, v112\n" \
  "v_pk_add_f32 v[190:191], v[190:191], v[192:193]\n" \
  "v_pk_add_f32 v[32:33], v[32:33], v[190:191]\n" \
  "v_lshlrev_b32 v190, 16, v97\n" \
  "v_and_b32 v191, 0xffff0000, v97\n" \
  "v_lshlrev_b32 v192, 16, v113\n" \
  "v_and_b32 v193, 0xffff0000, v113\n" \
  "v_pk_add_f32 v[190:191], v[190:191], v[192:193]\n" \
  "v_pk_add_f32 v[34:35], v[34:35], v[190:191]\n" \
  "v_lshlrev_b32 v190, 16, v98\n" \
  "v_and_b32 v191, 0xffff0000, v98\n" \
  "v_lshlrev_b32 v192, 16, v114\n" \
  "v_and_b32 v193, 0xffff0000, v114\n" \
  "v_pk_add_f32 v[190:191], v[190:191], v[192:193]\n" \
  "v_pk_add_f32 v[36:37], v[36:37], v[190:191]\n" \
  "v_lshlrev_b32 v190, 16, v99\n" \
  "v_and_b32 v191, 0xffff0000, v99\n" \
  "v_lshlrev_b32 v192, 16, v115\n" \
  "v_and_b32 v193, 0xffff0000, v115\n" \
  "v_pk_add_f32 v[190:191], v[190:191], v[192:193]\n" \
  "v_pk_add_f32 v[38:39], v[38:39], v[190:191]\n" \
  "v_cvt_pk_bf16_f32 v160, v32, v33\n" \
  "v_cvt_pk_bf16_f32 v161, v34, v35\n" \
  "v_cvt_pk_bf16_f32 v162, v36, v37\n" \
  "v_cvt_pk_bf16_f32 v163, v38, v39\n" \
  "buffer_store_dwordx4 v[160:163], v17, s[76:79], 0 offen offset:0\n" \
  "s_waitcnt vmcnt(5)\n" \
  "v_max3_u32 v188, v100, v101, v102\n" \
  "v_max3_u32 v189, v103, v116, v117\n" \
  "v_max3_u32 v188, v188, v118, v119\n" \
  "v_max_u32 v188, v188, v189\n" \
  "v_cmp_eq_u32 vcc, -1, v188\n" \
  "s_cbranch_vccz 25f\n" \
  "s_mov_b32 s15, 0x100000\n" \
  "24:\n" \
  "buffer_load_dwordx4 v[100:103], v12, s[48:51], 0 offen offset:2048 sc1\n" \
  "buffer_load_dwordx4 v[116:119], v12, s[52:55], 0 offen offset:2048 sc1\n" \
  "buffer_load_dwordx4 v[104:107], v12, s[48:51], s16 offen offset:0 sc1\n" \
  "buffer_load_dwordx4 v[120:123], v12, s[52:55], s16 offen offset:0 sc1\n" \
  "buffer_load_dwordx4 v[108:111], v12, s[48:51], s16 offen offset:2048 sc1\n" \
  "buffer_load_dwordx4 v[124:127], v12, s[52:55], s16 offen offset:2048 sc1\n" \
  "s_waitcnt vmcnt(0)\n" \
  "v_max3_u32 v188, v100, v101, v102\n" \
  "v_max3_u32 v189, v103, v116, v117\n" \
  "v_max3_u32 v188, v188, v118, v119\n" \
  "v_max_u32 v188, v188, v189\n" \
  "v_cmp_eq_u32 vcc, -1, v188\n" \
  "s_cbranch_vccz 25f\n" \
  "s_sub_u32 s15, s15, 1\n" \
  "s_cmp_lg_u32 s15, 0\n" \
  "s_cbranch_scc1 24b\n" \
  "25:\n" \
  "v_lshlrev_b32 v190, 16, v100\n" \
  "v_and_b32 v191, 0xffff0000, v100\n" \
  "v_lshlrev_b32 v192, 16, v116\n" \
  "v_and_b32 v193, 0xffff0000, v116\n" \
  "v_pk_add_f32 v[190:191], v[190:191], v[192:193]\n" \
  "v_pk_add_f32 v[40:41], v[40:41], v[190:191]\n" \
  "v_lshlrev_b32 v190, 16, v101\n" \
  "v_and_b32 v191, 0xffff0000, v101\n" \
  "v_lshlrev_b32 v192, 16, v117\n" \
  "v_and_b32 v193, 0xffff0000, v117\n" \
  "v_pk_add_f32 v[190:191], v[190:191], v[192:193]\n" \
  "v_pk_add_f32 v[42:43], v[42:43], v[190:191]\n" \
  "v_lshlrev_b32 v190, 16, v102\n" \
  "v_and_b32 v191, 0xffff0000, v102\n" \
  "v_lshlrev_b32 v192, 16, v118\n" \
  "v_and_b32 v193, 0xffff0000, v118\n" \
  "v_pk_add_f32 v[190:191], v[190:191], v[192:193]\n" \
  "v_pk_add_f32 v[44:45], v[44:45], v[190:191]\n" \
  "v_lshlrev_b32 v190, 16, v103\n" \
  "v_and_b32 v191, 0xffff0000, v103\n" \
  "v_lshlrev_b32 v192, 16, v119\n" \
  "v_and_b32 v193, 0xffff0000, v119\n" \
  "v_pk_add_f32 v[190:191], v[190:191], v[192:193]\n" \
  "v_pk_add_f32 v[46:47], v[46:47], v[190:191]\n" \
  "v_cvt_pk_bf16_f32 v164, v40, v41\n" \
  "v_cvt_pk_bf16_f32 v165, v42, v43\n" \
  "v_cvt_pk_bf16_f32 v166, v44, v45\n" \
  "v_cvt_pk_bf16_f32 v167, v46, v47\n" \
  "buffer_store_dwordx4 v[164:167], v17, s[76:79], 0 offen offset:64\n" \
  "s_waitcnt vmcnt(4)\n" \
  "v_max3_u32 v188, v104, v105, v106\n" \
  "v_max3_u32 v189, v107, v120, v121\n" \
  "v_max3_u32 v188, v188, v122, v123\n" \
  "v_max_u32 v188, v188, v189\n" \
  "v_cmp_eq_u32 vcc, -1, v188\n" \
  "s_cbranch_vccz 27f\n" \
  "s_mov_b32 s15, 0x100000\n" \
  "26:\n" \
  "buffer_load_dwordx4 v[104:107], v12, s[48:51], s16 offen offset:0 sc1\n" \
  "buffer_load_dwordx4 v[120:123], v12, s[52:55], s16 offen offset:0 sc1\n" \
  "buffer_load_dwordx4 v[108:111], v12, s[48:51], s16 offen offset:2048 sc1\n" \
  "buffer_load_dwordx4 v[124:127], v12, s[52:55], s16 offen offset:2048 sc1\n" \
  "s_waitcnt vmcnt(0)\n" \
  "v_max3_u32 v188, v104, v105, v106\n" \
  "v_max3_u32 v189, v107, v120, v121\n" \
  "v_max3_u32 v188, v188, v122, v123\n" \
  "v_max_u32 v188, v188, v189\n" \
  "v_cmp_eq_u32 vcc, -1, v188\n" \
  "s_cbranch_vccz 27f\n" \
  "s_sub_u32 s15, s15, 1\n" \
  "s_cmp_lg_u32 s15, 0\n" \
  "s_cbranch_scc1 26b\n" \
  "27:\n" \
  "v_lshlrev_b32 v190, 16, v104\n" \
  "v_and_b32 v191, 0xffff0000, v104\n" \
  "v_lshlrev_b32 v192, 16, v120\n" \
  "v_and_b32 v193, 0xffff0000, v120\n" \
  "v_pk_add_f32 v[190:191], v[190:191], v[192:193]\n" \
  "v_pk_add_f32 v[48:49], v[48:49], v[190:191]\n" \
  "v_lshlrev_b32 v190, 16, v105\n" \
  "v_and_b32 v191, 0xffff0000, v105\n" \
  "v_lshlrev_b32 v192, 16, v121\n" \
  "v_and_b32 v193, 0xffff0000, v121\n" \
  "v_pk_add_f32 v[190:191], v[190:191], v[192:193]\n" \
  "v_pk_add_f32 v[50:51], v[50:51], v[190:191]\n" \
  "v_lshlrev_b32 v190, 16, v106\n" \
  "v_and_b32 v191, 0xffff0000, v106\n" \
  "v_lshlrev_b32 v192, 16, v122\n" \
  "v_and_b32 v193, 0xffff0000, v122\n" \
  "v_pk_add_f32 v[190:191], v[190:191], v[192:193]\n" \
  "v_pk_add_f32 v[52:53], v[52:53], v[190:191]\n" \
  "v_lshlrev_b32 v190, 16, v107\n" \
  "v_and_b32 v191, 0xffff0000, v107\n" \
  "v_lshlrev_b32 v192, 16, v123\n" \
  "v_and_b32 v193, 0xffff0000, v123\n" \
  "v_pk_add_f32 v[190:191], v[190:191], v[192:193]\n" \
  "v_pk_add_f32 v[54:55], v[54:55], v[190:191]\n" \
  "v_cvt_pk_bf16_f32 v168, v48, v49\n" \
  "v_cvt_pk_bf16_f32 v169, v50, v51\n" \
  "v_cvt_pk_bf16_f32 v170, v52, v53\n" \
  "v_cvt_pk_bf16_f32 v171, v54, v55\n" \
  "buffer_store_dwordx4 v[168:171], v17, s[76:79], 0 offen offset:128\n" \
  "s_waitcnt vmcnt(3)\n" \
  "v_max3_u32 v188, v108, v109, v110\n" \
  "v_max3_u32 v189, v111, v124, v125\n" \
  "v_max3_u32 v188, v188, v126, v127\n" \
  "v_max_u32 v188, v188, v189\n" \
  "v_cmp_eq_u32 vcc, -1, v188\n" \
  "s_cbranch_vccz 29f\n" \
  "s_mov_b32 s15, 0x100000\n" \
  "28:\n" \
  "buffer_load_dwordx4 v[108:111], v12, s[48:51], s16 offen offset:2048 sc1\n" \
  "buffer_load_dwordx4 v[124:127], v12, s[52:55], s16 offen offset:2048 sc1\n" \
  "s_waitcnt vmcnt(0)\n" \
  "v_max3_u32 v188, v108, v109, v110\n" \
  "v_max3_u32 v189, v111, v124, v125\n" \
  "v_max3_u32 v188, v188, v126, v127\n" \
  "v_max_u32 v188, v188, v189\n" \
  "v_cmp_eq_u32 vcc, -1, v188\n" \
  "s_cbranch_vccz 29f\n" \
  "s_sub_u32 s15, s15, 1\n" \
  "s_cmp_lg_u32 s15, 0\n" \
  "s_cbranch_scc1 28b\n" \
  "29:\n" \
  "v_lshlrev_b32 v190, 16, v108\n" \
  "v_and_b32 v191, 0xffff0000, v108\n" \
  "v_lshlrev_b32 v192, 16, v124\n" \
  "v_and_b32 v193, 0xffff0000, v124\n" \
  "v_pk_add_f32 v[190:191], v[190:191], v[192:193]\n" \
  "v_pk_add_f32 v[56:57], v[56:57], v[190:191]\n" \
  "v_lshlrev_b32 v190, 16, v109\n" \
  "v_and_b32 v191, 0xffff0000, v109\n" \
  "v_lshlrev_b32 v192, 16, v125\n" \
  "v_and_b32 v193, 0xffff0000, v125\n" \
  "v_pk_add_f32 v[190:191], v[190:191], v[192:193]\n" \
  "v_pk_add_f32 v[58:59], v[58:59], v[190:191]\n" \
  "v_lshlrev_b32 v190, 16, v110\n" \
  "v_and_b32 v191, 0xffff0000, v110\n" \
  "v_lshlrev_b32 v192, 16, v126\n" \
  "v_and_b32 v193, 0xffff0000, v126\n" \
  "v_pk_add_f32 v[190:191], v[190:191], v[192:193]\n" \
  "v_pk_add_f32 v[60:61], v[60:61], v[190:191]\n" \
  "v_lshlrev_b32 v190, 16, v111\n" \
  "v_and_b32 v191, 0xffff0000, v111\n" \
  "v_lshlrev_b32 v192, 16, v127\n" \
  "v_and_b32 v193, 0xffff0000, v127\n" \
  "v_pk_add_f32 v[190:191], v[190:191], v[192:193]\n" \
  "v_pk_add_f32 v[62:63], v[62:63], v[190:191]\n" \
  "v_cvt_pk_bf16_f32 v172, v56, v57\n" \
  "v_cvt_pk_bf16_f32 v173, v58, v59\n" \
  "v_cvt_pk_bf16_f32 v174, v60, v61\n" \
  "v_cvt_pk_bf16_f32 v175, v62, v63\n" \
  "buffer_store_dwordx4 v[172:175], v17, s[76:79], 0 offen offset:192\n" \
  ".Lmg_end:\n" \
  "s_nop 1\n"
#define MG_CLOB "v14", "v15", "v32", "v33", "v34", "v35", "v36", "v37", "v38", "v39", "v40", "v41", "v42", "v43", "v44", "v45", "v46", "v47", "v48", "v49", "v50", "v51", "v52", "v53", "v54", "v55", "v56", "v57", "v58", "v59", "v60", "v61", "v62", "v63", "v64", "v65", "v66", "v67", "v68", "v69", "v70", "v71", "v72", "v73", "v74", "v75", "v76", "v77", "v78", "v79", "v80", "v81", "v82", "v83", "v84", "v85", "v86", "v87", "v88", "v89", "v90", "v91", "v92", "v93", "v94", "v95", "v96", "v97", "v98", "v99", "v100", "v101", "v102", "v103", "v104", "v105", "v106", "v107", "v108", "v109", "v110", "v111", "v112", "v113", "v114", "v115", "v116", "v117", "v118", "v119", "v120", "v121", "v122", "v123", "v124", "v125", "v126", "v127", "v128", "v129", "v130", "v131", "v132", "v133", "v134", "v135", "v136", "v137", "v138", "v139", "v140", "v141", "v142", "v143", "v144", "v145", "v146", "v147", "v148", "v149", "v150", "v151", "v152", "v153", "v154", "v155", "v156", "v157", "v158", "v159", "v160", "v161", "v162", "v163", "v164", "v165", "v166", "v167", "v168", "v169", "v170", "v171", "v172", "v173", "v174", "v175", "v176", "v177", "v178", "v179", "v180", "v181", "v182", "v183", "v184", "v185", "v186", "v187", "v188", "v189", "v190", "v191", "v192", "v193", "v194", "v195", "v196", "v197", "v198", "v199", "v200", "v201", "v202", "v203", "v204", "v205", "v206", "v207", "v208", "v209", "v210", "v211", "v212", "v213", "v214", "v215", "v216", "v217", "v218", "v219", "v220", "v221", "v222", "v223", "v224", "v225", "v226", "v227", "v228", "v229", "v230", "v231", "v232", "v233", "v234", "v235", "v236", "v237", "v238", "v239", "v240", "v241", "v242", "v243", "v244", "v245", "v246", "v247", "v248", "v249", "v250", "v251", "v252", "v253", "v254", "v255", "a0", "a1", "a2", "a3", "a4", "a5", "a6", "a7", "a8", "a9", "a10", "a11", "a12", "a13", "a14", "a15", "a16", "a17", "a18", "a19", "a20", "a21", "a22", "a23", "a24", "a25", "a26", "a27", "a28", "a29", "a30", "a31", "a32", "a33", "a34", "a35", "a36", "a37", "a38", "a39", "a40", "a41", "a42", "a43", "a44", "a45", "a46", "a47", "a48", "a49", "a50", "a51", "a52", "a53", "a54", "a55", "a56", "a57", "a58", "a59", "a60", "a61", "a62", "a63", "a64", "a65", "a66", "a67", "a68", "a69", "a70", "a71", "a72", "a73", "a74", "a75", "a76", "a77", "a78", "a79", "a80", "a81", "a82", "a83", "a84", "a85", "a86", "a87", "a88", "a89", "a90", "a91", "a92", "a93", "a94", "a95", "a96", "a97", "a98", "a99", "a100", "a101", "a102", "a103", "a104", "a105", "a106", "a107", "a108", "a109", "a110", "a111", "a112", "a113", "a114", "a115", "a116", "a117", "a118", "a119", "a120", "a121", "a122", "a123", "a124", "a125", "a126", "a127", "a128", "a129", "a130", "a131", "a132", "a133", "a134", "a135", "a136", "a137", "a138", "a139", "a140", "a141", "a142", "a143", "a144", "a145", "a146", "a147", "a148", "a149", "a150", "a151", "a152", "a153", "a154", "a155", "a156", "a157", "a158", "a159", "a168", "a169", "a170", "a171", "a172", "a173", "a174", "a175", "a176", "a177", "a178", "a179", "a180", "a181", "a182", "a183", "a184", "a185", "a186", "a187", "a188", "a189", "a190", "a191", "a192", "a193", "a194", "a195", "a196", "a197", "a198", "a199", "a200", "a201", "a202", "a203", "a204", "a205", "a206", "a207", "a208", "a209", "a210", "a211", "a212", "a213", "a214", "a215", "a216", "a217", "a218", "a219", "a220", "a221", "a222", "a223", "a224", "a225", "a226", "a227", "a228", "a229", "a230", "a231", "a232", "a233", "a234", "a235", "a236", "a237", "a238", "a239", "a240", "a241", "a242", "a243", "a244", "a245", "a246", "a247", "a248", "a249", "a250", "a251", "a252", "a253", "a254", "a255", "s8", "s9", "s10", "s11", "s12", "s13", "s14", "s15", "s16", "s17", "s18", "s19", "s20", "s21", "s22", "s23", "s28", "s30", "s31", "vcc"
#define MG_LDS 163840

typedef unsigned short u16;
typedef unsigned long long u64;
typedef short s16x8 __attribute__((ext_vector_type(8)));
typedef short s16x4 __attribute__((ext_vector_type(4)));
typedef float f32x2 __attribute__((ext_vector_type(2)));
typedef float f32x4 __attribute__((ext_vector_type(4)));
typedef float f32x32 __attribute__((ext_vector_type(32)));
typedef unsigned u32x4 __attribute__((ext_vector_type(4)));
typedef unsigned u32x2 __attribute__((ext_vector_type(2)));
typedef unsigned u32x8 __attribute__((ext_vector_type(8)));
typedef unsigned u32x16 __attribute__((ext_vector_type(16)));
typedef unsigned u32x32 __attribute__((ext_vector_type(32)));
typedef __bf16 bf16x2 __attribute__((ext_vector_type(2)));
#define LAS __attribute__((address_space(3)))
#define DI __device__ __forceinline__
constexpr int S = 2048, D = 1024, F = 3584;
constexpr size_t MB4 = (size_t)S * D * 2;
constexpr size_t OFF_MB = 0, MB_H = 32768  , OFF_UT = MB4, OFF_YB = 2 * MB4, OFF_X = 3 * MB4  , OFF_N = 5 * MB4  , OFF_P = 7 * MB4  ,
                 OFF_H = 11 * MB4  , OFF_M = 19 * MB4, OFF_DBG = 20 * MB4;
typedef __attribute__((address_space(1))) unsigned gu32;
#define RLX_AGENT __ATOMIC_RELAXED, __HIP_MEMORY_SCOPE_AGENT
#define STAMP(i)

constexpr unsigned SENT = 0xffffffffu;
#define PRE_TOUCH 0
#define CODE_TOUCH 1
#define ROTATE 0
#define FILL_AUX 16
DI float lo(unsigned w) { return __uint_as_float(w << 16); }
DI float hi(unsigned w) { return __uint_as_float(w & 0xffff0000u); }
DI unsigned pk(float a, float b) { bf16x2 r = __builtin_convertvector((f32x2){a, b}, bf16x2); return __builtin_bit_cast(unsigned, r); }

DI int fresh_tid(int w) { int l; asm volatile("v_mbcnt_lo_u32_b32 %0, -1, 0\n v_mbcnt_hi_u32_b32 %0, -1, %0" : "=v"(l)); return w * 64 + l; }
DI void touch(const void* p, unsigned lds_junk) { asm volatile("s_mov_b32 m0, %1\n s_nop 0\n global_load_lds_dword %0, off" :: "v"(p), "s"(lds_junk) : "memory"); }

DI void phase_pre(const u16* __restrict__ K, const u16* __restrict__ V, const u16* __restrict__ SH, const u16* HK, const u16* Q, char* ws, int bid, int tid) {
  u16* __restrict__ UT = (u16*)(ws + OFF_UT);
  const int cgp = bid & 15, tb = bid >> 4;
  const int lane = tid & 63, w = tid >> 6, qd = lane & 15, oct = tb * 16 + w * 4 + (lane >> 4);
  const int c0 = cgp * 64 + qd * 4, t0 = oct * 8;
  u32x2 kk[11], vv[8];
#pragma unroll
  for (int i = 0; i < 11; ++i) { const int t = t0 + i - 3; kk[i] = *(const u32x2*)(K + (size_t)(t < 0 ? 0 : t) * D + c0); if (t < 0) kk[i] = (u32x2){0u, 0u}; }
#pragma unroll
  for (int i = 0; i < 8; ++i) vv[i] = *(const u32x2*)(V + (size_t)(t0 + i) * D + c0);
  const u32x4 s01 = *(const u32x4*)(SH + c0 * 4), s23 = *(const u32x4*)(SH + c0 * 4 + 8);
  __builtin_amdgcn_sched_barrier(0);
  if (tid < 48) *(u32x4*)(ws + OFF_MB + (bid * 48 + tid) * 16) = (u32x4){0u, 0u, 0u, 0u};
  { const __amdgpu_buffer_rsrc_t ry = __builtin_amdgcn_make_buffer_rsrc(ws + OFF_YB + (size_t)bid * 16384, (short)0, 16384, 0x00020000);
#pragma unroll
    for (int i = 0; i < 4; ++i) __builtin_amdgcn_raw_buffer_store_b128((u32x4){SENT, SENT, SENT, SENT}, ry, (i * 256 + tid) * 16, 0, FILL_AUX); }
  __builtin_amdgcn_sched_barrier(0);
  const unsigned sw[8] = {s01.x, s01.y, s01.z, s01.w, s23.x, s23.y, s23.z, s23.w};
#pragma unroll
  for (int c = 0; c < 4; ++c) {
    const float w0 = lo(sw[2 * c]), w1 = hi(sw[2 * c]), w2 = lo(sw[2 * c + 1]), w3 = hi(sw[2 * c + 1]);
    float kf[11], u[8];
#pragma unroll
    for (int i = 0; i < 11; ++i) { const unsigned x = c < 2 ? kk[i].x : kk[i].y; kf[i] = (c & 1) ? hi(x) : lo(x); }
#pragma unroll
    for (int i = 0; i < 8; ++i) { const unsigned x = c < 2 ? vv[i].x : vv[i].y; const float vf = (c & 1) ? hi(x) : lo(x);
      u[i] = (kf[i] * w3 + kf[i + 1] * w2 + kf[i + 2] * w1 + kf[i + 3] * w0) * vf; }
    *(u32x4*)(UT + (size_t)(c0 + c) * S + t0) = (u32x4){pk(u[7], u[6]), pk(u[5], u[4]), pk(u[3], u[2]), pk(u[1], u[0])};
  }
}

constexpr int CV_CP = 4160, CV_U = 4 * CV_CP, CV_WAVE = CV_U + 4608, CV_STAGE = 4 * CV_WAVE;
DI u32x4 clamp_sent(u32x4 v) { return __builtin_elementwise_min(v, (u32x4){SENT - 1, SENT - 1, SENT - 1, SENT - 1}); }
DI int row_oct(int tid, int i) { return 16 * (tid >> 5) + 4 * i + ((tid >> 3) & 3); }
DI unsigned row_toff(int rb, int tid) { const int row = rb * 8 + (tid & 7), o0 = row_oct(tid, 0); return ((row >> 4) & 1) * 512 + (row & 15) * 16 + (o0 >> 1) * 1024 + (o0 & 1) * 256; }
constexpr int LDS_ROW = 114688, LDS_JUNK = LDS_ROW + 1024, LDS_MAIN = MG_LDS;
constexpr unsigned OOB = 0x80000000u;
DI int ftile(int pn, int j) { return 112 * (pn >> 4) + (j < 3 ? 3 * (pn & 15) + j : 45 + 4 * (pn & 15) + j); }
DI u32x4 srd(const void* p, unsigned bytes) { const u64 a = (u64)p; return (u32x4){(unsigned)a, (unsigned)(a >> 32) & 0xffffu, bytes, 0x00020000u}; }
struct G1W { u32x4 srdW; unsigned vD[7], sD0, sDm, sWo; };
DI void g1_setup(G1W& g, const u16* Wg, const u16* Wu, int pn, int tid) {
  const int lane = tid & 63, w = __builtin_amdgcn_readfirstlane(tid >> 6);
  g.srdW = srd((w >> 1) ? Wu : Wg, D * F * 2);
#pragma unroll
  for (int i = 0; i < 7; ++i) { const int P = (w & 1) * 448 + 64 * i + lane, r = P / 14, jj = P % 14, k = (r & 32) + 8 * ((r >> 2) & 3) + 4 * ((r >> 4) & 1) + (r & 3);
    g.vD[i] = k * 7168 + ftile(pn, jj >> 1) * 32 + 16 * (jj & 1) - 1024 * (i & 3); }
  g.sD0 = (w >> 1) * 14336 + (w & 1) * 7168; g.sDm = g.sD0; g.sWo = 0;
}

extern "C" __global__ __launch_bounds__(256, 1) void k_main(const u16* __restrict__ Q, const u16* __restrict__ HK, const u16* __restrict__ SD, const u16* Wg0, const u16* Wu0, const u16* Wd0, u16* __restrict__ OUT, char* ws0) {
  extern __shared__ __attribute__((aligned(16))) char smem[];
  LAS char* lds = (LAS char*)smem;
  const int bid = blockIdx.x, xcd = bid & 7, o = bid >> 3;
  const int pm = o >> 2, pn = xcd * 4 + (o & 3);
  const int kh = xcd & 1, pm2 = ((xcd >> 1) & 1) * 8 + (o & 7), pn2 = (xcd >> 2) * 4 + (o >> 3);
  const int wv = __builtin_amdgcn_readfirstlane(threadIdx.x >> 6);
  G1W g;
  {
    char* const ws = ws0; const u16 *const Wg = Wg0, *const Wu = Wu0, *const Wd = Wd0;
    const int tid = threadIdx.x, cg = xcd * 32 + o;
    STAMP(33);
    { const int ch = cg * 4 + wv; const u32x4 rh = srd(HK + (size_t)ch * S, S * 2), ru = srd(ws + OFF_UT + (size_t)ch * S * 2, S * 2); const unsigned L = wv * CV_WAVE, vo = (tid & 63) * 16;
      asm volatile("s_nop 4\n s_add_u32 m0, %3, 32\n s_nop 0\n"
                   "buffer_load_dwordx4 %0, %1, 0 offen lds\n buffer_load_dwordx4 %0, %1, 0 offen offset:1024 lds\n buffer_load_dwordx4 %0, %1, 0 offen offset:2048 lds\n buffer_load_dwordx4 %0, %1, 0 offen offset:3072 lds\n"
                   "s_add_u32 m0, %3, 17152\n s_nop 0\n"
                   "buffer_load_dwordx4 %0, %2, 0 offen lds\n buffer_load_dwordx4 %0, %2, 0 offen offset:1024 lds\n buffer_load_dwordx4 %0, %2, 0 offen offset:2048 lds\n buffer_load_dwordx4 %0, %2, 0 offen offset:3072 lds\n"
                   :: "v"(vo), "s"(rh), "s"(ru), "s"(L) : "memory", "scc"); }
    __builtin_amdgcn_sched_barrier(0);
    const unsigned dsw = ((const unsigned*)SD)[cg * 2 + (wv >> 1)], dsk = (wv & 1) ? dsw & 0xffff0000u : dsw << 16;
    g1_setup(g, Wg, Wu, pn, tid);
    const unsigned ta = (tid & 63) < 63 ? (unsigned)(((bid * 2 + (wv & 1)) * 63 + (tid & 63)) * 128) : OOB;
    u32x16 vcv;
    { const int lane = tid & 63, j = lane & 15, gq = lane >> 4, e = (j + 1) & 3; const unsigned L = wv * CV_WAVE;
      vcv = (u32x16){L + e * CV_CP + ((j + 1 - e) + 24 - 8 * gq) * 2, L + CV_U + 512 + (16 * j + 8 * gq - 16) * 2 - 448, L + CV_U + 512 + (16 * j + 8 * (gq >> 1) + 4 - 4 * (gq & 1)) * 2, L + lane * 16,
                     ta, 0u, 0u, 0u,
                     (unsigned)(((wv * 16 + gq) * 4 + (j >> 2)) * 16384 + cg * 64 + (j & 3) * 16), (unsigned)(bid * 16384 + row_oct(tid, 0) * 128 + (tid & 7) * 8),
                     (unsigned)(CV_STAGE + wv * 4160 + (j >> 1) * 64 + ((j & 1) * 8 + 2 * gq) * 2), (unsigned)(CV_STAGE + (j >> 2) * 4160 + (wv * 16 + gq) * 64 + (j & 3) * 8),
                     (unsigned)(118784 + (tid & 7) * 2048 + row_oct(tid, 0) * 16), 0u, 0u, 0u}; }
    const u32x4 dq = srd(Q + (size_t)bid * 8 * D, 16384), dy = srd(ws + OFF_YB, MB4);
    const int lane = tid & 63, w = wv;
    char* const Hl = ws + OFF_H; char* const Pl = ws + OFF_P;
    const u32x4 d0 = g.srdW, d1 = srd(ws + OFF_N + (size_t)(pm * 4 + w) * 131072, 131072), d2 = srd(Hl + (size_t)(pm * 4 + w) * 458752, 458752),
                d3 = srd(Pl + (bid >> 2) * 65536, 65536), d4 = srd(Pl + MB4 + (bid >> 2) * 65536, 65536), d5 = srd(Wd + (size_t)kh * 1792 * D, D * F),
                d6 = srd(Hl + ((size_t)(pm2 * 4 + w) * 224 + kh * 112) * 1024, 112 * 1024), d7 = srd(Pl + kh * MB4 + ((size_t)(pm2 * 4 + w) * 64 + pn2 * 8) * 1024, 8192),
                d8 = srd(ws + OFF_MB, 98304), d9 = srd(ws + OFF_N + (bid >> 2) * 65536, 65536), d10 = srd(OUT + (size_t)bid * 8 * D, 16384);
    u32x32 sa = {d0.x, d0.y, d0.z, d0.w, d1.x, d1.y, d1.z, d1.w, d2.x, d2.y, d2.z, d2.w, d3.x, d3.y, d3.z, d3.w,
                 d4.x, d4.y, d4.z, d4.w, d5.x, d5.y, d5.z, d5.w, d6.x, d6.y, d6.z, d6.w, d7.x, d7.y, d7.z, d7.w};
    u32x16 sb = {d8.x, d8.y, d8.z, d8.w, d9.x, d9.y, d9.z, d9.w, d10.x, d10.y, d10.z, d10.w, (unsigned)__builtin_amdgcn_readfirstlane(dsk), (unsigned)(w * CV_WAVE), g.sD0, (unsigned)w};
    const unsigned rA = ROTATE * 6 * (pn2 & 3), rB = 24 + ROTATE * 8 * (pn2 & 3);
    const u64 dbg = (u64)(ws + OFF_DBG + 131072 + bid * 1024);
    u32x16 sc = {(unsigned)ftile(pn, 0) * 1024u, (unsigned)ftile(pn, 3) * 1024u, (unsigned)w * 2048u, rA << 11, rA << 16, rB << 11, rB << 16, ((20 + rA) % 24) << 16,
                 (unsigned)((bid >> 5) * 4096 + (bid & 31) * 4), (unsigned)((pm * 32 + pn) * 128), (unsigned)dbg, (unsigned)(dbg >> 32), dy.x, dy.y, dy.z, dy.w};
    const unsigned vX = (lane >> 5) * 1024 + (lane & 31) * 16, vH = ((lane >> 4) & 1) * 512 + (lane >> 5) * 256 + (lane & 15) * 16;
    u32x8 va = {g.vD[0], g.vD[1], g.vD[2], g.vD[3], g.vD[4], g.vD[5], g.vD[6], vX};
    u32x4 vb = {vX + 65536, (unsigned)((lane >> 2) * 224 + 8 * (lane & 3)), vH, vH + 229376};
    u32x2 vc = {row_toff(bid, tid), lane < 14 ? (unsigned)(((((xcd >> 1) * 32 + o) * 4 + w) * 14 + lane) * 128) : OOB};
    const int q = lane >> 4, pc = lane & 15;
    const unsigned wD = (16 * (w & 1) + 4 * (w >> 1) + q) * 2048 + pn2 * 256 + (pc & 1) * 16;
    const unsigned cons = MB_H + (((2 * pm + (pc >> 3)) * 8 + (pc & 7)) * 2 + (pn >> 4)) * 256 + (pn & 15) * 4;
    const u32x8 vg = {wD + ((pc >> 1) ^ q) * 32, wD + 8 * 2048 + ((pc >> 1) ^ (4 + q)) * 32 - 1024, (unsigned)((lane >> 2) * 256 + ((lane >> 2) & 7) * 32 + 8 * (lane & 3)),
                      tid < 16 ? cons : tid < 32 ? (unsigned)(MB_H + ((bid >> 4) * 16 + pc) * 256 + 64 + (bid & 15) * 4) : OOB, tid < 32 ? cons + 128 + (tid & 16) * 4 : OOB,
                      (unsigned)(MB_H + ((pm2 * 8 + pn2) * 2 + kh) * 256 + (lane & 31) * 4), (unsigned)(w * 2048 + lane * 16),
                      lane < 21 ? (unsigned)((576 * 56 + (bid * 2 + (w & 1)) * 21 + lane) * 128) : OOB};
    asm volatile(MG_ASM : "+{s[36:67]}"(sa), "+{s[68:83]}"(sb), "+{s[84:99]}"(sc), "+{v[0:7]}"(va), "+{v[8:11]}"(vb), "+{v[12:13]}"(vc), "+{v[16:31]}"(vcv)
                 : "{a[160:167]}"(vg), "{s[24:27]}"(dq), "{s29}"((unsigned)((o * 4 + w) * 384)) : "memory", "scc", MG_CLOB);
  }
}

__global__ __launch_bounds__(256) void k_pre(const u16* K, const u16* V, const u16* SH, const u16* HK, const u16* Q, char* ws) {
  const int tid = threadIdx.x, bid = blockIdx.x; STAMP(30);
  if (__builtin_amdgcn_readfirstlane(tid) < 64) { unsigned off = ((bid >> 3) * 16 + (tid & 15)) * 128;
    asm volatile("s_getpc_b64 s[20:21]\n s_add_u32 s20, s20, k_main@rel32@lo+4\n s_addc_u32 s21, s21, k_main@rel32@hi+12\n v_min_u32 %0, .Lmg_end-k_main, %0\n s_mov_b32 m0, 0\n s_nop 0\n global_load_lds_dword %0, s[20:21]"
                 : "+v"(off) :: "memory", "scc", "s20", "s21"); }
  phase_pre(K, V, SH, HK, Q, ws, bid, tid);
  STAMP(31);
  STAMP(32);
}

extern "C" void kernel_launch(void* const* d_in, const int* in_sizes, int n_in, void* d_out, int out_size, void* d_ws, size_t ws_size, hipStream_t stream) {
  const u16* q = (const u16*)d_in[0]; const u16* k = (const u16*)d_in[1]; const u16* v = (const u16*)d_in[2];
  const u16* sh = (const u16*)d_in[3]; const u16* sk = (const u16*)d_in[4]; const u16* sD = (const u16*)d_in[5];
  const u16* wg = (const u16*)d_in[7]; const u16* wu = (const u16*)d_in[8]; const u16* wd = (const u16*)d_in[9];
  char* ws = (char*)d_ws;
  (void)hipFuncSetAttribute((const void*)k_main, hipFuncAttributeMaxDynamicSharedMemorySize, LDS_MAIN);
  k_pre<<<256, 256, 256, stream>>>(k, v, sh, sk, q, ws);
  k_main<<<256, 256, LDS_MAIN, stream>>>(q, sk, sD, wg, wu, wd, (u16*)d_out, ws);
}
```
